```python
import torch

def _softmax(x, axis=-1):
    return torch.softmax(x, dim=axis)
def _gelu(x):
    return torch.nn.functional.gelu(x, approximate="tanh")
def _silu(x):
    return torch.nn.functional.silu(x)
def _sigmoid(x):
    return torch.sigmoid(x)
def _layernorm(x, axis=-1, eps=1e-5):
    m = x.mean(dim=axis, keepdim=True)
    v = x.var(dim=axis, keepdim=True, unbiased=False)
    return (x - m) / torch.sqrt(v + eps)
def _scatter_add(tgt, idx, src, axis=0):
    return tgt.clone().index_add_(axis, idx, src)
def _segment_sum(src, seg, n):
    out = torch.zeros((n,) + src.shape[1:], dtype=src.dtype)
    return out.index_add_(0, seg, src)
def _pad(x, pw):
    flat = tuple(v for pair in reversed(pw) for v in pair)
    return torch.nn.functional.pad(x, flat)

kernel_name = 'sequential_preLN__ssm_conv_s4__dense_activation_relu__prompt'

def setup_inputs():

    _w = (lambda a: (lambda t: t.to(torch.bfloat16) if t.is_floating_point() else t)(torch.as_tensor(a)))
    def _A_setup():
        B, L, D, N = 1, 2048, 448, 256
        A_re = -(lambda _x: torch.exp(torch.as_tensor(_x)))((lambda *s: torch.randn(*s, dtype=torch.bfloat16) * 3.2)(D, N))
        A_im = torch.pi * (lambda *s: torch.randn(*s, dtype=torch.bfloat16) * 0.05)(D, N)
        B_re = (lambda *s: torch.randn(*s, dtype=torch.bfloat16) * 0.05)(D, N)
        B_im = (lambda *s: torch.randn(*s, dtype=torch.bfloat16) * 0.05)(D, N)
        C_re = (lambda *s: torch.randn(*s, dtype=torch.bfloat16) * 0.05)(D, N)
        C_im = (lambda *s: torch.randn(*s, dtype=torch.bfloat16) * 0.05)(D, N)
        log_dt = (lambda *s: torch.randn(*s, dtype=torch.bfloat16) * 0.05)(D) * 0.5 - 4.0
        Dskip = (lambda *s: torch.randn(*s, dtype=torch.bfloat16) * 0.05)(D)
        return {
            "u": _w((lambda *s: torch.randn(*s, dtype=torch.bfloat16) * 0.05)(B, L, D)),
            "A_re": _w(A_re),
            "A_im": _w(A_im),
            "B_re": _w(B_re),
            "B_im": _w(B_im),
            "C_re": _w(C_re),
            "C_im": _w(C_im),
            "log_dt": _w(log_dt),
            "Dskip": _w(Dskip),
        }
    _A_in = _A_setup()

    def _B_setup():
        B, S, D, Dff = 1, 2048, 448, 1792
        return {
            "x": _w((lambda *s: torch.randn(*s, dtype=torch.bfloat16) * 0.05)(B, S, D)),
            "w1": _w((lambda *s: torch.randn(*s, dtype=torch.bfloat16) * 0.05)(D, Dff)),
            "w2": _w((lambda *s: torch.randn(*s, dtype=torch.bfloat16) * 0.05)(Dff, D)),
        }
    _B_in = _B_setup()

    return {
        "A_u": _A_in["u"],
        "A_A_re": _A_in["A_re"],
        "A_A_im": _A_in["A_im"],
        "A_B_re": _A_in["B_re"],
        "A_B_im": _A_in["B_im"],
        "A_C_re": _A_in["C_re"],
        "A_C_im": _A_in["C_im"],
        "A_log_dt": _A_in["log_dt"],
        "A_Dskip": _A_in["Dskip"],
        "B_x": _B_in["x"],
        "B_w1": _B_in["w1"],
        "B_w2": _B_in["w2"],
    }

def reference(A_u, A_A_re, A_A_im, A_B_re, A_B_im, A_C_re, A_C_im, A_log_dt, A_Dskip, B_x, B_w1, B_w2):
    def _A_ref(u, A_re, A_im, B_re, B_im, C_re, C_im, log_dt, Dskip):
        Bsz, L, D = u.shape
        dt = torch.exp(log_dt)[:, None]
        A = A_re + 1j * A_im
        Bc = B_re + 1j * B_im
        Cc = C_re + 1j * C_im
        logdA = dt * A
        dB = dt * Bc
        l = (lambda x, dt: x.to(dt))(torch.arange(L), A_re.dtype)
        P = torch.exp(logdA[:, :, None] * l[None, None, :])
        K = torch.einsum("dn,dnl,dn->dl", Cc, P, dB).real
        L2 = 2 * L
        Kp = _pad(K, ((0, 0), (0, L)))
        up = (lambda x, *d: x.permute(*d))(u, 0, 2, 1)
        up = _pad(up, ((0, 0), (0, 0), (0, L)))
        Kf = (lambda x, axes=(-2,-1): torch.fft.rfft2(x.float(), dim=axes))(Kp[:, None, :], axes=(-2, -1))
        Uf = (lambda x, axes=(-2,-1): torch.fft.rfft2(x.float(), dim=axes))(up[:, :, None, :], axes=(-2, -1))
        Yf = Uf * Kf[None, :, :, :]
        y = (lambda x, s, axes=(-2,-1): torch.fft.irfft2(x, s=s, dim=axes).to(torch.bfloat16))(Yf, (1, L2), axes=(-2, -1))[:, :, 0, :L]
        y = (lambda x, *d: x.permute(*d))(y, 0, 2, 1) + u * Dskip[None, None, :]
        return y

    def _B_ref(x, w1, w2):
        h = torch.einsum("bsd,df->bsf", x, w1)
        zero = torch.zeros_like(h)
        h = torch.maximum(h, zero)
        return torch.einsum("bsf,fd->bsd", h, w2)

    x = A_u
    h = x + _A_ref(_layernorm(x, axis=-1), A_A_re, A_A_im, A_B_re, A_B_im, A_C_re, A_C_im, A_log_dt, A_Dskip)
    h2 = h + _B_ref(_layernorm(h, axis=-1), B_w1, B_w2)
    return h2
```

```cpp
#include <hip/hip_runtime.h>
#include <hip/hip_bf16.h>
#include <stdint.h>

static constexpr int L = 2048, D = 448, N = 256, F = 1792;

__device__ __forceinline__ float bf2f(uint16_t v) { return __uint_as_float(((uint32_t)v) << 16); }
__device__ __forceinline__ uint16_t f2bf(float x) { uint32_t u = __float_as_uint(x); u += 0x7fffu + ((u >> 16) & 1u); return (uint16_t)(u >> 16); }
__device__ __forceinline__ float rbf(float x) { return bf2f(f2bf(x)); }

__global__ void k_ln(const uint16_t* __restrict__ x, uint16_t* __restrict__ u) {
  int row = blockIdx.x; int lane = threadIdx.x;
  float v[7]; float s = 0.f;
  for (int i = 0; i < 7; ++i) { v[i] = bf2f(x[row * D + lane + 64 * i]); s += v[i]; }
  for (int o = 32; o; o >>= 1) s += __shfl_xor(s, o);
  float mean = s / D;
  float q = 0.f;
  for (int i = 0; i < 7; ++i) { float d = v[i] - mean; q += d * d; }
  for (int o = 32; o; o >>= 1) q += __shfl_xor(q, o);
  float var = q / D;
  float m = rbf(mean), vv = rbf(var);
  float sd = rbf(sqrtf(rbf(vv + 1e-5f)));
  for (int i = 0; i < 7; ++i) { float d = rbf(v[i] - m); u[row * D + lane + 64 * i] = f2bf(d / sd); }
}

__global__ void k_kgen(const uint16_t* Are, const uint16_t* Aim, const uint16_t* Bre, const uint16_t* Bim,
                       const uint16_t* Cre, const uint16_t* Cim, const uint16_t* logdt, float* K) {
  int d = blockIdx.x;
  float dt = rbf(expf(bf2f(logdt[d])));
  for (int l = threadIdx.x; l < L; l += blockDim.x) {
    float lf = rbf((float)l);
    float acc = 0.f;
    for (int n = 0; n < N; ++n) {
      float zr = dt * bf2f(Are[d * N + n]), zi = dt * bf2f(Aim[d * N + n]);
      float br = dt * bf2f(Bre[d * N + n]), bi = dt * bf2f(Bim[d * N + n]);
      float cr = bf2f(Cre[d * N + n]), ci = bf2f(Cim[d * N + n]);
      float wr = cr * br - ci * bi, wi = cr * bi + ci * br;
      float e = expf(zr * lf); float sn, cs; sincosf(zi * lf, &sn, &cs);
      acc += e * (wr * cs - wi * sn);
    }
    K[d * L + l] = acc;
  }
}

__global__ void k_conv(const uint16_t* x, const uint16_t* u, const float* K, const uint16_t* Dskip, uint16_t* h) {
  int d = blockIdx.x;
  __shared__ float su[L]; __shared__ float sk[L];
  for (int i = threadIdx.x; i < L; i += blockDim.x) { su[i] = bf2f(u[i * D + d]); sk[i] = K[d * L + i]; }
  __syncthreads();
  float ds = bf2f(Dskip[d]);
  for (int t = threadIdx.x; t < L; t += blockDim.x) {
    float acc = 0.f;
    for (int l = 0; l <= t; ++l) acc += sk[l] * su[t - l];
    float y = rbf(acc);
    float sv = rbf(su[t] * ds);
    float r = rbf(y + sv);
    h[t * D + d] = f2bf(bf2f(x[t * D + d]) + r);
  }
}

__global__ void k_gemm1(const uint16_t* a, const uint16_t* w1, uint16_t* h1) {
  int t = blockIdx.x; __shared__ float sa[D];
  for (int i = threadIdx.x; i < D; i += blockDim.x) sa[i] = bf2f(a[t * D + i]);
  __syncthreads();
  for (int f = threadIdx.x; f < F; f += blockDim.x) {
    float acc = 0.f;
    for (int k = 0; k < D; ++k) acc += sa[k] * bf2f(w1[k * F + f]);
    float v = rbf(acc); h1[t * F + f] = f2bf(v > 0.f ? v : 0.f);
  }
}
__global__ void k_gemm2(const uint16_t* h1, const uint16_t* w2, const uint16_t* h, uint16_t* out) {
  int t = blockIdx.x; __shared__ float sa[F];
  for (int i = threadIdx.x; i < F; i += blockDim.x) sa[i] = bf2f(h1[t * F + i]);
  __syncthreads();
  for (int d = threadIdx.x; d < D; d += blockDim.x) {
    float acc = 0.f;
    for (int k = 0; k < F; ++k) acc += sa[k] * bf2f(w2[k * D + d]);
    out[t * D + d] = f2bf(bf2f(h[t * D + d]) + rbf(acc));
  }
}

extern "C" void kernel_launch(void* const* d_in, const int* in_sizes, int n_in,
                              void* d_out, int out_size, void* d_ws, size_t ws_size,
                              hipStream_t stream) {
  const uint16_t* x = (const uint16_t*)d_in[0];
  const uint16_t* Are = (const uint16_t*)d_in[1];
  const uint16_t* Aim = (const uint16_t*)d_in[2];
  const uint16_t* Bre = (const uint16_t*)d_in[3];
  const uint16_t* Bim = (const uint16_t*)d_in[4];
  const uint16_t* Cre = (const uint16_t*)d_in[5];
  const uint16_t* Cim = (const uint16_t*)d_in[6];
  const uint16_t* logdt = (const uint16_t*)d_in[7];
  const uint16_t* Dskip = (const uint16_t*)d_in[8];
  const uint16_t* w1 = (const uint16_t*)d_in[10];
  const uint16_t* w2 = (const uint16_t*)d_in[11];
  char* ws = (char*)d_ws;
  uint16_t* u = (uint16_t*)ws; ws += (size_t)L * D * 2;
  uint16_t* h = (uint16_t*)ws; ws += (size_t)L * D * 2;
  uint16_t* a2 = (uint16_t*)ws; ws += (size_t)L * D * 2;
  uint16_t* h1 = (uint16_t*)ws; ws += (size_t)L * F * 2;
  float* K = (float*)ws; ws += (size_t)D * L * 4;
  k_ln<<<L, 64, 0, stream>>>(x, u);
  k_kgen<<<D, 256, 0, stream>>>(Are, Aim, Bre, Bim, Cre, Cim, logdt, K);
  k_conv<<<D, 256, 0, stream>>>(x, u, K, Dskip, h);
  k_ln<<<L, 64, 0, stream>>>(h, a2);
  k_gemm1<<<L, 256, 0, stream>>>(a2, w1, h1);
  k_gemm2<<<L, 256, 0, stream>>>(h1, w2, h, (uint16_t*)d_out);
}
```

```cpp
#include <hip/hip_runtime.h>
#include <hip/hip_bf16.h>
#include <stdint.h>
#include <type_traits>

static constexpr int L = 2048, D = 448, NM = 256, F = 1792;
static constexpr int LP = 2176;
static constexpr int KP = 2080;
static constexpr size_t OFF_UT = 0;
static constexpr size_t OFF_XT = OFF_UT + (size_t)D * LP * 2;
static constexpr size_t OFF_HT = OFF_XT + (size_t)D * LP * 2;
static constexpr size_t OFF_KR = OFF_HT + (size_t)D * LP * 2;
static constexpr size_t OFF_KINV = OFF_KR + (size_t)D * KP * 2;
static constexpr size_t OFF_W1F = OFF_KINV + 2048;
static constexpr size_t OFF_W1S = OFF_W1F + (size_t)F * D * 2;
static constexpr size_t OFF_W2F = OFF_W1S + 8192;
static constexpr size_t OFF_HN = OFF_W2F + (size_t)F * D * 2;
static constexpr size_t OFF_H1F = OFF_HN + (size_t)L * D * 2;

typedef unsigned short u16;
typedef unsigned int u32;
typedef _Float16 f16;
typedef f16 f16x2 __attribute__((ext_vector_type(2)));
typedef f16 f16x8 __attribute__((ext_vector_type(8)));
typedef __bf16 bf16x2 __attribute__((ext_vector_type(2)));
typedef __bf16 bf16x8 __attribute__((ext_vector_type(8)));
typedef float f32x2 __attribute__((ext_vector_type(2)));
typedef float f32x4 __attribute__((ext_vector_type(4)));
typedef u32 u32x2 __attribute__((ext_vector_type(2)));
typedef u32 u32x4 __attribute__((ext_vector_type(4)));
typedef short s16x4 __attribute__((ext_vector_type(4)));
#define LDS3 __attribute__((address_space(3)))

#define DI __device__ __forceinline__
DI float bf2f(u16 v) { return __uint_as_float(((u32)v) << 16); }
DI float bflo(u32 v) { return __uint_as_float(v << 16); }
DI float bfhi(u32 v) { return __uint_as_float(v & 0xffff0000u); }
DI u32 pk_bf16(float a, float b) { f32x2 v = {a, b}; return __builtin_bit_cast(u32, __builtin_convertvector(v, bf16x2)); }
DI u32 pk_f16(float a, float b) { f32x2 v = {a, b}; return __builtin_bit_cast(u32, __builtin_convertvector(v, f16x2)); }
DI float rbf(float x) { return bflo(pk_bf16(x, 0.f)); }
DI f16x2 as_f16x2(u32 v) { return __builtin_bit_cast(f16x2, v); }
DI bf16x2 as_bf16x2(u32 v) { return __builtin_bit_cast(bf16x2, v); }
DI float wave_sum(float v) {
#pragma unroll
  for (int o = 32; o; o >>= 1) v += __shfl_xor(v, o);
  return v;
}
DI float wave_max(float v) {
#pragma unroll
  for (int o = 32; o; o >>= 1) v = fmaxf(v, __shfl_xor(v, o));
  return v;
}
template <int B, int E, class Fn> DI void static_for(Fn&& f) {
  if constexpr (B < E) { f(std::integral_constant<int, B>{}); static_for<B + 1, E>(f); }
}
DI s16x4 tr_read(const void* p) { return __builtin_amdgcn_ds_read_tr16_b64_v4i16((LDS3 s16x4*)p); }

#define TR(ws, kid, slot) do {} while (0)
struct K1Args {
  const u16 *x, *Are, *Aim, *Bre, *Bim, *Cre, *Cim, *logdt, *w1, *w2;
  char* ws;
};

static constexpr int K1_LN = 128, K1_KG = D, K1_W1 = 56, K1_W2 = 56;
static constexpr int K1_SMEM = 36 * 1024;

DI void k1_ln(const K1Args& a, int blk, char* smem) {
  const int tid = threadIdx.x, lane = tid & 63, w = tid >> 6;
  const int t0 = blk * 16;
  u16* su = (u16*)smem;
  u16* sx = su + 16 * 448;
  u32x4 v[4];
#pragma unroll
  for (int i = 0; i < 4; ++i) {
    v[i] = u32x4{0, 0, 0, 0};
    if (lane < 56) v[i] = *(const u32x4*)(a.x + (size_t)(t0 + 4 * w + i) * D + lane * 8);
  }
#pragma unroll
  for (int i = 0; i < 4; ++i) {
    float f[8];
#pragma unroll
    for (int k = 0; k < 4; ++k) { f[2 * k] = bflo(v[i][k]); f[2 * k + 1] = bfhi(v[i][k]); }
    float s = 0.f;
#pragma unroll
    for (int k = 0; k < 8; ++k) s += f[k];
    s = wave_sum(s);
    const float mean = s * (1.0f / D);
    float q = 0.f;
#pragma unroll
    for (int k = 0; k < 8; ++k) { float dd = f[k] - mean; q += dd * dd; }
    if (lane >= 56) q = 0.f;
    q = wave_sum(q);
    const float var = q * (1.0f / D);
    const float m = rbf(mean);
    const float sd = rbf(sqrtf(rbf(rbf(var) + 1e-5f)));
    const float inv = 1.0f / sd;
    u32x4 o;
#pragma unroll
    for (int k = 0; k < 4; ++k) {
      u32 dpk = pk_bf16(f[2 * k] - m, f[2 * k + 1] - m);
      u32 upk = pk_bf16(bflo(dpk) * inv, bfhi(dpk) * inv);
      o[k] = pk_f16(bflo(upk), bfhi(upk));
    }
    if (lane < 56) {
      *(u32x4*)(su + (4 * w + i) * 448 + lane * 8) = o;
      *(u32x4*)(sx + (4 * w + i) * 448 + lane * 8) = v[i];
    }
  }
  __syncthreads();
  if (tid < 224) {
#pragma unroll
    for (int arr = 0; arr < 2; ++arr) {
      const u32* src = (const u32*)(arr ? sx : su) + tid;
      u32 r[16];
#pragma unroll
      for (int i = 0; i < 16; ++i) r[i] = src[i * 224];
      u32x4 lo0, lo1, hi0, hi1;
#pragma unroll
      for (int i = 0; i < 4; ++i) {
        lo0[i] = __builtin_amdgcn_perm(r[2 * i + 1], r[2 * i], 0x05040100);
        hi0[i] = __builtin_amdgcn_perm(r[2 * i + 1], r[2 * i], 0x07060302);
        lo1[i] = __builtin_amdgcn_perm(r[8 + 2 * i + 1], r[8 + 2 * i], 0x05040100);
        hi1[i] = __builtin_amdgcn_perm(r[8 + 2 * i + 1], r[8 + 2 * i], 0x07060302);
      }
      u16* dst = (u16*)(a.ws + (arr ? OFF_XT : OFF_UT)) + (size_t)(2 * tid) * LP + t0;
      *(u32x4*)(dst) = lo0; *(u32x4*)(dst + 8) = lo1;
      *(u32x4*)(dst + LP) = hi0; *(u32x4*)(dst + LP + 8) = hi1;
    }
  }
}

DI void k1_kgen(const K1Args& a, int d, char* smem) {
  const int tid = threadIdx.x, lane = tid & 63, w = tid >> 6;
  f32x4* prm = (f32x4*)smem;
  float* red = (float*)(smem + 256 * 48);
  float* part = (float*)(smem + 256 * 48 + 256);
  float* Fv = (float*)smem;
  const float LOG2E = 1.4426950408889634f, INV2PI = 0.15915494309189535f;
  const int n = tid;
  const float dt = rbf(expf(bf2f(a.logdt[d])));
  const float are = bf2f(a.Are[d * NM + n]), aim = bf2f(a.Aim[d * NM + n]);
  const float bre = dt * bf2f(a.Bre[d * NM + n]), bim = dt * bf2f(a.Bim[d * NM + n]);
  const float cre = bf2f(a.Cre[d * NM + n]), cim = bf2f(a.Cim[d * NM + n]);
  const float zr = (dt * are) * LOG2E, zi = (dt * aim) * INV2PI;
  const float cr = cre * bre - cim * bim, ci = cre * bim + cim * bre;
  const float mx = fmaxf(fabsf(cr), fabsf(ci)), mn = fminf(fabsf(cr), fabsf(ci));
  const bool nz = mx > 0.f;
  const float rr = nz ? mn / mx : 0.f;
  const float lc = nz ? (log2f(mx) + 0.5f * log2f(1.0f + rr * rr)) : -1000.f;
  const float ph = nz ? atan2f(ci, cr) * INV2PI : 0.f;
  const float sY = fmaxf(0.f, zr * 504.f);
  float gm = lc + fmaxf(0.f, zr * 1536.f) + sY;
  gm = fmaxf(gm, lc + zr * 2048.f);
  gm = wave_max(gm);
  if (lane == 0) red[w] = gm;
  TR(a.ws, 0, 1);
  __syncthreads();
  const float G = ceilf(fmaxf(fmaxf(red[0], red[1]), fmaxf(red[2], red[3]))) - 14.f;
  const float lcs = nz ? (lc + sY - G) : -INFINITY;
  float e1r, e1i, e2r, e2i, e3r, e3i;
  {
    float m1 = __builtin_amdgcn_exp2f(zr * 128.f), a1 = __builtin_amdgcn_fractf(zi * 128.f);
    float m2 = __builtin_amdgcn_exp2f(zr * 256.f), a2 = __builtin_amdgcn_fractf(zi * 256.f);
    float m3 = __builtin_amdgcn_exp2f(zr * 384.f), a3 = __builtin_amdgcn_fractf(zi * 384.f);
    e1r = m1 * __builtin_amdgcn_cosf(a1); e1i = m1 * __builtin_amdgcn_sinf(a1);
    e2r = m2 * __builtin_amdgcn_cosf(a2); e2i = m2 * __builtin_amdgcn_sinf(a2);
    e3r = m3 * __builtin_amdgcn_cosf(a3); e3i = m3 * __builtin_amdgcn_sinf(a3);
  }
  prm[n * 3 + 0] = f32x4{zr, zi, lcs, ph};
  prm[n * 3 + 1] = f32x4{-sY, e1r, e1i, e2r};
  prm[n * 3 + 2] = f32x4{e2i, e3r, e3i, 0.f};
  float t2048 = nz ? __builtin_amdgcn_exp2f(zr * 2048.f + (lc - G)) * __builtin_amdgcn_cosf(__builtin_amdgcn_fractf(zi * 2048.f + ph)) : 0.f;
  t2048 = wave_sum(t2048);
  if (lane == 0) red[4 + w] = t2048;
  __syncthreads();

  TR(a.ws, 0, 2);
  const int r = lane & 15, g = lane >> 4;
  const float av = (r < 8) ? (float)r : (r < 12) ? (float)(256 + 2 * (r - 8)) : (r < 14) ? (float)(512 + 4 * (r - 12)) : (float)(1024 + 512 * (r - 14));
  const float bv = (float)(8 * r);
  f32x4 acc[4];
#pragma unroll
  for (int m = 0; m < 4; ++m) acc[m] = f32x4{0.f, 0.f, 0.f, 0.f};
#pragma unroll
  for (int s = 0; s < 4; ++s) {
    u32x4 xf, y0, y1, y2, y3;
#pragma unroll
    for (int i = 0; i < 4; ++i) {
      const int nn = 64 * w + 16 * s + 4 * g + i;
      const f32x4 p0 = prm[nn * 3 + 0], p1 = prm[nn * 3 + 1], p2 = prm[nn * 3 + 2];
      float ex = __builtin_amdgcn_exp2f(__builtin_fmaf(p0[0], av, p0[2]));
      float ax = __builtin_amdgcn_fractf(__builtin_fmaf(p0[1], av, p0[3]));
      xf[i] = pk_f16(ex * __builtin_amdgcn_cosf(ax), -ex * __builtin_amdgcn_sinf(ax));
      float ey = __builtin_amdgcn_exp2f(__builtin_fmaf(p0[0], bv, p1[0]));
      float ay = __builtin_amdgcn_fractf(p0[1] * bv);
      float yr = ey * __builtin_amdgcn_cosf(ay), yi = ey * __builtin_amdgcn_sinf(ay);
      y0[i] = pk_f16(yr, yi);
      y1[i] = pk_f16(yr * p1[1] - yi * p1[2], yr * p1[2] + yi * p1[1]);
      y2[i] = pk_f16(yr * p1[3] - yi * p2[0], yr * p2[0] + yi * p1[3]);
      y3[i] = pk_f16(yr * p2[1] - yi * p2[2], yr * p2[2] + yi * p2[1]);
    }
    f16x8 xa = __builtin_bit_cast(f16x8, xf);
    __builtin_amdgcn_sched_barrier(0);
    acc[0] = __builtin_amdgcn_mfma_f32_16x16x32_f16(xa, __builtin_bit_cast(f16x8, y0), acc[0], 0, 0, 0);
    acc[1] = __builtin_amdgcn_mfma_f32_16x16x32_f16(xa, __builtin_bit_cast(f16x8, y1), acc[1], 0, 0, 0);
    acc[2] = __builtin_amdgcn_mfma_f32_16x16x32_f16(xa, __builtin_bit_cast(f16x8, y2), acc[2], 0, 0, 0);
    acc[3] = __builtin_amdgcn_mfma_f32_16x16x32_f16(xa, __builtin_bit_cast(f16x8, y3), acc[3], 0, 0, 0);
    __builtin_amdgcn_sched_barrier(0);
  }
  TR(a.ws, 0, 3);
#pragma unroll
  for (int m = 0; m < 4; ++m)
#pragma unroll
    for (int k = 0; k < 4; ++k) part[w * 1024 + (4 * g + k) * 64 + 16 * m + r] = acc[m][k];
  __syncthreads();
  f32x4 sv;
  {
    const f32x4* pp = (const f32x4*)part;
    sv = pp[tid] + pp[256 + tid] + pp[512 + tid] + pp[768 + tid];
  }
  const int row = tid >> 4, b0 = (tid & 15) * 4;
  const bool valid = (row >= 12) || (b0 < 32);
  float mxv = valid ? fmaxf(fmaxf(fabsf(sv[0]), fabsf(sv[1])), fmaxf(fabsf(sv[2]), fabsf(sv[3]))) : 0.f;
  const float f2048 = red[4] + red[5] + red[6] + red[7];
  mxv = fmaxf(mxv, fabsf(f2048));
  mxv = wave_max(mxv);
  *(f32x4*)(Fv + tid * 4) = sv;
  if (tid == 0) Fv[1024] = f2048;
  if (lane == 0) red[8 + w] = mxv;
  __syncthreads();
  const float mall = fmaxf(fmaxf(red[8], red[9]), fmaxf(red[10], red[11]));
  int e = 0;
  if (mall > 0.f) e = (int)((__float_as_uint(mall) >> 23) & 0xff) - 127;
  e = max(-100, min(100, e));
  const float scale = __uint_as_float((u32)(127 + 14 - e) << 23);
  if (tid == 0) {
    ((float*)(a.ws + OFF_KINV))[d] = ldexpf(1.0f, (int)G + e - 14);
  }
  TR(a.ws, 0, 4);
  float vals[8];
#pragma unroll
  for (int k = 0; k < 8; ++k) {
    const int l = 2047 - (8 * tid + k);
    const int t = (int)rbf((float)l);
    int slot;
    if (t < 256) slot = (t & 7) * 64 + (t >> 3);
    else if (t < 512) slot = (8 + ((t >> 1) & 3)) * 64 + ((t - 256) >> 3);
    else if (t < 1024) slot = (12 + ((t >> 2) & 1)) * 64 + ((t - 512) >> 3);
    else if (t < 2048) slot = (14 + ((t - 1024) >> 9)) * 64 + (((t - 1024) & 511) >> 3);
    else slot = 1024;
    vals[k] = Fv[slot] * scale;
  }
  u16* kr = (u16*)(a.ws + OFF_KR) + (size_t)d * KP;
  u32x4 o = {pk_f16(vals[0], vals[1]), pk_f16(vals[2], vals[3]), pk_f16(vals[4], vals[5]), pk_f16(vals[6], vals[7])};
  *(u32x4*)(kr + 8 * tid) = o;
  if (tid < 4) *(u32x4*)(kr + 2048 + 8 * tid) = u32x4{0, 0, 0, 0};
}

DI void k1_w1(const K1Args& a, int j, char* smem) {
  const int tid = threadIdx.x, lane = tid & 63, w = tid >> 6;
  constexpr int RS = 80;
  float* red = (float*)(smem + 448 * RS);
  u32x4 v[7];
#pragma unroll
  for (int i = 0; i < 7; ++i) v[i] = *(const u32x4*)(a.w1 + (size_t)((tid >> 2) + 64 * i) * F + 32 * j + (tid & 3) * 8);
#pragma unroll
  for (int i = 0; i < 7; ++i) *(u32x4*)(smem + ((tid >> 2) + 64 * i) * RS + (tid & 3) * 16) = v[i];
  __syncthreads();
  const int g = lane >> 4, q = (lane & 15) >> 2, p = lane & 3;
  float cs[2] = {0.f, 0.f};
  const bf16x2 ones = __builtin_bit_cast(bf16x2, 0x3f803f80u);
  u32x4* dst = (u32x4*)(a.ws + OFF_W1F);
  for (int ks = w; ks < 14; ks += 4) {
#pragma unroll
    for (int ph = 0; ph < 2; ++ph) {
      s16x4 lo = tr_read(smem + (32 * ks + 8 * g + q) * RS + (16 * ph + 4 * p) * 2);
      s16x4 hi = tr_read(smem + (32 * ks + 8 * g + 4 + q) * RS + (16 * ph + 4 * p) * 2);
      u32x2 l2 = __builtin_bit_cast(u32x2, lo), h2 = __builtin_bit_cast(u32x2, hi);
      u32x4 fr = {l2[0], l2[1], h2[0], h2[1]};
#pragma unroll
      for (int k = 0; k < 4; ++k) cs[ph] = __builtin_amdgcn_fdot2_f32_bf16(as_bf16x2(fr[k]), ones, cs[ph], false);
      dst[((size_t)(2 * j + ph) * 14 + ks) * 64 + lane] = fr;
    }
  }
#pragma unroll
  for (int ph = 0; ph < 2; ++ph) {
    cs[ph] += __shfl_xor(cs[ph], 16); cs[ph] += __shfl_xor(cs[ph], 32);
    if (lane < 16) red[w * 32 + 16 * ph + lane] = cs[ph];
  }
  __syncthreads();
  if (tid < 32) ((float*)(a.ws + OFF_W1S))[32 * j + tid] = red[tid] + red[32 + tid] + red[64 + tid] + red[96 + tid];
}

DI void k1_w2(const K1Args& a, int kap, char* smem) {
  const int tid = threadIdx.x, lane = tid & 63, w = tid >> 6;
  constexpr int RS = 912;
  const u32x4* src = (const u32x4*)(a.w2 + (size_t)kap * 32 * D);
  u32x4 v[7];
#pragma unroll
  for (int i = 0; i < 7; ++i) v[i] = src[tid + 256 * i];
#pragma unroll
  for (int i = 0; i < 7; ++i) { int c = tid + 256 * i; int row = c / 56, ch = c % 56; *(u32x4*)(smem + row * RS + ch * 16) = v[i]; }
  __syncthreads();
  const int g = lane >> 4, q = (lane & 15) >> 2, p = lane & 3;
  u32x4* dst = (u32x4*)(a.ws + OFF_W2F);
  for (int dl = w; dl < 28; dl += 4) {
    s16x4 lo = tr_read(smem + (4 * g + q) * RS + (16 * dl + 4 * p) * 2);
    s16x4 hi = tr_read(smem + (16 + 4 * g + q) * RS + (16 * dl + 4 * p) * 2);
    u32x2 l2 = __builtin_bit_cast(u32x2, lo), h2 = __builtin_bit_cast(u32x2, hi);
    dst[((size_t)dl * 56 + kap) * 64 + lane] = u32x4{l2[0], l2[1], h2[0], h2[1]};
  }
}

__global__ __launch_bounds__(256) void k1_pre(K1Args a) {
  __shared__ __attribute__((aligned(16))) char smem[K1_SMEM];
  int b = blockIdx.x;
  TR(a.ws, 0, 0);
  if (b < K1_LN) { k1_ln(a, b, smem); TR(a.ws, 0, 7); return; }
  b -= K1_LN;
  if (b < K1_KG) { k1_kgen(a, b, smem); TR(a.ws, 0, 7); return; }
  b -= K1_KG;
  if (b < K1_W1) { k1_w1(a, b, smem); TR(a.ws, 0, 7); return; }
  b -= K1_W1;
  k1_w2(a, b, smem); TR(a.ws, 0, 7);
}

static constexpr int CPS = 4160;
static constexpr int K2_SMEM = 8 * CPS + (256 + 2048) * 2;

__global__ __launch_bounds__(256) void k2_conv(char* ws, const u16* Dskip) {
  __shared__ __attribute__((aligned(16))) char smem[K2_SMEM];
  const int tid = threadIdx.x, lane = tid & 63, w = tid >> 6;
  const int d = blockIdx.x;
  TR(ws, 1, 0);
  const u16* uT = (const u16*)(ws + OFF_UT) + (size_t)d * LP;
  const u16* xT = (const u16*)(ws + OFF_XT) + (size_t)d * LP;
  const u32x4* kr = (const u32x4*)((const u16*)(ws + OFF_KR) + (size_t)d * KP);
  u16* su = (u16*)(smem + 8 * CPS);
  const int c = lane & 15, g = lane >> 4;
  u32x4 k0 = kr[tid], k1 = kr[tid + 1];
  u32x4 kx0 = u32x4{0, 0, 0, 0}, kx1 = kx0;
  if (tid == 0) { kx0 = kr[256]; kx1 = kr[257]; }
  u32x4 uv = *(const u32x4*)(uT + tid * 8);
  u32x2 xv[2];
#pragma unroll
  for (int i = 0; i < 2; ++i) xv[i] = *(const u32x2*)(xT + 256 * (2 * w + i) + 16 * c + 4 * g);
  const float kinv = ((const float*)(ws + OFF_KINV))[d];
  const float dsk = bf2f(Dskip[d]);
  auto put = [&](int ch, u32x4 A, u32x4 B) {
    u32 e[8] = {A[0], A[1], A[2], A[3], B[0], B[1], B[2], B[3]};
    u32 o[7];
#pragma unroll
    for (int i = 0; i < 7; ++i) o[i] = __builtin_amdgcn_alignbyte(e[i + 1], e[i], 2);
#pragma unroll
    for (int s = 0; s < 4; ++s) {
      *(u32x4*)(smem + (2 * s) * CPS + ch * 16) = u32x4{e[s], e[s + 1], e[s + 2], e[s + 3]};
      *(u32x4*)(smem + (2 * s + 1) * CPS + ch * 16) = u32x4{o[s], o[s + 1], o[s + 2], o[s + 3]};
    }
  };
  put(tid, k0, k1);
  if (tid == 0) put(256, kx0, kx1);
  *(u32x4*)(su + 256 + tid * 8) = uv;
  if (tid < 32) *(u32x4*)(su + tid * 8) = u32x4{0, 0, 0, 0};
  TR(ws, 1, 1);
  __syncthreads();
  TR(ws, 1, 2);

  f32x4 acc[8];
#pragma unroll
  for (int q = 0; q < 8; ++q) acc[q] = f32x4{0.f, 0.f, 0.f, 0.f};
  const int sig = (7 - c) & 7;
  const char* abase = smem + sig * CPS + 16 * ((c < 8 ? 253 : 252) + g);
#pragma unroll
  for (int rr = 0; rr < 2; ++rr) {
    const int r = 2 * w + rr;
    f16x8 B[8];
#pragma unroll
    for (int q = 0; q < 8; ++q) B[q] = *(const f16x8*)(su + 240 + 256 * q - 32 * r + 16 * c + 8 * g);
#pragma unroll
    for (int m = 0; m < 8; ++m) {
      f16x8 A = *(const f16x8*)(abase - 64 * (r + 8 * m));
#pragma unroll
      for (int q = m; q < 8; ++q) acc[q] = __builtin_amdgcn_mfma_f32_16x16x32_f16(A, B[q - m], acc[q], 0, 0, 0);
    }
  }
  TR(ws, 1, 3);
  __syncthreads();
  f32x4* rb = (f32x4*)smem;
#pragma unroll
  for (int q = 0; q < 8; ++q)
    if ((q >> 1) != w) rb[(w * 8 + q) * 64 + lane] = acc[q];
  __syncthreads();
  u16* hT = (u16*)(ws + OFF_HT) + (size_t)d * LP;
#pragma unroll
  for (int i = 0; i < 2; ++i) {
    f32x4 y;
    f32x4 own = acc[0];
#pragma unroll
    for (int q = 0; q < 8; ++q) if (q == 2 * w + i) own = acc[q];
    y = own;
    const int q = 2 * w + i;
#pragma unroll
    for (int s = 1; s < 4; ++s) y += rb[(((w + s) & 3) * 8 + q) * 64 + lane];
    const int t = 256 * q + 16 * c + 4 * g;
    u32x2 uu = *(const u32x2*)(su + 256 + t);
    f16x2 ua = as_f16x2(uu[0]), ub = as_f16x2(uu[1]);
    float uf[4] = {(float)ua[0], (float)ua[1], (float)ub[0], (float)ub[1]};
    u32 y01 = pk_bf16(y[0] * kinv, y[1] * kinv), y23 = pk_bf16(y[2] * kinv, y[3] * kinv);
    u32 s01 = pk_bf16(uf[0] * dsk, uf[1] * dsk), s23 = pk_bf16(uf[2] * dsk, uf[3] * dsk);
    u32 r01 = pk_bf16(bflo(y01) + bflo(s01), bfhi(y01) + bfhi(s01));
    u32 r23 = pk_bf16(bflo(y23) + bflo(s23), bfhi(y23) + bfhi(s23));
    u32 h01 = pk_bf16(bflo(xv[i][0]) + bflo(r01), bfhi(xv[i][0]) + bfhi(r01));
    u32 h23 = pk_bf16(bflo(xv[i][1]) + bflo(r23), bfhi(xv[i][1]) + bfhi(r23));
    *(u32x2*)(hT + t) = u32x2{h01, h23};
  }
  TR(ws, 1, 7);
}

static constexpr int K3_SMEM = 448 * 128 + 1024;
DI u32 swz(int row) { return (u32)(((row & 3) << 2) | ((row >> 2) & 3)); }

template <int N> DI void wait_vm() { asm volatile("s_waitcnt vmcnt(%0)" :: "n"(N) : "memory"); }
DI void glds16(u32 lds_dst, u32 voff, const void* sbase) {
  u32 keep;
  asm volatile("s_mov_b32 %0, m0\n\ts_mov_b32 m0, %1\n\ts_nop 0\n\tglobal_load_lds_dwordx4 %2, %3\n\ts_mov_b32 m0, %0"
               : "=&s"(keep) : "s"(lds_dst), "v"(voff), "s"(sbase) : "memory");
}
template <int OFF> DI void gload16(u32x4& dst, u32 voff, const void* sbase) {
  asm volatile("global_load_dwordx4 %0, %1, %2 offset:%3" : "=v"(dst) : "v"(voff), "s"(sbase), "n"(OFF) : "memory");
}
template <int OFF> DI void trread(u32x2& dst, u32 addr) {
  asm volatile("ds_read_b64_tr_b16 %0, %1 offset:%2" : "=v"(dst) : "v"(addr), "n"(OFF) : "memory");
}

template <int N_> struct K3Step {
  static DI void issue(u32x2 (&B)[8][2], u32 (&ad)[2][8]) {
    constexpr int ks = N_ >> 3, c = N_ & 7, slot = N_ & 7;
    constexpr int off = (ks & 7) * 8192;
    trread<off>(B[slot][0], ad[0][c]);
    trread<off>(B[slot][1], ad[1][c]);
  }
};

template <bool FIRST> DI void mfma_bf16(f32x4& acc, const u32x4& a, const u32x4& b) {
  if constexpr (FIRST) asm volatile("v_mfma_f32_16x16x32_bf16 %0, %1, %2, 0" : "=a"(acc) : "v"(a), "v"(b));
  else asm volatile("v_mfma_f32_16x16x32_bf16 %0, %1, %2, %0" : "+a"(acc) : "v"(a), "v"(b));
}
template <bool FIRST> DI void mfma_bf16_nop(f32x4& acc, const u32x4& a, const u32x4& b) {
  if constexpr (FIRST) asm volatile("s_nop 1\n\tv_mfma_f32_16x16x32_bf16 %0, %1, %2, 0" : "=a"(acc) : "v"(a), "v"(b));
  else asm volatile("s_nop 1\n\tv_mfma_f32_16x16x32_bf16 %0, %1, %2, %0" : "+a"(acc) : "v"(a), "v"(b));
}
template <int OFF> DI void ldsread128(u32x4& dst, u32 addr) {
  asm volatile("ds_read_b128 %0, %1 offset:%2" : "=v"(dst) : "v"(addr), "n"(OFF) : "memory");
}
static constexpr int G1_R = 8;
static constexpr int G1_STG = 16384;
static constexpr int K3_SMEM2 = G1_R * G1_STG + 1024;

__global__ __launch_bounds__(512, 2) void k3_gemm1(char* ws) {
  extern __shared__ __attribute__((aligned(16))) char smem[];
  const int tid = threadIdx.x, lane = tid & 63;
  const int wv = __builtin_amdgcn_readfirstlane(tid >> 6);
  const int w = wv & 3;
  const int xcd = blockIdx.x & 7, bi = blockIdx.x >> 3;
  const int tb = 2 * xcd + bi / 14, fb = bi % 14;
  TR(ws, 2, 0);
  const int c16 = lane & 15, g = lane >> 4, q = (lane & 15) >> 2, p = lane & 3;
  const u32 lds0 = (u32)(size_t)(LDS3 char*)smem;
  f32x2* st = (f32x2*)(smem + G1_R * G1_STG);

  if (wv >= 4) {
    u32 vh[2];
#pragma unroll
    for (int k = 0; k < 2; ++k) {
      const int row = 8 * w + 4 * k + g;
      vh[k] = (u32)(row * LP * 2) + (u32)((c16 ^ swz(row)) * 16);
    }
    const char* hb = ws + OFF_HT + tb * 256;
    const char* ab = ws + OFF_W1F + (size_t)(fb * 8 + 2 * w) * 14336;
    const u32 va0 = lane * 16, va1 = lane * 16 + 14336;
    auto ld = [&](auto kc) {
      constexpr int ks = decltype(kc)::value;
      constexpr int sl = (ks % G1_R) * G1_STG;
      glds16(lds0 + sl + (8 * w) * 256, vh[0], hb + (size_t)ks * 32 * LP * 2);
      glds16(lds0 + sl + (8 * w + 4) * 256, vh[1], hb + (size_t)ks * 32 * LP * 2);
      glds16(lds0 + sl + 8192 + (2 * w) * 1024, va0, ab + ks * 1024);
      glds16(lds0 + sl + 8192 + (2 * w + 1) * 1024, va1, ab + ks * 1024);
    };
    static_for<0, G1_R>(ld);
    auto stepl = [&](auto kc) {
      constexpr int ks = decltype(kc)::value;
      constexpr int issued = (G1_R + (ks > 3 ? ks - 3 : 0)) < 14 ? (G1_R + (ks > 3 ? ks - 3 : 0)) : 14;
      wait_vm<4 * (issued - ks - 1)>();
      __builtin_amdgcn_s_barrier();
      if constexpr (ks >= 3 && ks - 3 + G1_R < 14) ld(std::integral_constant<int, ks - 3 + G1_R>{});
    };
    static_for<0, 14>(stepl);
    __builtin_amdgcn_s_barrier();
    __builtin_amdgcn_s_barrier();
    return;
  }
  const int fi = w >> 1, ti = w & 1;
  f32x4 wsv[4];
  {
    const float* w1s = (const float*)(ws + OFF_W1S) + fb * 128 + 64 * fi + 4 * g;
#pragma unroll
    for (int i = 0; i < 4; ++i) wsv[i] = *(const f32x4*)(w1s + 16 * i);
  }
  f32x4 acc[4][4];
  float s1[2] = {0.f, 0.f}, s2[2] = {0.f, 0.f};
  const bf16x2 ones = __builtin_bit_cast(bf16x2, 0x3f803f80u);
  u32 ad[2][4];
#pragma unroll
  for (int t = 0; t < 2; ++t) {
    const int row = 8 * g + 4 * t + q;
    const u32 a0 = lds0 + (u32)(row * 256) + (((u32)(p >> 1) ^ swz(row)) << 4) + 8 * (p & 1);
#pragma unroll
    for (int c = 0; c < 4; ++c) ad[t][c] = a0 ^ (u32)(32 * (4 * ti + ((2 * fi + c) & 3)));
  }
  const u32 aa = lds0 + 8192 + (4 * fi) * 1024 + lane * 16;
  u32x2 B[8][2];
  u32x4 A[2][4];
  constexpr int LA = 4;
  u32 adh[2][4];
#pragma unroll
  for (int t = 0; t < 2; ++t)
#pragma unroll
    for (int c = 0; c < 4; ++c) adh[t][c] = ad[t][c] + 65536u;
  const u32 aah = aa + 65536u;
  auto pre = [&](auto nc) {
    constexpr int n = decltype(nc)::value;
    if constexpr ((n & 3) == 0) {
      constexpr int ks = n >> 2;
      constexpr int sl = (ks % G1_R);
      constexpr int off = (sl & 3) * G1_STG;
      __builtin_amdgcn_s_barrier();
      const u32 b = sl < 4 ? aa : aah;
      ldsread128<off>(A[ks & 1][0], b); ldsread128<off + 1024>(A[ks & 1][1], b);
      ldsread128<off + 2048>(A[ks & 1][2], b); ldsread128<off + 3072>(A[ks & 1][3], b);
    }
  };
  auto rd0 = [&](auto nc) { constexpr int n = decltype(nc)::value; constexpr int sl = (n >> 2) % G1_R;
    trread<(sl & 3) * G1_STG>(B[n & 7][0], sl < 4 ? ad[0][n & 3] : adh[0][n & 3]); };
  auto rd1 = [&](auto nc) { constexpr int n = decltype(nc)::value; constexpr int sl = (n >> 2) % G1_R;
    trread<(sl & 3) * G1_STG>(B[n & 7][1], sl < 4 ? ad[1][n & 3] : adh[1][n & 3]); };
  auto issue = [&](auto nc) { pre(nc); rd0(nc); rd1(nc); };
  auto step = [&](auto nc) {
    constexpr int n = decltype(nc)::value;
    constexpr int ks = n >> 2, c = n & 3, sl = n & 7;
    constexpr int last = (55 < n + LA - 1) ? 55 : n + LA - 1;
    constexpr int nA = (last / 4) - (n / 4);
    constexpr int outst = (last - n) * 2 + nA * 4;
    static_assert(outst <= 15, "lgkm");
    asm volatile("s_waitcnt lgkmcnt(%6)" : "+v"(B[sl][0]), "+v"(B[sl][1]), "+v"(A[ks & 1][0]), "+v"(A[ks & 1][1]), "+v"(A[ks & 1][2]), "+v"(A[ks & 1][3]) : "n"(outst) : "memory");
    u32x4 bfr = {B[sl][0][0], B[sl][0][1], B[sl][1][0], B[sl][1][1]};
    mfma_bf16_nop<ks == 0>(acc[0][c], A[ks & 1][0], bfr);
    __builtin_amdgcn_sched_barrier(0);
    if constexpr (n + LA < 56) { pre(std::integral_constant<int, n + LA>{}); rd0(std::integral_constant<int, n + LA>{}); }
    __builtin_amdgcn_sched_barrier(0);
    mfma_bf16<ks == 0>(acc[1][c], A[ks & 1][1], bfr);
    __builtin_amdgcn_sched_barrier(0);
    if constexpr (n + LA < 56) rd1(std::integral_constant<int, n + LA>{});
    __builtin_amdgcn_sched_barrier(0);
    mfma_bf16<ks == 0>(acc[2][c], A[ks & 1][2], bfr);
    if constexpr (c < 2) {
#pragma unroll
      for (int k = 0; k < 2; ++k) {
        bf16x2 x2 = as_bf16x2(bfr[k]);
        s1[c] = __builtin_amdgcn_fdot2_f32_bf16(x2, ones, s1[c], false);
        s2[c] = __builtin_amdgcn_fdot2_f32_bf16(x2, x2, s2[c], false);
      }
    }
    __builtin_amdgcn_sched_barrier(0);
    mfma_bf16<ks == 0>(acc[3][c], A[ks & 1][3], bfr);
    if constexpr (c < 2) {
#pragma unroll
      for (int k = 2; k < 4; ++k) {
        bf16x2 x2 = as_bf16x2(bfr[k]);
        s1[c] = __builtin_amdgcn_fdot2_f32_bf16(x2, ones, s1[c], false);
        s2[c] = __builtin_amdgcn_fdot2_f32_bf16(x2, x2, s2[c], false);
      }
      if (ks == fb) {
        const int tt = 4 * ti + ((2 * fi + c) & 3);
        *(u32x4*)((u16*)(ws + OFF_HN) + (size_t)(tb * 128 + 16 * tt + c16) * D + 32 * fb + 8 * g) = bfr;
      }
    }
    __builtin_amdgcn_sched_barrier(0);
  };
  static_for<0, LA>(issue);
  static_for<0, 56>(step);
  asm volatile("s_nop 7\n\ts_nop 7" ::: "memory");
  TR(ws, 2, 3);
  __builtin_amdgcn_s_barrier();
#pragma unroll
  for (int i = 0; i < 2; ++i) {
    float a = s1[i], b = s2[i];
    a += __shfl_xor(a, 16); a += __shfl_xor(a, 32);
    b += __shfl_xor(b, 16); b += __shfl_xor(b, 32);
    const float mean = a * (1.0f / D);
    const float var = fmaxf(b * (1.0f / D) - mean * mean, 0.f);
    const float m = rbf(mean);
    const float sd = rbf(sqrtf(rbf(rbf(var) + 1e-5f)));
    const int tt = 4 * ti + ((2 * fi + i) & 3);
    if (lane < 16) st[tt * 16 + lane] = f32x2{m, 1.0f / sd};
  }
  asm volatile("s_waitcnt lgkmcnt(0)" ::: "memory");
  __builtin_amdgcn_s_barrier();
  u32x4* h1f = (u32x4*)(ws + OFF_H1F);
#pragma unroll
  for (int c = 0; c < 4; ++c) {
    const int tt = 4 * ti + ((2 * fi + c) & 3);
    const f32x2 ms = st[tt * 16 + c16];
#pragma unroll
    for (int pp = 0; pp < 2; ++pp) {
      float v0[4], v1[4];
#pragma unroll
      for (int k = 0; k < 4; ++k) {
        v0[k] = fmaxf((acc[2 * pp][c][k] - ms[0] * wsv[2 * pp][k]) * ms[1], 0.f);
        v1[k] = fmaxf((acc[2 * pp + 1][c][k] - ms[0] * wsv[2 * pp + 1][k]) * ms[1], 0.f);
      }
      u32x4 o = {pk_bf16(v0[0], v0[1]), pk_bf16(v0[2], v0[3]), pk_bf16(v1[0], v1[1]), pk_bf16(v1[2], v1[3])};
      h1f[((size_t)(tb * 8 + tt) * 56 + fb * 4 + 2 * fi + pp) * 64 + lane] = o;
    }
  }
  TR(ws, 2, 7);
}

static constexpr int G2_R = 4;
static constexpr int G2_STG = 32768;
static constexpr int K4_RS = 272;
static constexpr int K4_SMEM2 = G2_R * G2_STG;
__global__ __launch_bounds__(512, 2) void k4_gemm2(const char* ws, u16* out) {
  extern __shared__ __attribute__((aligned(16))) char smem[];
  const int tid = threadIdx.x, lane = tid & 63;
  const int wv = __builtin_amdgcn_readfirstlane(tid >> 6);
  const int w = wv & 3;
  const int xcd = blockIdx.x & 7, bi = blockIdx.x >> 3;
  const int tb = 4 * xcd + bi / 7, db = bi % 7;
  TR((char*)ws, 3, 0);
  const u32 lds0 = (u32)(size_t)(LDS3 char*)smem;
  u32x4 res[1];
  if (wv < 4) { }
  if (wv >= 4) {
    const char* hbase = ws + OFF_H1F + ((size_t)(tb * 4) * 56 + 14 * w) * 1024;
    const char* wbase = ws + OFF_W2F + ((size_t)(db * 4) * 56 + 14 * w) * 1024;
    u32 vo[4];
#pragma unroll
    for (int i = 0; i < 4; ++i) vo[i] = lane * 16 + i * 56 * 1024;
    auto ld = [&](auto kc) {
      constexpr int k = decltype(kc)::value;
      constexpr int sl = (k % G2_R) * G2_STG;
#pragma unroll
      for (int i = 0; i < 4; ++i) {
        glds16(lds0 + sl + w * 8192 + i * 1024, vo[i], wbase + k * 1024);
        glds16(lds0 + sl + w * 8192 + 4096 + i * 1024, vo[i], hbase + k * 1024);
      }
    };
    static_for<0, G2_R>(ld);
    auto stepl = [&](auto kc) {
      constexpr int k = decltype(kc)::value;
      constexpr int issued = (G2_R + (k > 1 ? k - 1 : 0)) < 14 ? (G2_R + (k > 1 ? k - 1 : 0)) : 14;
      wait_vm<8 * (issued - k - 1)>();
      __builtin_amdgcn_s_barrier();
      if constexpr (k >= 1 && k - 1 + G2_R < 14) ld(std::integral_constant<int, k - 1 + G2_R>{});
    };
    static_for<0, 14>(stepl);
    __builtin_amdgcn_s_barrier();
    __builtin_amdgcn_s_barrier();
  }
  f32x4 acc[4][4];
  const int c16 = lane & 15, g = lane >> 4;
  u32x4 res2;
  if (wv < 4) {
    const u16* hN = (const u16*)(ws + OFF_HN);
    res[0] = *(const u32x4*)(hN + (size_t)(tb * 64 + (tid >> 3)) * D + db * 64 + (tid & 7) * 8);
    res2 = *(const u32x4*)(hN + (size_t)(tb * 64 + 32 + (tid >> 3)) * D + db * 64 + (tid & 7) * 8);
    const u32 base = lds0 + w * 8192 + lane * 16;
    auto step = [&](auto kc) {
      constexpr int k = decltype(kc)::value;
      constexpr u32 sl = (u32)((k % G2_R) * G2_STG);
      __builtin_amdgcn_s_barrier();
      u32x4 wf[4], hf[4];
      const u32 b = base + sl;
      ldsread128<0>(wf[0], b); ldsread128<4096>(hf[0], b);
      ldsread128<1024>(wf[1], b); ldsread128<5120>(hf[1], b);
      ldsread128<2048>(wf[2], b); ldsread128<6144>(hf[2], b);
      ldsread128<3072>(wf[3], b); ldsread128<7168>(hf[3], b);
      asm volatile("s_waitcnt lgkmcnt(0)" : "+v"(wf[0]), "+v"(wf[1]), "+v"(wf[2]), "+v"(wf[3]), "+v"(hf[0]), "+v"(hf[1]), "+v"(hf[2]), "+v"(hf[3]) :: "memory");
#pragma unroll
      for (int dl = 0; dl < 4; ++dl)
#pragma unroll
        for (int ta = 0; ta < 4; ++ta)
          mfma_bf16<k == 0>(acc[dl][ta], wf[dl], hf[ta]);
      __builtin_amdgcn_sched_barrier(0);
    };
    static_for<0, 14>(step);
    asm volatile("s_nop 7\n\ts_nop 7" ::: "memory");
    TR((char*)ws, 3, 3);
    __builtin_amdgcn_s_barrier();
#pragma unroll
    for (int dl = 0; dl < 4; ++dl)
#pragma unroll
      for (int ta = 0; ta < 4; ++ta)
        *(f32x4*)(smem + (w * 64 + 16 * ta + c16) * K4_RS + (16 * dl + 4 * g) * 4) = acc[dl][ta];
    asm volatile("s_waitcnt lgkmcnt(0)" ::: "memory");
    __builtin_amdgcn_s_barrier();
  }
  if (wv < 4) {
#pragma unroll
    for (int i = 0; i < 2; ++i) {
      const int t = (tid >> 3) + 32 * i, chn = tid & 7;
      const u32x4 rr = i ? res2 : res[0];
      f32x4 s0 = {0.f, 0.f, 0.f, 0.f}, s1 = s0;
#pragma unroll
      for (int ww = 0; ww < 4; ++ww) {
        s0 += *(const f32x4*)(smem + (ww * 64 + t) * K4_RS + chn * 32);
        s1 += *(const f32x4*)(smem + (ww * 64 + t) * K4_RS + chn * 32 + 16);
      }
      u32 p0 = pk_bf16(s0[0], s0[1]), p1 = pk_bf16(s0[2], s0[3]), p2 = pk_bf16(s1[0], s1[1]), p3 = pk_bf16(s1[2], s1[3]);
      u32x4 o;
      o[0] = pk_bf16(bflo(rr[0]) + bflo(p0), bfhi(rr[0]) + bfhi(p0));
      o[1] = pk_bf16(bflo(rr[1]) + bflo(p1), bfhi(rr[1]) + bfhi(p1));
      o[2] = pk_bf16(bflo(rr[2]) + bflo(p2), bfhi(rr[2]) + bfhi(p2));
      o[3] = pk_bf16(bflo(rr[3]) + bflo(p3), bfhi(rr[3]) + bfhi(p3));
      *(u32x4*)(out + (size_t)(tb * 64 + t) * D + db * 64 + chn * 8) = o;
    }
  }
  TR((char*)ws, 3, 7);
}

extern "C" void kernel_launch(void* const* d_in, const int* in_sizes, int n_in,
                              void* d_out, int out_size, void* d_ws, size_t ws_size,
                              hipStream_t stream) {
  K1Args a;
  a.x = (const u16*)d_in[0]; a.Are = (const u16*)d_in[1]; a.Aim = (const u16*)d_in[2];
  a.Bre = (const u16*)d_in[3]; a.Bim = (const u16*)d_in[4]; a.Cre = (const u16*)d_in[5];
  a.Cim = (const u16*)d_in[6]; a.logdt = (const u16*)d_in[7];
  a.w1 = (const u16*)d_in[10]; a.w2 = (const u16*)d_in[11]; a.ws = (char*)d_ws;
  const u16* Dskip = (const u16*)d_in[8];
  char* ws = (char*)d_ws;
  hipFuncSetAttribute((const void*)k3_gemm1, hipFuncAttributeMaxDynamicSharedMemorySize, K3_SMEM2);
  hipFuncSetAttribute((const void*)k4_gemm2, hipFuncAttributeMaxDynamicSharedMemorySize, K4_SMEM2);
  k1_pre<<<K1_LN + K1_KG + K1_W1 + K1_W2, 256, 0, stream>>>(a);
  k2_conv<<<D, 256, 0, stream>>>(ws, Dskip);
  k3_gemm1<<<224, 512, K3_SMEM2, stream>>>(ws);
  k4_gemm2<<<224, 512, K4_SMEM2, stream>>>(ws, (u16*)d_out);
}
```

```cpp
#include <hip/hip_runtime.h>
#include <hip/hip_bf16.h>
#include <stdint.h>
#include <type_traits>

static constexpr int L = 2048, D = 448, NM = 256, F = 1792;
static constexpr int LP = 2176;
static constexpr int KP = 2080;
static constexpr size_t OFF_UT = 0;
static constexpr size_t OFF_XT = OFF_UT + (size_t)D * LP * 2;
static constexpr size_t OFF_HT = OFF_XT + (size_t)D * LP * 2;
static constexpr size_t OFF_KR = OFF_HT + (size_t)D * LP * 2;
static constexpr size_t OFF_KINV = OFF_KR + (size_t)D * KP * 2;
static constexpr size_t OFF_W1F = OFF_KINV + 2048;
static constexpr size_t OFF_W1S = OFF_W1F + (size_t)F * D * 2;
static constexpr size_t OFF_W2F = OFF_W1S + 8192;
static constexpr size_t OFF_HN = OFF_W2F + (size_t)F * D * 2;
static constexpr size_t OFF_H1F = OFF_HN + (size_t)L * D * 2;
static constexpr size_t OFF_SYNC = OFF_H1F + (size_t)L * F * 2;
static constexpr size_t OFF_ST = OFF_UT;
static constexpr size_t SY_EPA = 0, SY_EPB = 128, SY_LN = 1024  , SY_SEEN = 8192, SY_H1 = 16384;
static constexpr size_t WS_USED = OFF_SYNC + 32768;

typedef unsigned short u16;
typedef unsigned int u32;
typedef _Float16 f16;
typedef f16 f16x2 __attribute__((ext_vector_type(2)));
typedef f16 f16x8 __attribute__((ext_vector_type(8)));
typedef __bf16 bf16x2 __attribute__((ext_vector_type(2)));
typedef __bf16 bf16x8 __attribute__((ext_vector_type(8)));
typedef float f32x2 __attribute__((ext_vector_type(2)));
typedef float f32x4 __attribute__((ext_vector_type(4)));
typedef u32 u32x2 __attribute__((ext_vector_type(2)));
typedef u32 u32x4 __attribute__((ext_vector_type(4)));
typedef short s16x4 __attribute__((ext_vector_type(4)));
#define LDS3 __attribute__((address_space(3)))

#define DI __device__ __forceinline__
DI float bf2f(u16 v) { return __uint_as_float(((u32)v) << 16); }
DI float bflo(u32 v) { return __uint_as_float(v << 16); }
DI float bfhi(u32 v) { return __uint_as_float(v & 0xffff0000u); }
DI u32 pk_bf16(float a, float b) { f32x2 v = {a, b}; return __builtin_bit_cast(u32, __builtin_convertvector(v, bf16x2)); }
DI u32 pk_f16(float a, float b) { f32x2 v = {a, b}; return __builtin_bit_cast(u32, __builtin_convertvector(v, f16x2)); }
DI float rbf(float x) { return bflo(pk_bf16(x, 0.f)); }
DI f16x2 as_f16x2(u32 v) { return __builtin_bit_cast(f16x2, v); }
DI bf16x2 as_bf16x2(u32 v) { return __builtin_bit_cast(bf16x2, v); }
template <int CTRL, int RM> DI float dppf(float old, float v) {
  return __int_as_float(__builtin_amdgcn_update_dpp(__float_as_int(old), __float_as_int(v), CTRL, RM, 0xf, false));
}
DI float wave_sum(float v) {
  v += dppf<0x111, 0xf>(0.f, v); v += dppf<0x112, 0xf>(0.f, v); v += dppf<0x114, 0xf>(0.f, v); v += dppf<0x118, 0xf>(0.f, v);
  v += dppf<0x142, 0xa>(0.f, v); v += dppf<0x143, 0xc>(0.f, v);
  return __int_as_float(__builtin_amdgcn_readlane(__float_as_int(v), 63));
}
DI float wave_max(float v) {
  v = fmaxf(v, dppf<0x111, 0xf>(v, v)); v = fmaxf(v, dppf<0x112, 0xf>(v, v)); v = fmaxf(v, dppf<0x114, 0xf>(v, v)); v = fmaxf(v, dppf<0x118, 0xf>(v, v));
  v = fmaxf(v, dppf<0x142, 0xa>(v, v)); v = fmaxf(v, dppf<0x143, 0xc>(v, v));
  return __int_as_float(__builtin_amdgcn_readlane(__float_as_int(v), 63));
}
typedef unsigned long long u64;
typedef __attribute__((address_space(1))) u64 gu64;
DI u64 ld_agent(const void* p) { return __hip_atomic_load((const gu64*)p, __ATOMIC_RELAXED, __HIP_MEMORY_SCOPE_AGENT); }
DI void st_agent(void* p, u64 v) { __hip_atomic_store((gu64*)p, v, __ATOMIC_RELAXED, __HIP_MEMORY_SCOPE_AGENT); }
static constexpr u64 TAG_INC = 0x9E3779B97F4A7C15ull;
typedef __amdgpu_buffer_rsrc_t rsrc_t;
DI rsrc_t ws_rsrc(char* ws) { return __builtin_amdgcn_make_buffer_rsrc(ws, 0, (int)WS_USED, 0x00020000); }
template <int B, int E, class Fn> DI void static_for(Fn&& f) {
  if constexpr (B < E) { f(std::integral_constant<int, B>{}); static_for<B + 1, E>(f); }
}
DI s16x4 tr_read(const void* p) { return __builtin_amdgcn_ds_read_tr16_b64_v4i16((LDS3 s16x4*)p); }

#define TR(ws, kid, slot) do {} while (0)
struct KAArgs {
  const u16 *x, *Are, *Aim, *Bre, *Bim, *Cre, *Cim, *logdt, *Dskip, *w1, *w2;
  char* ws;
};

static constexpr int KA_LN = 64;
static constexpr int CPS = 4160;
static constexpr int KA_LNRS = 912;
static constexpr int KA_SMEM = 60 * 1024;

DI u64 ka_ln(const KAArgs& a, int blk, char* smem) {
  const int tid = threadIdx.x, lane = tid & 63;
  const int w = __builtin_amdgcn_readfirstlane(tid >> 6);
  const int t0 = blk * 32;
  __builtin_amdgcn_s_setprio(2);
  u32x4 v[8];
#pragma unroll
  for (int i = 0; i < 8; ++i) {
    v[i] = u32x4{0, 0, 0, 0};
    if (lane < 56) v[i] = *(const u32x4*)(a.x + (size_t)(t0 + 8 * w + i) * D + lane * 8);
  }
#pragma unroll
  for (int i = 0; i < 8; ++i)
    if (lane < 56) *(u32x4*)(smem + (8 * w + i) * KA_LNRS + lane * 16) = v[i];
  TR(a.ws, 0, 2);
  __syncthreads();
  const rsrc_t rs = ws_rsrc(a.ws);
  const int g = lane >> 4, i16 = lane & 15, q = i16 >> 2, p = lane & 3;
  {
    s16x4 lo[7], hi[7];
#pragma unroll
    for (int k = 0; k < 7; ++k) {
      const char* src = smem + (8 * g + q) * KA_LNRS + (16 * (w + 4 * k) + 4 * p) * 2;
      lo[k] = tr_read(src); hi[k] = tr_read(src + 4 * KA_LNRS);
    }
    __builtin_amdgcn_sched_barrier(0);
#pragma unroll
    for (int k = 0; k < 7; ++k) {
      u32x2 l2 = __builtin_bit_cast(u32x2, lo[k]), h2 = __builtin_bit_cast(u32x2, hi[k]);
      const u32 off = (u32)(OFF_XT + (size_t)(16 * (w + 4 * k) + i16) * LP * 2 + (t0 + 8 * g) * 2);
      __builtin_amdgcn_raw_buffer_store_b128(u32x4{l2[0], l2[1], h2[0], h2[1]}, rs, off, 0, 16);
    }
  }
  if (w < 2) {
    f32x4 a1 = {0.f, 0.f, 0.f, 0.f}, a2 = a1;
    const u32x4 one4 = {0x3f803f80u, 0x3f803f80u, 0x3f803f80u, 0x3f803f80u};
    bf16x8 fr[14];
#pragma unroll
    for (int ks = 0; ks < 14; ++ks) fr[ks] = *(const bf16x8*)(smem + (16 * w + i16) * KA_LNRS + (32 * ks + 8 * g) * 2);
    __builtin_amdgcn_sched_barrier(0);
#pragma unroll
    for (int ks = 0; ks < 14; ++ks) {
      a1 = __builtin_amdgcn_mfma_f32_16x16x32_bf16(fr[ks], __builtin_bit_cast(bf16x8, one4), a1, 0, 0, 0);
      a2 = __builtin_amdgcn_mfma_f32_16x16x32_bf16(fr[ks], fr[ks], a2, 0, 0, 0);
    }
    const float s = p == 0 ? a1[0] : p == 1 ? a1[1] : p == 2 ? a1[2] : a1[3];
    const float q2 = p == 0 ? a2[0] : p == 1 ? a2[1] : p == 2 ? a2[2] : a2[3];
    const float mean = s * (1.0f / D);
    const float var = fmaxf(q2 * (1.0f / D) - mean * mean, 0.f);
    const float sd = rbf(__builtin_amdgcn_sqrtf(rbf(rbf(var) + 1e-5f)));
    if (g == q) __builtin_amdgcn_raw_buffer_store_b32(pk_bf16(mean, sd), rs, (u32)(OFF_ST + (t0 + 16 * w + i16) * 4), 0, 16);
  }
  const u64 ep = ld_agent(a.ws + OFF_SYNC + SY_EPA);
  TR(a.ws, 0, 3);
  asm volatile("s_waitcnt vmcnt(0)" ::: "memory");
  const u64 tag = ep + TAG_INC;
  __syncthreads();
  if (tid < 8) st_agent(a.ws + OFF_SYNC + SY_LN + tid * 512 + blk * 8, tag);
  __builtin_amdgcn_s_setprio(0);
  return tag;
}

DI void ka_w1(const KAArgs& a, int j, char* smem) {
  const int tid = threadIdx.x, lane = tid & 63, w = tid >> 6;
  constexpr int RS = 80;
  float* red = (float*)(smem + 448 * RS);
  u32x4 v[7];
#pragma unroll
  for (int i = 0; i < 7; ++i) v[i] = *(const u32x4*)(a.w1 + (size_t)((tid >> 2) + 64 * i) * F + 32 * j + (tid & 3) * 8);
#pragma unroll
  for (int i = 0; i < 7; ++i) *(u32x4*)(smem + ((tid >> 2) + 64 * i) * RS + (tid & 3) * 16) = v[i];
  __syncthreads();
  const int g = lane >> 4, q = (lane & 15) >> 2, p = lane & 3;
  float cs[2] = {0.f, 0.f};
  const bf16x2 ones = __builtin_bit_cast(bf16x2, 0x3f803f80u);
  u32x4* dst = (u32x4*)(a.ws + OFF_W1F);
  for (int ks = w; ks < 14; ks += 4) {
#pragma unroll
    for (int ph = 0; ph < 2; ++ph) {
      s16x4 lo = tr_read(smem + (32 * ks + 8 * g + q) * RS + (16 * ph + 4 * p) * 2);
      s16x4 hi = tr_read(smem + (32 * ks + 8 * g + 4 + q) * RS + (16 * ph + 4 * p) * 2);
      u32x2 l2 = __builtin_bit_cast(u32x2, lo), h2 = __builtin_bit_cast(u32x2, hi);
      u32x4 fr = {l2[0], l2[1], h2[0], h2[1]};
#pragma unroll
      for (int k = 0; k < 4; ++k) cs[ph] = __builtin_amdgcn_fdot2_f32_bf16(as_bf16x2(fr[k]), ones, cs[ph], false);
      dst[((size_t)(2 * j + ph) * 14 + ks) * 64 + lane] = fr;
    }
  }
#pragma unroll
  for (int ph = 0; ph < 2; ++ph) {
    cs[ph] += __shfl_xor(cs[ph], 16); cs[ph] += __shfl_xor(cs[ph], 32);
    if (lane < 16) red[w * 32 + 16 * ph + lane] = cs[ph];
  }
  __syncthreads();
  if (tid < 32) ((float*)(a.ws + OFF_W1S))[32 * j + tid] = red[tid] + red[32 + tid] + red[64 + tid] + red[96 + tid];
  __syncthreads();
}

DI void ka_w2(const KAArgs& a, int kap, char* smem) {
  const int tid = threadIdx.x, lane = tid & 63, w = tid >> 6;
  constexpr int RS = 912;
  const u32x4* src = (const u32x4*)(a.w2 + (size_t)kap * 32 * D);
  u32x4 v[7];
#pragma unroll
  for (int i = 0; i < 7; ++i) v[i] = src[tid + 256 * i];
#pragma unroll
  for (int i = 0; i < 7; ++i) { int c = tid + 256 * i; int row = c / 56, ch = c % 56; *(u32x4*)(smem + row * RS + ch * 16) = v[i]; }
  __syncthreads();
  const int g = lane >> 4, q = (lane & 15) >> 2, p = lane & 3;
  u32x4* dst = (u32x4*)(a.ws + OFF_W2F);
  for (int dl = w; dl < 28; dl += 4) {
    s16x4 lo = tr_read(smem + (4 * g + q) * RS + (16 * dl + 4 * p) * 2);
    s16x4 hi = tr_read(smem + (16 + 4 * g + q) * RS + (16 * dl + 4 * p) * 2);
    u32x2 l2 = __builtin_bit_cast(u32x2, lo), h2 = __builtin_bit_cast(u32x2, hi);
    dst[((size_t)dl * 56 + kap) * 64 + lane] = u32x4{l2[0], l2[1], h2[0], h2[1]};
  }
  __syncthreads();
}

static constexpr int CH_SU = 8 * CPS;
static constexpr int CH_RED = CH_SU + 4608;
static constexpr int CH_TAB = 0;
static constexpr int CH_PART = 0;
static constexpr int CH_RED0 = 59392;
static constexpr int CH_F = CH_RED + 256;
static constexpr int CH_SX = 47104;
static constexpr int CH_ST = CH_SX + 4096;

constexpr int rne_bf16_int(int l) {
  if (l < 256) return l;
  int sh = 0; while ((l >> sh) >= 256) ++sh;
  const int q = l >> sh, rem = l & ((1 << sh) - 1), half = 1 << (sh - 1);
  const int up = (rem > half) || (rem == half && (q & 1));
  return (q + up) << sh;
}
constexpr int kslot(int m) {
  const int l = 2047 - m;
  if (l < 0) return 1025;
  const int t = rne_bf16_int(l);
  if (t >= 2048) return 1024;
  const int seg = (t >= 256) + (t >= 512) + (t >= 1024);
  const int row = seg == 0 ? (t & 7) : seg == 1 ? 8 + ((t >> 1) & 3) : seg == 2 ? 12 + ((t >> 2) & 1) : 14 + ((t >> 9) & 1);
  const int b = (t >> 3) & (seg >= 2 ? 63 : 31);
  return b * 16 + row;
}
struct SlotTab { u16 v[2064]; };
constexpr SlotTab make_slot_tab() { SlotTab t{}; for (int m = 0; m < 2064; ++m) t.v[m] = (u16)(kslot(m) * 4); return t; }
__device__ __attribute__((aligned(16))) const SlotTab g_slot_tab = make_slot_tab();
DI void glds16_sc1(u32 lds_dst, u32 voff, const void* sbase) {
  u32 keep;
  asm volatile("s_mov_b32 %0, m0\n\ts_mov_b32 m0, %1\n\ts_nop 0\n\tglobal_load_lds_dwordx4 %2, %3 sc1\n\ts_mov_b32 m0, %0"
               : "=&s"(keep) : "s"(lds_dst), "v"(voff), "s"(sbase) : "memory");
}
DI f32x2 pkfma_b(f32x2 a, float b, f32x2 c) {
  f32x2 r, bb = {b, b};
  asm("v_pk_fma_f32 %0, %1, %2, %3 op_sel_hi:[1,0,1]" : "=v"(r) : "v"(a), "v"(bb), "v"(c));
  return r;
}
DI f32x2 cmul(f32x2 y, f32x2 e) {
  f32x2 t, r;
  asm("v_pk_mul_f32 %0, %1, %2 op_sel_hi:[1,0]" : "=v"(t) : "v"(y), "v"(e));
  asm("v_pk_fma_f32 %0, %1, %2, %3 op_sel:[1,1,0] op_sel_hi:[0,1,1] neg_lo:[0,1,0]" : "=v"(r) : "v"(y), "v"(e), "v"(t));
  return r;
}
DI f32x2 cmul_conj(f32x2 p, f32x2 q) {
  f32x2 t, r;
  asm("v_pk_mul_f32 %0, %1, %2 op_sel_hi:[1,0] neg_hi:[1,0]" : "=v"(t) : "v"(p), "v"(q));
  asm("v_pk_fma_f32 %0, %1, %2, %3 op_sel:[1,1,0] op_sel_hi:[0,1,1] neg_lo:[1,0,0] neg_hi:[1,0,0]" : "=v"(r) : "v"(p), "v"(q), "v"(t));
  return r;
}
DI float atan2_rev(float y, float x) {
  const float ax = fabsf(x), ay = fabsf(y);
  const float mx = fmaxf(ax, ay), mn = fminf(ax, ay);
  const float t = mn * __builtin_amdgcn_rcpf(mx);
  const float s = t * t;
  float p = 0.0028662257f;
  p = __builtin_fmaf(p, s, -0.0161657367f); p = __builtin_fmaf(p, s, 0.0429096138f); p = __builtin_fmaf(p, s, -0.0752896400f);
  p = __builtin_fmaf(p, s, 0.1065626393f); p = __builtin_fmaf(p, s, -0.1420889944f); p = __builtin_fmaf(p, s, 0.1999355085f);
  p = __builtin_fmaf(p, s, -0.3333314528f);
  float r = __builtin_fmaf(p * s, t, t) * 0.15915494309189535f;
  if (ay > ax) r = 0.25f - r;
  if (x < 0.f) r = 0.5f - r;
  return y < 0.f ? -r : r;
}

DI void ka_channel(const KAArgs& a, int d, char* smem) {
  const int tid = threadIdx.x, lane = tid & 63;
  const int w = __builtin_amdgcn_readfirstlane(tid >> 6);
  const float LOG2E = 1.4426950408889634f, INV2PI = 0.15915494309189535f;
  const u32 lds0 = (u32)(size_t)(LDS3 char*)smem;
  const int n = tid;
  const u16 r_are = a.Are[d * NM + n], r_aim = a.Aim[d * NM + n], r_bre = a.Bre[d * NM + n], r_bim = a.Bim[d * NM + n];
  const u16 r_cre = a.Cre[d * NM + n], r_cim = a.Cim[d * NM + n], r_ldt = a.logdt[d], r_dsk = a.Dskip[d];
  u32 slot2[8];
  {
    const u32x4 s0 = *(const u32x4*)(g_slot_tab.v + 8 * tid), s1 = *(const u32x4*)(g_slot_tab.v + 8 * tid + 8);
#pragma unroll
    for (int k = 0; k < 4; ++k) { slot2[k] = s0[k]; slot2[4 + k] = s1[k]; }
  }
  u64 ep = 0;
  if (w == 0) ep = ld_agent(a.ws + OFF_SYNC + SY_EPA);
  float* red = (float*)(smem + CH_RED0);
  const float dt = rbf(expf(bf2f(r_ldt)));
  const float bre = dt * bf2f(r_bre), bim = dt * bf2f(r_bim);
  const float cre = bf2f(r_cre), cim = bf2f(r_cim);
  const float zr = (dt * bf2f(r_are)) * LOG2E, zi = (dt * bf2f(r_aim)) * INV2PI;
  const float cr = cre * bre - cim * bim, ci = cre * bim + cim * bre;
  const float mx = fmaxf(fabsf(cr), fabsf(ci));
  const float lc = mx > 1e-36f ? __builtin_amdgcn_logf(mx) + 0.5f : -1000.f;
  const float sY = fmaxf(0.f, zr * 504.f);
  const float gm = fmaxf(lc + fmaxf(0.f, zr * 1536.f) + sY, lc + zr * 2048.f);
  const float G = ceilf(wave_max(gm));
  auto cexp = [&](float t, float lg) -> f32x2 {
    const float m = __builtin_amdgcn_exp2f(__builtin_fmaf(zr, t, lg)), f = __builtin_amdgcn_fractf(zi * t);
    return f32x2{m * __builtin_amdgcn_cosf(f), m * __builtin_amdgcn_sinf(f)};
  };
  {
    f32x2* tb = (f32x2*)(smem + CH_TAB + n * 224);
    const f32x2 one = {1.f, 0.f};
    const f32x2 e1 = cexp(1.f, 0.f), e2 = cmul(e1, e1), e3 = cmul(e2, e1), e4 = cexp(4.f, 0.f), e6 = cmul(e3, e3);
    const f32x2 e8 = cexp(8.f, 0.f), e16 = cmul(e8, e8), e24 = cmul(e16, e8);
    const f32x2 e32 = cexp(32.f, 0.f), e64 = cmul(e32, e32), e96 = cmul(e64, e32);
    const f32x2 e128 = cexp(128.f, 0.f), e256 = cexp(256.f, 0.f), e384 = cmul(e256, e128);
    const f32x2 e512 = cexp(512.f, 0.f), e1024 = cexp(1024.f, 0.f);
    const float sy2 = __builtin_amdgcn_exp2f(-sY);
    const float cs = __builtin_amdgcn_exp2f(sY - G + 14.f);
    const f32x2 c0 = {cr * cs, ci * cs};
    *(f32x4*)(tb + 0) = f32x4{1.f, 0.f, e8[0], e8[1]};      *(f32x4*)(tb + 2) = f32x4{e16[0], e16[1], e24[0], e24[1]};
    *(f32x4*)(tb + 4) = f32x4{sy2, 0.f, e32[0] * sy2, e32[1] * sy2}; *(f32x4*)(tb + 6) = f32x4{e64[0] * sy2, e64[1] * sy2, e96[0] * sy2, e96[1] * sy2};
    const f32x2 p1 = cmul(c0, e4), p2 = cmul(c0, e256), p3 = cmul(c0, e512);
    *(f32x4*)(tb + 8) = f32x4{c0[0], c0[1], p1[0], p1[1]};  *(f32x4*)(tb + 10) = f32x4{p2[0], p2[1], p3[0], p3[1]};
    *(f32x4*)(tb + 12) = f32x4{1.f, 0.f, e1[0], e1[1]};     *(f32x4*)(tb + 14) = f32x4{e2[0], e2[1], e3[0], e3[1]};
    *(f32x4*)(tb + 16) = f32x4{1.f, 0.f, e2[0], e2[1]};     *(f32x4*)(tb + 18) = f32x4{e4[0], e4[1], e6[0], e6[1]};
    *(f32x4*)(tb + 20) = f32x4{1.f, 0.f, e4[0], e4[1]};     *(f32x4*)(tb + 22) = f32x4{e512[0], e512[1], e1024[0], e1024[1]};
    *(f32x4*)(tb + 24) = f32x4{e128[0], e128[1], e256[0], e256[1]}; *(f32x4*)(tb + 26) = f32x4{e384[0], e384[1], 0.f, 0.f};
    (void)one;
    const f32x2 e2048 = cexp(2048.f, 14.f - G);
    float t2048 = cr * e2048[0] - ci * e2048[1];
    t2048 = wave_sum(t2048);
    if (lane == 0) { red[w] = G; red[4 + w] = t2048; }
  }
  __builtin_amdgcn_fence(__ATOMIC_RELEASE, "wavefront");
  __builtin_amdgcn_wave_barrier();
  TR(a.ws, 0, 2);
  const char* flg = a.ws + OFF_SYNC + SY_LN + (d & 7) * 512 + lane * 8;
  u64 fl = 0;
  const u64 tag = ep + TAG_INC;
  if (w == 0) { fl = ld_agent(flg); if (lane == 0) st_agent(a.ws + OFF_SYNC + SY_SEEN + d * 8, tag); }
  bool have = (w != 0);
  const int c = lane & 15, g = lane >> 4;
  auto load_x = [&]() {
#pragma unroll
    for (int i = 0; i < 4; ++i) glds16_sc1(lds0 + CH_SX + i * 1024, lane * 16 + i * 1024, a.ws + OFF_XT + (size_t)d * LP * 2);
#pragma unroll
    for (int i = 0; i < 8; ++i) glds16_sc1(lds0 + CH_ST + i * 1024, lane * 16 + i * 1024, a.ws + OFF_ST);
  };
  const int r = c;
  f32x4 acc[4];
#pragma unroll
  for (int m = 0; m < 4; ++m) acc[m] = f32x4{0.f, 0.f, 0.f, 0.f};
  {
    const char* tb = smem + CH_TAB + (64 * w + 4 * g) * 224;
    const char* pA = tb + (c & 3) * 8;
    const char* pB = tb + 32 + (c >> 2) * 8;
    const char* pP = tb + 64 + (r >> 2) * 8;
    const char* pQ = tb + 96 + ((r < 8 ? 0 : r < 12 ? 4 : 8) + (r & 3)) * 8;
    const char* pE = tb + 192;
    f32x2 tA[2][4], tB[2][4], tP[2][4], tQ[2][4], tE3[2][4];
    f32x4 tE[2][4];
    auto ldtab = [&](auto sc_) {
      constexpr int s = decltype(sc_)::value;
#pragma unroll
      for (int i = 0; i < 4; ++i) {
        const int o = (16 * s + i) * 224;
        tA[s & 1][i] = *(const f32x2*)(pA + o); tB[s & 1][i] = *(const f32x2*)(pB + o);
        tP[s & 1][i] = *(const f32x2*)(pP + o); tQ[s & 1][i] = *(const f32x2*)(pQ + o);
        tE[s & 1][i] = *(const f32x4*)(pE + o); tE3[s & 1][i] = *(const f32x2*)(pE + o + 16);
      }
    };
    auto kstep = [&](auto sc_) {
      constexpr int s = decltype(sc_)::value;
      if constexpr (s < 3) ldtab(std::integral_constant<int, s + 1>{});
      __builtin_amdgcn_sched_barrier(0);
      u32x4 xf, y0, y1, y2, y3;
#pragma unroll
      for (int i = 0; i < 4; ++i) {
        const f32x2 yv = cmul(tA[s & 1][i], tB[s & 1][i]);
        const f32x2 xv = cmul_conj(tP[s & 1][i], tQ[s & 1][i]);
        y0[i] = pk_f16(yv[0], yv[1]);
        xf[i] = pk_f16(xv[0], xv[1]);
        const f32x4 E12 = tE[s & 1][i];
        const f32x2 v1 = cmul(yv, f32x2{E12[0], E12[1]}), v2 = cmul(yv, f32x2{E12[2], E12[3]}), v3 = cmul(yv, tE3[s & 1][i]);
        y1[i] = pk_f16(v1[0], v1[1]); y2[i] = pk_f16(v2[0], v2[1]); y3[i] = pk_f16(v3[0], v3[1]);
      }
      f16x8 xa = __builtin_bit_cast(f16x8, xf);
      __builtin_amdgcn_sched_barrier(0);
      acc[0] = __builtin_amdgcn_mfma_f32_16x16x32_f16(xa, __builtin_bit_cast(f16x8, y0), acc[0], 0, 0, 0);
      acc[1] = __builtin_amdgcn_mfma_f32_16x16x32_f16(xa, __builtin_bit_cast(f16x8, y1), acc[1], 0, 0, 0);
      acc[2] = __builtin_amdgcn_mfma_f32_16x16x32_f16(xa, __builtin_bit_cast(f16x8, y2), acc[2], 0, 0, 0);
      acc[3] = __builtin_amdgcn_mfma_f32_16x16x32_f16(xa, __builtin_bit_cast(f16x8, y3), acc[3], 0, 0, 0);
      __builtin_amdgcn_sched_barrier(0);
    };
    ldtab(std::integral_constant<int, 0>{});
    static_for<0, 4>(kstep);
  }
  if (!have) {
    if (__all(fl == tag)) { have = true; }
    else fl = ld_agent(flg);
  }
  TR(a.ws, 0, 3);
  float* part = (float*)(smem + CH_TAB);
#pragma unroll
  for (int m = 0; m < 4; ++m) *(f32x4*)(part + w * 3584 + (16 * m + r) * 16 + 4 * g) = acc[m];
  __syncthreads();
  if (w == 0 && !have) {
    if (__all(fl == tag)) { have = true; load_x(); }
    else fl = ld_agent(flg);
  } else if (w == 0) load_x();
  float* Fv = (float*)(smem + CH_F);
  const float Gm = fmaxf(fmaxf(red[0], red[1]), fmaxf(red[2], red[3]));
  float sc[4];
#pragma unroll
  for (int k = 0; k < 4; ++k) sc[k] = __builtin_amdgcn_exp2f(red[k] - Gm - 8.f);
  {
    const f32x4* pp = (const f32x4*)part;
    f32x4 sv = pp[tid] * sc[0] + pp[896 + tid] * sc[1] + pp[1792 + tid] * sc[2] + pp[2688 + tid] * sc[3];
    *(f32x4*)(Fv + tid * 4) = sv;
    if (tid == 0) { Fv[1024] = red[4] * sc[0] + red[5] * sc[1] + red[6] * sc[2] + red[7] * sc[3]; Fv[1025] = 0.f; }
  }
  const float kinv = __builtin_amdgcn_exp2f(Gm - 6.f);
  __syncthreads();
  TR(a.ws, 0, 4);
  {
    u32 e[8];
#pragma unroll
    for (int k = 0; k < 4; ++k)
      e[k] = pk_f16(*(const float*)((const char*)Fv + (slot2[k] & 0xffffu)), *(const float*)((const char*)Fv + (slot2[k] >> 16)));
#pragma unroll
    for (int k = 4; k < 8; ++k)
      e[k] = pk_f16(*(const float*)((const char*)Fv + (slot2[k] & 0xffffu)), *(const float*)((const char*)Fv + (slot2[k] >> 16)));
    auto put = [&](int ch, const u32 (&ee)[8]) {
      u32 o[7];
#pragma unroll
      for (int i = 0; i < 7; ++i) o[i] = __builtin_amdgcn_alignbyte(ee[i + 1], ee[i], 2);
#pragma unroll
      for (int s = 0; s < 4; ++s) {
        *(u32x4*)(smem + (2 * s) * CPS + ch * 16) = u32x4{ee[s], ee[s + 1], ee[s + 2], ee[s + 3]};
        *(u32x4*)(smem + (2 * s + 1) * CPS + ch * 16) = u32x4{o[s], o[s + 1], o[s + 2], o[s + 3]};
      }
    };
    put(tid, e);
    if (tid == 255) { const u32 z[8] = {0, 0, 0, 0, 0, 0, 0, 0}; put(256, z); }
  }
  u16* su = (u16*)(smem + CH_SU);
  const u16* sx = (const u16*)(smem + CH_SX);
  if (w == 0) {
    if (!have) {
      unsigned spins = 0;
      while (!__all(fl == tag)) { __builtin_amdgcn_s_sleep(2); fl = ld_agent(flg); if (++spins > (1u << 20)) break; }
      load_x();
    }
    asm volatile("s_waitcnt vmcnt(0)" ::: "memory");
  }
  __syncthreads();
  {
    const u32x4 xr = *(const u32x4*)(sx + tid * 8);
    const u32x4 sa = *(const u32x4*)(smem + CH_ST + tid * 32), sb = *(const u32x4*)(smem + CH_ST + tid * 32 + 16);
    u32x4 o;
#pragma unroll
    for (int k = 0; k < 4; ++k) {
      const u32 s0 = k < 2 ? sa[2 * k] : sb[2 * k - 4], s1 = k < 2 ? sa[2 * k + 1] : sb[2 * k - 3];
      const u32 dpk = pk_bf16(bflo(xr[k]) - bflo(s0), bfhi(xr[k]) - bflo(s1));
      const u32 upk = pk_bf16(bflo(dpk) * __builtin_amdgcn_rcpf(bfhi(s0)), bfhi(dpk) * __builtin_amdgcn_rcpf(bfhi(s1)));
      o[k] = pk_f16(bflo(upk), bfhi(upk));
    }
    *(u32x4*)(su + 256 + tid * 8) = o;
    if (tid < 32) *(u32x4*)(su + tid * 8) = u32x4{0, 0, 0, 0};
  }
  __syncthreads();
  TR(a.ws, 0, 5);
  f32x4 cv[8];
#pragma unroll
  for (int q = 0; q < 8; ++q) cv[q] = f32x4{0.f, 0.f, 0.f, 0.f};
  const int sig = (7 - c) & 7;
  const char* abase = smem + sig * CPS + 16 * ((c < 8 ? 253 : 252) + g);
#pragma unroll
  for (int rr2 = 0; rr2 < 2; ++rr2) {
    const int rs2 = 2 * w + rr2;
    f16x8 B[8];
#pragma unroll
    for (int q = 0; q < 8; ++q) B[q] = *(const f16x8*)(su + 240 + 256 * q - 32 * rs2 + 16 * c + 8 * g);
    f16x8 A[8];
#pragma unroll
    for (int m = 0; m < 8; ++m) A[m] = *(const f16x8*)(abase - 64 * (rs2 + 8 * m));
#pragma unroll
    for (int m = 0; m < 8; ++m)
#pragma unroll
      for (int q = m; q < 8; ++q) cv[q] = __builtin_amdgcn_mfma_f32_16x16x32_f16(A[m], B[q - m], cv[q], 0, 0, 0);
  }
  TR(a.ws, 0, 6);
  __syncthreads();
  f32x4* rb = (f32x4*)smem;
#pragma unroll
  for (int q = 0; q < 8; ++q)
    if ((q >> 1) != w) rb[(w * 8 + q) * 64 + lane] = cv[q];
  __syncthreads();
  const float dsk = bf2f(r_dsk);
  u16* hT = (u16*)(a.ws + OFF_HT) + (size_t)d * LP;
#pragma unroll
  for (int i = 0; i < 2; ++i) {
    f32x4 y = cv[0];
#pragma unroll
    for (int q = 0; q < 8; ++q) if (q == 2 * w + i) y = cv[q];
    const int q = 2 * w + i;
#pragma unroll
    for (int s = 1; s < 4; ++s) y += rb[(((w + s) & 3) * 8 + q) * 64 + lane];
    const int t = 256 * q + 16 * c + 4 * g;
    u32x2 uu = *(const u32x2*)(su + 256 + t);
    const u32x2 xx = *(const u32x2*)(sx + t);
    f16x2 ua = as_f16x2(uu[0]), ub = as_f16x2(uu[1]);
    float uf[4] = {(float)ua[0], (float)ua[1], (float)ub[0], (float)ub[1]};
    u32 y01 = pk_bf16(y[0] * kinv, y[1] * kinv), y23 = pk_bf16(y[2] * kinv, y[3] * kinv);
    u32 s01 = pk_bf16(uf[0] * dsk, uf[1] * dsk), s23 = pk_bf16(uf[2] * dsk, uf[3] * dsk);
    u32 r01 = pk_bf16(bflo(y01) + bflo(s01), bfhi(y01) + bfhi(s01));
    u32 r23 = pk_bf16(bflo(y23) + bflo(s23), bfhi(y23) + bfhi(s23));
    u32 h01 = pk_bf16(bflo(xx[0]) + bflo(r01), bfhi(xx[0]) + bfhi(r01));
    u32 h23 = pk_bf16(bflo(xx[1]) + bflo(r23), bfhi(xx[1]) + bfhi(r23));
    *(u32x2*)(hT + t) = u32x2{h01, h23};
  }
}

__global__ __launch_bounds__(256, 2) void ka_ssm(KAArgs a) {
  __shared__ __attribute__((aligned(16))) char smem[KA_SMEM];
  const int b = blockIdx.x;
  TR(a.ws, 0, 0);
  char* sy = a.ws + OFF_SYNC;
  if (b < KA_LN) {
    const u64 tag = ka_ln(a, b, smem);
    TR(a.ws, 0, 1);
    __syncthreads();
    for (int task = b; task < 112; task += KA_LN) {
      if (task < 56) ka_w1(a, task, smem); else ka_w2(a, task - 56, smem);
    }
    if (b == 0 && threadIdx.x < 64) {
      const int lane = threadIdx.x;
      unsigned spins = 0;
      for (;;) {
        bool ok = ld_agent(sy + SY_LN + lane * 8) == tag;
#pragma unroll
        for (int k = 0; k < 7; ++k) ok &= ld_agent(sy + SY_SEEN + (k * 64 + lane) * 8) == tag;
        if (__all(ok)) break;
        __builtin_amdgcn_s_sleep(2);
        if (++spins > (1u << 20)) break;
      }
      if (lane == 0) st_agent(sy + SY_EPA, tag);
    }
    TR(a.ws, 0, 7);
    return;
  }
  ka_channel(a, b - KA_LN, smem);
  TR(a.ws, 0, 7);
}

static constexpr int K3_SMEM = 448 * 128 + 1024;
DI u32 swz(int row) { return (u32)(((row & 3) << 2) | ((row >> 2) & 3)); }

template <int N> DI void wait_vm() { asm volatile("s_waitcnt vmcnt(%0)" :: "n"(N) : "memory"); }
DI void glds16(u32 lds_dst, u32 voff, const void* sbase) {
  u32 keep;
  asm volatile("s_mov_b32 %0, m0\n\ts_mov_b32 m0, %1\n\ts_nop 0\n\tglobal_load_lds_dwordx4 %2, %3\n\ts_mov_b32 m0, %0"
               : "=&s"(keep) : "s"(lds_dst), "v"(voff), "s"(sbase) : "memory");
}
template <int OFF> DI void gload16(u32x4& dst, u32 voff, const void* sbase) {
  asm volatile("global_load_dwordx4 %0, %1, %2 offset:%3" : "=v"(dst) : "v"(voff), "s"(sbase), "n"(OFF) : "memory");
}
template <int OFF> DI void trread(u32x2& dst, u32 addr) {
  asm volatile("ds_read_b64_tr_b16 %0, %1 offset:%2" : "=v"(dst) : "v"(addr), "n"(OFF) : "memory");
}

template <int N_> struct K3Step {
  static DI void issue(u32x2 (&B)[8][2], u32 (&ad)[2][8]) {
    constexpr int ks = N_ >> 3, c = N_ & 7, slot = N_ & 7;
    constexpr int off = (ks & 7) * 8192;
    trread<off>(B[slot][0], ad[0][c]);
    trread<off>(B[slot][1], ad[1][c]);
  }
};

template <bool FIRST> DI void mfma_bf16(f32x4& acc, const u32x4& a, const u32x4& b) {
  if constexpr (FIRST) asm volatile("v_mfma_f32_16x16x32_bf16 %0, %1, %2, 0" : "=a"(acc) : "v"(a), "v"(b));
  else asm volatile("v_mfma_f32_16x16x32_bf16 %0, %1, %2, %0" : "+a"(acc) : "v"(a), "v"(b));
}
template <bool FIRST> DI void mfma_bf16_nop(f32x4& acc, const u32x4& a, const u32x4& b) {
  if constexpr (FIRST) asm volatile("s_nop 1\n\tv_mfma_f32_16x16x32_bf16 %0, %1, %2, 0" : "=a"(acc) : "v"(a), "v"(b));
  else asm volatile("s_nop 1\n\tv_mfma_f32_16x16x32_bf16 %0, %1, %2, %0" : "+a"(acc) : "v"(a), "v"(b));
}
template <int OFF> DI void ldsread128(u32x4& dst, u32 addr) {
  asm volatile("ds_read_b128 %0, %1 offset:%2" : "=v"(dst) : "v"(addr), "n"(OFF) : "memory");
}
static constexpr int G1_R = 8;
static constexpr int G1_STG = 16384;
static constexpr int K3_SMEM2 = G1_R * G1_STG + 1024;

__global__ __launch_bounds__(512, 2) void k3_gemm1(char* ws) {
  extern __shared__ __attribute__((aligned(16))) char smem[];
  const int tid = threadIdx.x, lane = tid & 63;
  const int wv = __builtin_amdgcn_readfirstlane(tid >> 6);
  const int w = wv & 3;
  const int xcd = blockIdx.x & 7, bi = blockIdx.x >> 3;
  const int tb = 2 * xcd + bi / 14, fb = bi % 14;
  TR(ws, 2, 0);
  const int c16 = lane & 15, g = lane >> 4, q = (lane & 15) >> 2, p = lane & 3;
  const u32 lds0 = (u32)(size_t)(LDS3 char*)smem;
  f32x2* st = (f32x2*)(smem + G1_R * G1_STG);

  if (wv >= 4) {
    u32 vh[2];
#pragma unroll
    for (int k = 0; k < 2; ++k) {
      const int row = 8 * w + 4 * k + g;
      vh[k] = (u32)(row * LP * 2) + (u32)((c16 ^ swz(row)) * 16);
    }
    const char* hb = ws + OFF_HT + tb * 256;
    const char* ab = ws + OFF_W1F + (size_t)(fb * 8 + 2 * w) * 14336;
    const u32 va0 = lane * 16, va1 = lane * 16 + 14336;
    auto ld = [&](auto kc) {
      constexpr int ks = decltype(kc)::value;
      constexpr int sl = (ks % G1_R) * G1_STG;
      glds16(lds0 + sl + (8 * w) * 256, vh[0], hb + (size_t)ks * 32 * LP * 2);
      glds16(lds0 + sl + (8 * w + 4) * 256, vh[1], hb + (size_t)ks * 32 * LP * 2);
      glds16(lds0 + sl + 8192 + (2 * w) * 1024, va0, ab + ks * 1024);
      glds16(lds0 + sl + 8192 + (2 * w + 1) * 1024, va1, ab + ks * 1024);
    };
    static_for<0, G1_R>(ld);
    auto stepl = [&](auto kc) {
      constexpr int ks = decltype(kc)::value;
      constexpr int issued = (G1_R + (ks > 3 ? ks - 3 : 0)) < 14 ? (G1_R + (ks > 3 ? ks - 3 : 0)) : 14;
      wait_vm<4 * (issued - ks - 1)>();
      __builtin_amdgcn_s_barrier();
      if constexpr (ks >= 3 && ks - 3 + G1_R < 14) ld(std::integral_constant<int, ks - 3 + G1_R>{});
    };
    static_for<0, 14>(stepl);
    __builtin_amdgcn_s_barrier();
    __builtin_amdgcn_s_barrier();
    return;
  }
  const int fi = w >> 1, ti = w & 1;
  f32x4 wsv[4];
  {
    const float* w1s = (const float*)(ws + OFF_W1S) + fb * 128 + 64 * fi + 4 * g;
#pragma unroll
    for (int i = 0; i < 4; ++i) wsv[i] = *(const f32x4*)(w1s + 16 * i);
  }
  f32x4 acc[4][4];
  float s1[2] = {0.f, 0.f}, s2[2] = {0.f, 0.f};
  const bf16x2 ones = __builtin_bit_cast(bf16x2, 0x3f803f80u);
  u32 ad[2][4];
#pragma unroll
  for (int t = 0; t < 2; ++t) {
    const int row = 8 * g + 4 * t + q;
    const u32 a0 = lds0 + (u32)(row * 256) + (((u32)(p >> 1) ^ swz(row)) << 4) + 8 * (p & 1);
#pragma unroll
    for (int c = 0; c < 4; ++c) ad[t][c] = a0 ^ (u32)(32 * (4 * ti + ((2 * fi + c) & 3)));
  }
  const u32 aa = lds0 + 8192 + (4 * fi) * 1024 + lane * 16;
  u32x2 B[8][2];
  u32x4 A[2][4];
  constexpr int LA = 4;
  u32 adh[2][4];
#pragma unroll
  for (int t = 0; t < 2; ++t)
#pragma unroll
    for (int c = 0; c < 4; ++c) adh[t][c] = ad[t][c] + 65536u;
  const u32 aah = aa + 65536u;
  auto pre = [&](auto nc) {
    constexpr int n = decltype(nc)::value;
    if constexpr ((n & 3) == 0) {
      constexpr int ks = n >> 2;
      constexpr int sl = (ks % G1_R);
      constexpr int off = (sl & 3) * G1_STG;
      __builtin_amdgcn_s_barrier();
      const u32 b = sl < 4 ? aa : aah;
      ldsread128<off>(A[ks & 1][0], b); ldsread128<off + 1024>(A[ks & 1][1], b);
      ldsread128<off + 2048>(A[ks & 1][2], b); ldsread128<off + 3072>(A[ks & 1][3], b);
    }
  };
  auto rd0 = [&](auto nc) { constexpr int n = decltype(nc)::value; constexpr int sl = (n >> 2) % G1_R;
    trread<(sl & 3) * G1_STG>(B[n & 7][0], sl < 4 ? ad[0][n & 3] : adh[0][n & 3]); };
  auto rd1 = [&](auto nc) { constexpr int n = decltype(nc)::value; constexpr int sl = (n >> 2) % G1_R;
    trread<(sl & 3) * G1_STG>(B[n & 7][1], sl < 4 ? ad[1][n & 3] : adh[1][n & 3]); };
  auto issue = [&](auto nc) { pre(nc); rd0(nc); rd1(nc); };
  auto step = [&](auto nc) {
    constexpr int n = decltype(nc)::value;
    constexpr int ks = n >> 2, c = n & 3, sl = n & 7;
    constexpr int last = (55 < n + LA - 1) ? 55 : n + LA - 1;
    constexpr int nA = (last / 4) - (n / 4);
    constexpr int outst = (last - n) * 2 + nA * 4;
    static_assert(outst <= 15, "lgkm");
    asm volatile("s_waitcnt lgkmcnt(%6)" : "+v"(B[sl][0]), "+v"(B[sl][1]), "+v"(A[ks & 1][0]), "+v"(A[ks & 1][1]), "+v"(A[ks & 1][2]), "+v"(A[ks & 1][3]) : "n"(outst) : "memory");
    u32x4 bfr = {B[sl][0][0], B[sl][0][1], B[sl][1][0], B[sl][1][1]};
    mfma_bf16_nop<ks == 0>(acc[0][c], A[ks & 1][0], bfr);
    __builtin_amdgcn_sched_barrier(0);
    if constexpr (n + LA < 56) { pre(std::integral_constant<int, n + LA>{}); rd0(std::integral_constant<int, n + LA>{}); }
    __builtin_amdgcn_sched_barrier(0);
    mfma_bf16<ks == 0>(acc[1][c], A[ks & 1][1], bfr);
    __builtin_amdgcn_sched_barrier(0);
    if constexpr (n + LA < 56) rd1(std::integral_constant<int, n + LA>{});
    __builtin_amdgcn_sched_barrier(0);
    mfma_bf16<ks == 0>(acc[2][c], A[ks & 1][2], bfr);
    if constexpr (c < 2) {
#pragma unroll
      for (int k = 0; k < 2; ++k) {
        bf16x2 x2 = as_bf16x2(bfr[k]);
        s1[c] = __builtin_amdgcn_fdot2_f32_bf16(x2, ones, s1[c], false);
        s2[c] = __builtin_amdgcn_fdot2_f32_bf16(x2, x2, s2[c], false);
      }
    }
    __builtin_amdgcn_sched_barrier(0);
    mfma_bf16<ks == 0>(acc[3][c], A[ks & 1][3], bfr);
    if constexpr (c < 2) {
#pragma unroll
      for (int k = 2; k < 4; ++k) {
        bf16x2 x2 = as_bf16x2(bfr[k]);
        s1[c] = __builtin_amdgcn_fdot2_f32_bf16(x2, ones, s1[c], false);
        s2[c] = __builtin_amdgcn_fdot2_f32_bf16(x2, x2, s2[c], false);
      }
      if (ks == fb) {
        const int tt = 4 * ti + ((2 * fi + c) & 3);
        *(u32x4*)((u16*)(ws + OFF_HN) + (size_t)(tb * 128 + 16 * tt + c16) * D + 32 * fb + 8 * g) = bfr;
      }
    }
    __builtin_amdgcn_sched_barrier(0);
  };
  static_for<0, LA>(issue);
  static_for<0, 56>(step);
  asm volatile("s_nop 7\n\ts_nop 7" ::: "memory");
  TR(ws, 2, 3);
  __builtin_amdgcn_s_barrier();
#pragma unroll
  for (int i = 0; i < 2; ++i) {
    float a = s1[i], b = s2[i];
    a += __shfl_xor(a, 16); a += __shfl_xor(a, 32);
    b += __shfl_xor(b, 16); b += __shfl_xor(b, 32);
    const float mean = a * (1.0f / D);
    const float var = fmaxf(b * (1.0f / D) - mean * mean, 0.f);
    const float m = rbf(mean);
    const float sd = rbf(sqrtf(rbf(rbf(var) + 1e-5f)));
    const int tt = 4 * ti + ((2 * fi + i) & 3);
    if (lane < 16) st[tt * 16 + lane] = f32x2{m, 1.0f / sd};
  }
  asm volatile("s_waitcnt lgkmcnt(0)" ::: "memory");
  __builtin_amdgcn_s_barrier();
  u32x4* h1f = (u32x4*)(ws + OFF_H1F);
#pragma unroll
  for (int c = 0; c < 4; ++c) {
    const int tt = 4 * ti + ((2 * fi + c) & 3);
    const f32x2 ms = st[tt * 16 + c16];
#pragma unroll
    for (int pp = 0; pp < 2; ++pp) {
      float v0[4], v1[4];
#pragma unroll
      for (int k = 0; k < 4; ++k) {
        v0[k] = fmaxf((acc[2 * pp][c][k] - ms[0] * wsv[2 * pp][k]) * ms[1], 0.f);
        v1[k] = fmaxf((acc[2 * pp + 1][c][k] - ms[0] * wsv[2 * pp + 1][k]) * ms[1], 0.f);
      }
      u32x4 o = {pk_bf16(v0[0], v0[1]), pk_bf16(v0[2], v0[3]), pk_bf16(v1[0], v1[1]), pk_bf16(v1[2], v1[3])};
      h1f[((size_t)(tb * 8 + tt) * 56 + fb * 4 + 2 * fi + pp) * 64 + lane] = o;
    }
  }
  TR(ws, 2, 7);
}

static constexpr int G2_R = 4;
static constexpr int G2_STG = 32768;
static constexpr int K4_RS = 272;
static constexpr int K4_SMEM2 = G2_R * G2_STG;
__global__ __launch_bounds__(512, 2) void k4_gemm2(const char* ws, u16* out) {
  extern __shared__ __attribute__((aligned(16))) char smem[];
  const int tid = threadIdx.x, lane = tid & 63;
  const int wv = __builtin_amdgcn_readfirstlane(tid >> 6);
  const int w = wv & 3;
  const int xcd = blockIdx.x & 7, bi = blockIdx.x >> 3;
  const int tb = 4 * xcd + bi / 7, db = bi % 7;
  TR((char*)ws, 3, 0);
  const u32 lds0 = (u32)(size_t)(LDS3 char*)smem;
  u32x4 res[1];
  if (wv < 4) { }
  if (wv >= 4) {
    const char* hbase = ws + OFF_H1F + ((size_t)(tb * 4) * 56 + 14 * w) * 1024;
    const char* wbase = ws + OFF_W2F + ((size_t)(db * 4) * 56 + 14 * w) * 1024;
    u32 vo[4];
#pragma unroll
    for (int i = 0; i < 4; ++i) vo[i] = lane * 16 + i * 56 * 1024;
    auto ld = [&](auto kc) {
      constexpr int k = decltype(kc)::value;
      constexpr int sl = (k % G2_R) * G2_STG;
#pragma unroll
      for (int i = 0; i < 4; ++i) {
        glds16(lds0 + sl + w * 8192 + i * 1024, vo[i], wbase + k * 1024);
        glds16(lds0 + sl + w * 8192 + 4096 + i * 1024, vo[i], hbase + k * 1024);
      }
    };
    static_for<0, G2_R>(ld);
    auto stepl = [&](auto kc) {
      constexpr int k = decltype(kc)::value;
      constexpr int issued = (G2_R + (k > 1 ? k - 1 : 0)) < 14 ? (G2_R + (k > 1 ? k - 1 : 0)) : 14;
      wait_vm<8 * (issued - k - 1)>();
      __builtin_amdgcn_s_barrier();
      if constexpr (k >= 1 && k - 1 + G2_R < 14) ld(std::integral_constant<int, k - 1 + G2_R>{});
    };
    static_for<0, 14>(stepl);
    __builtin_amdgcn_s_barrier();
    __builtin_amdgcn_s_barrier();
  }
  f32x4 acc[4][4];
  const int c16 = lane & 15, g = lane >> 4;
  u32x4 res2;
  if (wv < 4) {
    const u16* hN = (const u16*)(ws + OFF_HN);
    res[0] = *(const u32x4*)(hN + (size_t)(tb * 64 + (tid >> 3)) * D + db * 64 + (tid & 7) * 8);
    res2 = *(const u32x4*)(hN + (size_t)(tb * 64 + 32 + (tid >> 3)) * D + db * 64 + (tid & 7) * 8);
    const u32 base = lds0 + w * 8192 + lane * 16;
    auto step = [&](auto kc) {
      constexpr int k = decltype(kc)::value;
      constexpr u32 sl = (u32)((k % G2_R) * G2_STG);
      __builtin_amdgcn_s_barrier();
      u32x4 wf[4], hf[4];
      const u32 b = base + sl;
      ldsread128<0>(wf[0], b); ldsread128<4096>(hf[0], b);
      ldsread128<1024>(wf[1], b); ldsread128<5120>(hf[1], b);
      ldsread128<2048>(wf[2], b); ldsread128<6144>(hf[2], b);
      ldsread128<3072>(wf[3], b); ldsread128<7168>(hf[3], b);
      asm volatile("s_waitcnt lgkmcnt(0)" : "+v"(wf[0]), "+v"(wf[1]), "+v"(wf[2]), "+v"(wf[3]), "+v"(hf[0]), "+v"(hf[1]), "+v"(hf[2]), "+v"(hf[3]) :: "memory");
#pragma unroll
      for (int dl = 0; dl < 4; ++dl)
#pragma unroll
        for (int ta = 0; ta < 4; ++ta)
          mfma_bf16<k == 0>(acc[dl][ta], wf[dl], hf[ta]);
      __builtin_amdgcn_sched_barrier(0);
    };
    static_for<0, 14>(step);
    asm volatile("s_nop 7\n\ts_nop 7" ::: "memory");
    TR((char*)ws, 3, 3);
    __builtin_amdgcn_s_barrier();
#pragma unroll
    for (int dl = 0; dl < 4; ++dl)
#pragma unroll
      for (int ta = 0; ta < 4; ++ta)
        *(f32x4*)(smem + (w * 64 + 16 * ta + c16) * K4_RS + (16 * dl + 4 * g) * 4) = acc[dl][ta];
    asm volatile("s_waitcnt lgkmcnt(0)" ::: "memory");
    __builtin_amdgcn_s_barrier();
  }
  if (wv < 4) {
#pragma unroll
    for (int i = 0; i < 2; ++i) {
      const int t = (tid >> 3) + 32 * i, chn = tid & 7;
      const u32x4 rr = i ? res2 : res[0];
      f32x4 s0 = {0.f, 0.f, 0.f, 0.f}, s1 = s0;
#pragma unroll
      for (int ww = 0; ww < 4; ++ww) {
        s0 += *(const f32x4*)(smem + (ww * 64 + t) * K4_RS + chn * 32);
        s1 += *(const f32x4*)(smem + (ww * 64 + t) * K4_RS + chn * 32 + 16);
      }
      u32 p0 = pk_bf16(s0[0], s0[1]), p1 = pk_bf16(s0[2], s0[3]), p2 = pk_bf16(s1[0], s1[1]), p3 = pk_bf16(s1[2], s1[3]);
      u32x4 o;
      o[0] = pk_bf16(bflo(rr[0]) + bflo(p0), bfhi(rr[0]) + bfhi(p0));
      o[1] = pk_bf16(bflo(rr[1]) + bflo(p1), bfhi(rr[1]) + bfhi(p1));
      o[2] = pk_bf16(bflo(rr[2]) + bflo(p2), bfhi(rr[2]) + bfhi(p2));
      o[3] = pk_bf16(bflo(rr[3]) + bflo(p3), bfhi(rr[3]) + bfhi(p3));
      *(u32x4*)(out + (size_t)(tb * 64 + t) * D + db * 64 + chn * 8) = o;
    }
  }
  TR((char*)ws, 3, 7);
}

extern "C" void kernel_launch(void* const* d_in, const int* in_sizes, int n_in,
                              void* d_out, int out_size, void* d_ws, size_t ws_size,
                              hipStream_t stream) {
  KAArgs a;
  a.x = (const u16*)d_in[0]; a.Are = (const u16*)d_in[1]; a.Aim = (const u16*)d_in[2];
  a.Bre = (const u16*)d_in[3]; a.Bim = (const u16*)d_in[4]; a.Cre = (const u16*)d_in[5];
  a.Cim = (const u16*)d_in[6]; a.logdt = (const u16*)d_in[7]; a.Dskip = (const u16*)d_in[8];
  a.w1 = (const u16*)d_in[10]; a.w2 = (const u16*)d_in[11]; a.ws = (char*)d_ws;
  char* ws = (char*)d_ws;
  (void)hipFuncSetAttribute((const void*)k3_gemm1, hipFuncAttributeMaxDynamicSharedMemorySize, K3_SMEM2);
  (void)hipFuncSetAttribute((const void*)k4_gemm2, hipFuncAttributeMaxDynamicSharedMemorySize, K4_SMEM2);
  ka_ssm<<<KA_LN + D, 256, 0, stream>>>(a);
  k3_gemm1<<<224, 512, K3_SMEM2, stream>>>(ws);
  k4_gemm2<<<224, 512, K4_SMEM2, stream>>>(ws, (u16*)d_out);
}
```

```cpp
#define PF_CODE 0
#define PF_W 1
#include <hip/hip_runtime.h>
#include <hip/hip_bf16.h>
#include <stdint.h>
#include <type_traits>

static constexpr int L = 2048, D = 448, NM = 256, F = 1792;
static constexpr int LP = 2176;
static constexpr size_t OFF_ST = 0;
static constexpr size_t OFF_XT = 8192;
static constexpr size_t OFF_HT = OFF_XT + (size_t)D * LP * 2;
static constexpr size_t OFF_W1S = OFF_HT + (size_t)D * LP * 2;
static constexpr size_t OFF_HN = OFF_W1S + 8192;
static constexpr size_t OFF_H1F = OFF_HN + (size_t)L * D * 2;
static constexpr size_t OFF_SYNC = OFF_H1F + (size_t)L * F * 2;
static constexpr size_t SY_EP = 0, SY_LN = 1024  , SY_W1S = 4096, SY_CV = 8192  , SY_H1 = 32768  ;
static constexpr size_t WS_USED = OFF_SYNC + 65536;

typedef unsigned short u16;
typedef unsigned int u32;
typedef unsigned long long u64;
typedef _Float16 f16;
typedef f16 f16x2 __attribute__((ext_vector_type(2)));
typedef f16 f16x8 __attribute__((ext_vector_type(8)));
typedef __bf16 bf16x2 __attribute__((ext_vector_type(2)));
typedef __bf16 bf16x8 __attribute__((ext_vector_type(8)));
typedef float f32x2 __attribute__((ext_vector_type(2)));
typedef float f32x4 __attribute__((ext_vector_type(4)));
typedef u32 u32x2 __attribute__((ext_vector_type(2)));
typedef u32 u32x4 __attribute__((ext_vector_type(4)));
typedef short s16x4 __attribute__((ext_vector_type(4)));
#define LDS3 __attribute__((address_space(3)))
typedef __attribute__((address_space(1))) u64 gu64;
typedef __amdgpu_buffer_rsrc_t rsrc_t;

#define DI __device__ __forceinline__
DI float bf2f(u16 v) { return __uint_as_float(((u32)v) << 16); }
DI float bflo(u32 v) { return __uint_as_float(v << 16); }
DI float bfhi(u32 v) { return __uint_as_float(v & 0xffff0000u); }
DI u32 pk_bf16(float a, float b) { f32x2 v = {a, b}; return __builtin_bit_cast(u32, __builtin_convertvector(v, bf16x2)); }
DI u32 pk_f16(float a, float b) { f32x2 v = {a, b}; return __builtin_bit_cast(u32, __builtin_convertvector(v, f16x2)); }
DI float rbf(float x) { return bflo(pk_bf16(x, 0.f)); }
DI f16x2 as_f16x2(u32 v) { return __builtin_bit_cast(f16x2, v); }
DI bf16x2 as_bf16x2(u32 v) { return __builtin_bit_cast(bf16x2, v); }
template <int CTRL, int RM> DI float dppf(float old, float v) {
  return __int_as_float(__builtin_amdgcn_update_dpp(__float_as_int(old), __float_as_int(v), CTRL, RM, 0xf, false));
}
DI float wave_sum(float v) {
  v += dppf<0x111, 0xf>(0.f, v); v += dppf<0x112, 0xf>(0.f, v); v += dppf<0x114, 0xf>(0.f, v); v += dppf<0x118, 0xf>(0.f, v);
  v += dppf<0x142, 0xa>(0.f, v); v += dppf<0x143, 0xc>(0.f, v);
  return __int_as_float(__builtin_amdgcn_readlane(__float_as_int(v), 63));
}
DI float wave_max(float v) {
  v = fmaxf(v, dppf<0x111, 0xf>(v, v)); v = fmaxf(v, dppf<0x112, 0xf>(v, v)); v = fmaxf(v, dppf<0x114, 0xf>(v, v)); v = fmaxf(v, dppf<0x118, 0xf>(v, v));
  v = fmaxf(v, dppf<0x142, 0xa>(v, v)); v = fmaxf(v, dppf<0x143, 0xc>(v, v));
  return __int_as_float(__builtin_amdgcn_readlane(__float_as_int(v), 63));
}
DI u64 ld_agent(const void* p) { return __hip_atomic_load((const gu64*)p, __ATOMIC_RELAXED, __HIP_MEMORY_SCOPE_AGENT); }
DI void st_agent(void* p, u64 v) { __hip_atomic_store((gu64*)p, v, __ATOMIC_RELAXED, __HIP_MEMORY_SCOPE_AGENT); }
static constexpr u64 TAG_INC = 0x9E3779B97F4A7C15ull;
DI rsrc_t ws_rsrc(char* ws) { return __builtin_amdgcn_make_buffer_rsrc(ws, 0, (int)WS_USED, 0x00020000); }
template <int B, int E, class Fn> DI void static_for(Fn&& f) {
  if constexpr (B < E) { f(std::integral_constant<int, B>{}); static_for<B + 1, E>(f); }
}
template <int N> using IC = std::integral_constant<int, N>;
DI s16x4 tr_read(const void* p) { return __builtin_amdgcn_ds_read_tr16_b64_v4i16((LDS3 s16x4*)p); }
template <int N> DI void wait_vm() { asm volatile("s_waitcnt vmcnt(%0)" :: "n"(N) : "memory"); }
DI void bar() { __builtin_amdgcn_s_barrier(); }
DI void lds_bar() { asm volatile("s_waitcnt lgkmcnt(0)" ::: "memory"); __builtin_amdgcn_s_barrier(); }
template <bool SC1> DI void glds16(u32 lds_dst, u32 voff, const void* sbase) {
  u32 keep;
  if constexpr (SC1)
    asm volatile("s_mov_b32 %0, m0\n\ts_mov_b32 m0, %1\n\ts_nop 0\n\tglobal_load_lds_dwordx4 %2, %3 sc1\n\ts_mov_b32 m0, %0"
                 : "=&s"(keep) : "s"(lds_dst), "v"(voff), "s"(sbase) : "memory");
  else
    asm volatile("s_mov_b32 %0, m0\n\ts_mov_b32 m0, %1\n\ts_nop 0\n\tglobal_load_lds_dwordx4 %2, %3\n\ts_mov_b32 m0, %0"
                 : "=&s"(keep) : "s"(lds_dst), "v"(voff), "s"(sbase) : "memory");
}
template <int OFF> DI void trread(u32x2& dst, u32 addr) {
  asm volatile("ds_read_b64_tr_b16 %0, %1 offset:%2" : "=v"(dst) : "v"(addr), "n"(OFF) : "memory");
}
template <int OFF> DI void ldsread128(u32x4& dst, u32 addr) {
  asm volatile("ds_read_b128 %0, %1 offset:%2" : "=v"(dst) : "v"(addr), "n"(OFF) : "memory");
}
template <bool FIRST> DI void mfma_bf16(f32x4& acc, const u32x4& a, const u32x4& b) {
  if constexpr (FIRST) asm volatile("s_nop 1\n\tv_mfma_f32_16x16x32_bf16 %0, %1, %2, 0" : "=v"(acc) : "v"(a), "v"(b));
  else asm volatile("s_nop 1\n\tv_mfma_f32_16x16x32_bf16 %0, %1, %2, %0" : "+v"(acc) : "v"(a), "v"(b));
}
DI u32x4 cat(const u32x2& a, const u32x2& b) { return u32x4{a[0], a[1], b[0], b[1]}; }
DI u32 swz(int row) { return (u32)(((row & 3) << 2) | ((row >> 2) & 3)); }
DI f32x2 cmul(f32x2 y, f32x2 e) {
  f32x2 t, r;
  asm("v_pk_mul_f32 %0, %1, %2 op_sel_hi:[1,0]" : "=v"(t) : "v"(y), "v"(e));
  asm("v_pk_fma_f32 %0, %1, %2, %3 op_sel:[1,1,0] op_sel_hi:[0,1,1] neg_lo:[0,1,0]" : "=v"(r) : "v"(y), "v"(e), "v"(t));
  return r;
}
DI f32x2 cmul_conj(f32x2 p, f32x2 q) {
  f32x2 t, r;
  asm("v_pk_mul_f32 %0, %1, %2 op_sel_hi:[1,0] neg_hi:[1,0]" : "=v"(t) : "v"(p), "v"(q));
  asm("v_pk_fma_f32 %0, %1, %2, %3 op_sel:[1,1,0] op_sel_hi:[0,1,1] neg_lo:[1,0,0] neg_hi:[1,0,0]" : "=v"(r) : "v"(p), "v"(q), "v"(t));
  return r;
}

#define TR(ws, slot) do {} while (0)
#define TRW(ws, slot, cond) do {} while (0)
#define TRDUMP(ws) do {} while (0)

struct Args {
  const u16 *x, *Are, *Aim, *Bre, *Bim, *Cre, *Cim, *logdt, *Dskip, *w1, *w2;
  char* ws;
  u16* out;
  const void* pad;
};

constexpr int rne_bf16_int(int l) {
  if (l < 256) return l;
  int sh = 0; while ((l >> sh) >= 256) ++sh;
  const int q = l >> sh, rem = l & ((1 << sh) - 1), half = 1 << (sh - 1);
  const int up = (rem > half) || (rem == half && (q & 1));
  return (q + up) << sh;
}
constexpr int kslot(int m) {
  const int l = 2047 - m;
  if (l < 0) return 1025;
  const int t = rne_bf16_int(l);
  if (t >= 2048) return 1024;
  const int seg = (t >= 256) + (t >= 512) + (t >= 1024);
  const int row = seg == 0 ? (t & 7) : seg == 1 ? 8 + ((t >> 1) & 3) : seg == 2 ? 12 + ((t >> 2) & 1) : 14 + ((t >> 9) & 1);
  const int b = (t >> 3) & (seg >= 2 ? 63 : 31);
  return b * 16 + row;
}
struct SlotTab { u16 v[2064]; };
constexpr SlotTab make_slot_tab() { SlotTab t{}; for (int m = 0; m < 2064; ++m) t.v[m] = (u16)(kslot(m) * 4); return t; }
__device__ __attribute__((aligned(16))) const SlotTab g_slot_tab = make_slot_tab();

static constexpr int NAUX = 32;
static constexpr int NMAIN = 224;
static constexpr int SMEM_BYTES = 160 * 1024;
static constexpr unsigned SPIN_CAP = 1u << 20;
static constexpr int LDS_SIG = SMEM_BYTES - 272;

template <int NPL> DI void wait_flags(const char* base, int n, u64 tag) {
  const int lane = threadIdx.x & 63;
  unsigned spins = 0;
  for (;;) {
    bool ok = true;
#pragma unroll
    for (int k = 0; k < NPL; ++k) { const int i = k * 64 + lane; if (i < n) ok &= (ld_agent(base + i * 8) == tag); }
    if (__all(ok)) break;
    __builtin_amdgcn_s_sleep(1);
    if (++spins > SPIN_CAP) break;
  }
}

template <int NPL> DI void loaders_wait(char* smem, int idx, int w, const char* base, int n, u64 tag) {
  volatile u32* sig = (volatile u32*)(smem + LDS_SIG) + idx;
  if (w == 0) {
    wait_flags<NPL>(base, n, tag);
    *sig = 1u;
  } else {
    unsigned spins = 0;
    while (*sig == 0u) { __builtin_amdgcn_s_sleep(1); if (++spins > (SPIN_CAP << 4)) break; }
  }
  asm volatile("" ::: "memory");
}

DI void l2_touch(const char* base, size_t bytes, int part, int nparts) {
  const int tid = threadIdx.x;
  const size_t per = (bytes / nparts + 8191) & ~(size_t)8191;
  const size_t b0 = per * part, b1 = (b0 + per < bytes) ? b0 + per : bytes;
  u32 acc = 0;
  for (size_t o = b0 + (size_t)tid * 16; o + 16 <= b1; o += 8192 * 4) {
    u32x4 v0 = *(const u32x4*)(base + o), v1 = {0, 0, 0, 0}, v2 = v1, v3 = v1;
    if (o + 8192 + 16 <= b1) v1 = *(const u32x4*)(base + o + 8192);
    if (o + 16384 + 16 <= b1) v2 = *(const u32x4*)(base + o + 16384);
    if (o + 24576 + 16 <= b1) v3 = *(const u32x4*)(base + o + 24576);
    acc ^= v0[0] ^ v1[0] ^ v2[0] ^ v3[0];
  }
  asm volatile("" :: "v"(acc));
}
static constexpr int LNRS = 912;
DI void aux_block(const Args& a, int blk, char* smem) {
  const int tid = threadIdx.x, lane = tid & 63;
  const int w = __builtin_amdgcn_readfirstlane(tid >> 6);
  const int t0 = blk * 64;
  __builtin_amdgcn_s_setprio(2);
  u32x4 v[8];
#pragma unroll
  for (int i = 0; i < 8; ++i) {
    v[i] = u32x4{0, 0, 0, 0};
    if (lane < 56) v[i] = *(const u32x4*)(a.x + (size_t)(t0 + 8 * w + i) * D + lane * 8);
  }
#pragma unroll
  for (int i = 0; i < 8; ++i)
    if (lane < 56) *(u32x4*)(smem + (8 * w + i) * LNRS + lane * 16) = v[i];
  TR(a.ws, 1);
  __syncthreads();
  TR(a.ws, 5);
  const rsrc_t rs = ws_rsrc(a.ws);
  const int g = lane >> 4, i16 = lane & 15, q = i16 >> 2, p = lane & 3;
  if (w >= 4) {
    const char* src0 = smem + (16 * g + 8 * (p >> 1) + q) * LNRS + (4 * (p & 1)) * 2;
    const u32 off0 = (u32)(OFF_XT + (size_t)(i16 & 7) * LP * 2 + (t0 + 16 * g + 8 * (i16 >> 3)) * 2);
#pragma unroll
    for (int h2 = 0; h2 < 2; ++h2) {
      s16x4 lo[7], hi[7];
#pragma unroll
      for (int k = 0; k < 7; ++k) {
        const int cg = (w - 4) + 4 * (k + 7 * h2);
        lo[k] = tr_read(src0 + cg * 16); hi[k] = tr_read(src0 + cg * 16 + 4 * LNRS);
      }
      __builtin_amdgcn_sched_barrier(0);
#pragma unroll
      for (int k = 0; k < 7; ++k) {
        const int cg = (w - 4) + 4 * (k + 7 * h2);
        __builtin_amdgcn_raw_buffer_store_b128(cat(__builtin_bit_cast(u32x2, lo[k]), __builtin_bit_cast(u32x2, hi[k])), rs, off0 + (u32)(cg * 8 * LP * 2), 0, 16);
      }
      __builtin_amdgcn_sched_barrier(0);
    }
  }
  TR(a.ws, 4);
  if (w < 4) {
    f32x4 a1 = {0.f, 0.f, 0.f, 0.f}, a2 = a1;
    const u32x4 one4 = {0x3f803f80u, 0x3f803f80u, 0x3f803f80u, 0x3f803f80u};
    bf16x8 fr[14];
#pragma unroll
    for (int ks = 0; ks < 14; ++ks) fr[ks] = *(const bf16x8*)(smem + (16 * w + i16) * LNRS + (32 * ks + 8 * g) * 2);
    __builtin_amdgcn_sched_barrier(0);
#pragma unroll
    for (int ks = 0; ks < 14; ++ks) {
      a1 = __builtin_amdgcn_mfma_f32_16x16x32_bf16(fr[ks], __builtin_bit_cast(bf16x8, one4), a1, 0, 0, 0);
      a2 = __builtin_amdgcn_mfma_f32_16x16x32_bf16(fr[ks], fr[ks], a2, 0, 0, 0);
    }
    const float s = p == 0 ? a1[0] : p == 1 ? a1[1] : p == 2 ? a1[2] : a1[3];
    const float q2 = p == 0 ? a2[0] : p == 1 ? a2[1] : p == 2 ? a2[2] : a2[3];
    const float mean = s * (1.0f / D);
    const float var = fmaxf(q2 * (1.0f / D) - mean * mean, 0.f);
    const float sd = rbf(__builtin_amdgcn_sqrtf(rbf(rbf(var) + 1e-5f)));
    if (g == q) __builtin_amdgcn_raw_buffer_store_b32(pk_bf16(mean, sd), rs, (u32)(OFF_ST + (t0 + 16 * w + i16) * 4), 0, 16);
  }
  const u64 ep = ld_agent(w == 0 ? (const char*)(a.ws + OFF_SYNC + SY_EP) : (const char*)(a.x + (size_t)(t0 + w) * D));
  TR(a.ws, 2);
  asm volatile("s_waitcnt vmcnt(0)" ::: "memory");
  const u64 tag = ep + TAG_INC;
  __syncthreads();
  if (tid < 8) st_agent(a.ws + OFF_SYNC + SY_LN + tid * 256 + blk * 8, tag);
  __builtin_amdgcn_s_setprio(0);
  TR(a.ws, 3);
  {
    float* red = (float*)smem;
    const int cp = tid % 28, rg = tid / 28;
    float s0 = 0.f, s1 = 0.f;
    if (rg < 16) {
      u32 x[28];
#pragma unroll
      for (int i = 0; i < 28; ++i) x[i] = *(const u32*)(a.w1 + (size_t)(rg + 16 * i) * F + 56 * blk + 2 * cp);
#pragma unroll
      for (int i = 0; i < 28; ++i) { s0 += bflo(x[i]); s1 += bfhi(x[i]); }
      red[rg * 56 + 2 * cp] = s0; red[rg * 56 + 2 * cp + 1] = s1;
    }
    __syncthreads();
    if (tid < 56) {
      float s = 0.f;
#pragma unroll
      for (int i = 0; i < 16; ++i) s += red[i * 56 + tid];
      __builtin_amdgcn_raw_buffer_store_b32(__float_as_uint(s), rs, (u32)(OFF_W1S + (56 * blk + tid) * 4), 0, 16);
    }
    asm volatile("s_waitcnt vmcnt(0)" ::: "memory");
    __syncthreads();
    if (tid == 0) st_agent(a.ws + OFF_SYNC + SY_W1S + blk * 8, tag);
  }
  l2_touch((const char*)a.w1, (size_t)D * F * 2, blk >> 3, 4);
  l2_touch((const char*)a.w2, (size_t)D * F * 2, blk >> 3, 4);
  if (blk == 0 && w == 0) {
    wait_flags<1>(a.ws + OFF_SYNC + SY_LN, NAUX, tag);
    wait_flags<4>(a.ws + OFF_SYNC + SY_CV, NMAIN, tag);
    if (lane == 0) st_agent(a.ws + OFF_SYNC + SY_EP, tag);
  }
}

static constexpr int CPS = 4160;
static constexpr int CH_BYTES = 61440;
static constexpr int CH_TAB = 0;
static constexpr int CH_SU = 8 * CPS;
static constexpr int CH_F = CH_SU + 4608 + 256;
static constexpr int CH_SX = 47104;
static constexpr int CH_ST = CH_SX + 4096;
static constexpr int CH_RED = 59392;

DI u64 phase_a(const Args& a, int m, char* smem_all) {
  const int tid = threadIdx.x & 255, lane = threadIdx.x & 63;
  const int wv = __builtin_amdgcn_readfirstlane(threadIdx.x >> 6);
  const int w = wv & 3, hw = wv >> 2;
  const int d = 2 * m + hw;
  char* smem = smem_all + hw * CH_BYTES;
  const float LOG2E = 1.4426950408889634f, INV2PI = 0.15915494309189535f;
  const u32 lds0 = (u32)(size_t)(LDS3 char*)smem;
  const int n = tid;
  const u16 r_are = a.Are[d * NM + n], r_aim = a.Aim[d * NM + n], r_bre = a.Bre[d * NM + n], r_bim = a.Bim[d * NM + n];
  const u16 r_cre = a.Cre[d * NM + n], r_cim = a.Cim[d * NM + n], r_ldt = a.logdt[d], r_dsk = a.Dskip[d];
  u32 slot2[8];
  {
    const u32x4 s0 = *(const u32x4*)(g_slot_tab.v + 8 * tid), s1 = *(const u32x4*)(g_slot_tab.v + 8 * tid + 8);
#pragma unroll
    for (int k = 0; k < 4; ++k) { slot2[k] = s0[k]; slot2[4 + k] = s1[k]; }
  }
  const u64 ep = ld_agent(wv == 0 ? (const char*)(a.ws + OFF_SYNC + SY_EP) : (const char*)(a.Are + d * NM + 4 * w));
  float* red = (float*)(smem + CH_RED);
  const float dt = rbf(expf(bf2f(r_ldt)));
  const float bre = dt * bf2f(r_bre), bim = dt * bf2f(r_bim);
  const float cre = bf2f(r_cre), cim = bf2f(r_cim);
  const float zr = (dt * bf2f(r_are)) * LOG2E, zi = (dt * bf2f(r_aim)) * INV2PI;
  const float cr = cre * bre - cim * bim, ci = cre * bim + cim * bre;
  const float mx = fmaxf(fabsf(cr), fabsf(ci));
  const float lc = mx > 1e-36f ? __builtin_amdgcn_logf(mx) + 0.5f : -1000.f;
  const float sY = fmaxf(0.f, zr * 504.f);
  const float gm = fmaxf(lc + fmaxf(0.f, zr * 1536.f) + sY, lc + zr * 2048.f);
  const float G = ceilf(wave_max(gm));
  auto cexp = [&](float t, float lg) -> f32x2 {
    const float mm = __builtin_amdgcn_exp2f(__builtin_fmaf(zr, t, lg)), f = __builtin_amdgcn_fractf(zi * t);
    return f32x2{mm * __builtin_amdgcn_cosf(f), mm * __builtin_amdgcn_sinf(f)};
  };
  {
    f32x2* tb = (f32x2*)(smem + CH_TAB + n * 224);
    const f32x2 e1 = cexp(1.f, 0.f), e2 = cmul(e1, e1), e3 = cmul(e2, e1), e4 = cexp(4.f, 0.f), e6 = cmul(e3, e3);
    const f32x2 e8 = cexp(8.f, 0.f), e16 = cmul(e8, e8), e24 = cmul(e16, e8);
    const f32x2 e32 = cexp(32.f, 0.f), e64 = cmul(e32, e32), e96 = cmul(e64, e32);
    const f32x2 e128 = cexp(128.f, 0.f), e256 = cexp(256.f, 0.f), e384 = cmul(e256, e128);
    const f32x2 e512 = cexp(512.f, 0.f), e1024 = cexp(1024.f, 0.f);
    const float sy2 = __builtin_amdgcn_exp2f(-sY);
    const float cs = __builtin_amdgcn_exp2f(sY - G + 14.f);
    const f32x2 c0 = {cr * cs, ci * cs};
    *(f32x4*)(tb + 0) = f32x4{1.f, 0.f, e8[0], e8[1]};      *(f32x4*)(tb + 2) = f32x4{e16[0], e16[1], e24[0], e24[1]};
    *(f32x4*)(tb + 4) = f32x4{sy2, 0.f, e32[0] * sy2, e32[1] * sy2}; *(f32x4*)(tb + 6) = f32x4{e64[0] * sy2, e64[1] * sy2, e96[0] * sy2, e96[1] * sy2};
    const f32x2 p1 = cmul(c0, e4), p2 = cmul(c0, e256), p3 = cmul(c0, e512);
    *(f32x4*)(tb + 8) = f32x4{c0[0], c0[1], p1[0], p1[1]};  *(f32x4*)(tb + 10) = f32x4{p2[0], p2[1], p3[0], p3[1]};
    *(f32x4*)(tb + 12) = f32x4{1.f, 0.f, e1[0], e1[1]};     *(f32x4*)(tb + 14) = f32x4{e2[0], e2[1], e3[0], e3[1]};
    *(f32x4*)(tb + 16) = f32x4{1.f, 0.f, e2[0], e2[1]};     *(f32x4*)(tb + 18) = f32x4{e4[0], e4[1], e6[0], e6[1]};
    *(f32x4*)(tb + 20) = f32x4{1.f, 0.f, e4[0], e4[1]};     *(f32x4*)(tb + 22) = f32x4{e512[0], e512[1], e1024[0], e1024[1]};
    *(f32x4*)(tb + 24) = f32x4{e128[0], e128[1], e256[0], e256[1]}; *(f32x4*)(tb + 26) = f32x4{e384[0], e384[1], 0.f, 0.f};
    const f32x2 e2048 = cexp(2048.f, 14.f - G);
    float t2048 = cr * e2048[0] - ci * e2048[1];
    t2048 = wave_sum(t2048);
    if (lane == 0) { red[w] = G; red[4 + w] = t2048; }
  }
  __builtin_amdgcn_fence(__ATOMIC_RELEASE, "wavefront");
  __builtin_amdgcn_wave_barrier();
  TR(a.ws, 1);
  const u64 tag = ep + TAG_INC;
  const char* flg = a.ws + OFF_SYNC + SY_LN + (m & 7) * 256 + (lane & 31) * 8;
  u64 fl = 0;
  bool have = (wv != 0);
  const int c = lane & 15, g = lane >> 4;
  auto load_x = [&]() {
#pragma unroll
    for (int i = 0; i < 8; ++i) glds16<true>(lds0 + CH_ST + i * 1024, lane * 16 + i * 1024, a.ws + OFF_ST);
#pragma unroll
    for (int i = 0; i < 4; ++i) {
      glds16<true>(lds0 + CH_SX + i * 1024, lane * 16 + i * 1024, a.ws + OFF_XT + (size_t)(2 * m) * LP * 2);
      glds16<true>(lds0 + CH_BYTES + CH_SX + i * 1024, lane * 16 + i * 1024, a.ws + OFF_XT + (size_t)(2 * m + 1) * LP * 2);
    }
  };
  const int r = c;
  f32x4 acc[4];
#pragma unroll
  for (int k = 0; k < 4; ++k) acc[k] = f32x4{0.f, 0.f, 0.f, 0.f};
  {
    const char* tb = smem + CH_TAB + (64 * w + 4 * g) * 224;
    const char* pA = tb + (c & 3) * 8;
    const char* pB = tb + 32 + (c >> 2) * 8;
    const char* pP = tb + 64 + (r >> 2) * 8;
    const char* pQ = tb + 96 + ((r < 8 ? 0 : r < 12 ? 4 : 8) + (r & 3)) * 8;
    const char* pE = tb + 192;
    f32x2 tA[2][4], tB[2][4], tP[2][4], tQ[2][4], tE3[2][4];
    f32x4 tE[2][4];
    auto ldtab = [&](auto sc_) {
      constexpr int s = decltype(sc_)::value;
#pragma unroll
      for (int i = 0; i < 4; ++i) {
        const int o = (16 * s + i) * 224;
        tA[s & 1][i] = *(const f32x2*)(pA + o); tB[s & 1][i] = *(const f32x2*)(pB + o);
        tP[s & 1][i] = *(const f32x2*)(pP + o); tQ[s & 1][i] = *(const f32x2*)(pQ + o);
        tE[s & 1][i] = *(const f32x4*)(pE + o); tE3[s & 1][i] = *(const f32x2*)(pE + o + 16);
      }
    };
    auto kstep = [&](auto sc_) {
      constexpr int s = decltype(sc_)::value;
      if constexpr (s < 3) ldtab(IC<s + 1>{});
      __builtin_amdgcn_sched_barrier(0);
      u32x4 xf, y0, y1, y2, y3;
#pragma unroll
      for (int i = 0; i < 4; ++i) {
        const f32x2 yv = cmul(tA[s & 1][i], tB[s & 1][i]);
        const f32x2 xv = cmul_conj(tP[s & 1][i], tQ[s & 1][i]);
        y0[i] = pk_f16(yv[0], yv[1]);
        xf[i] = pk_f16(xv[0], xv[1]);
        const f32x4 E12 = tE[s & 1][i];
        const f32x2 v1 = cmul(yv, f32x2{E12[0], E12[1]}), v2 = cmul(yv, f32x2{E12[2], E12[3]}), v3 = cmul(yv, tE3[s & 1][i]);
        y1[i] = pk_f16(v1[0], v1[1]); y2[i] = pk_f16(v2[0], v2[1]); y3[i] = pk_f16(v3[0], v3[1]);
      }
      f16x8 xa = __builtin_bit_cast(f16x8, xf);
      __builtin_amdgcn_sched_barrier(0);
      acc[0] = __builtin_amdgcn_mfma_f32_16x16x32_f16(xa, __builtin_bit_cast(f16x8, y0), acc[0], 0, 0, 0);
      acc[1] = __builtin_amdgcn_mfma_f32_16x16x32_f16(xa, __builtin_bit_cast(f16x8, y1), acc[1], 0, 0, 0);
      acc[2] = __builtin_amdgcn_mfma_f32_16x16x32_f16(xa, __builtin_bit_cast(f16x8, y2), acc[2], 0, 0, 0);
      acc[3] = __builtin_amdgcn_mfma_f32_16x16x32_f16(xa, __builtin_bit_cast(f16x8, y3), acc[3], 0, 0, 0);
      __builtin_amdgcn_sched_barrier(0);
    };
    ldtab(IC<0>{});
    static_for<0, 4>(kstep);
  }
  if (wv == 0) fl = ld_agent(flg);
  TR(a.ws, 2);
  float* part = (float*)(smem + CH_TAB);
#pragma unroll
  for (int k = 0; k < 4; ++k) *(f32x4*)(part + w * 3584 + (16 * k + r) * 16 + 4 * g) = acc[k];
  __syncthreads();
  float* Fv = (float*)(smem + CH_F);
  const float Gm = fmaxf(fmaxf(red[0], red[1]), fmaxf(red[2], red[3]));
  float sc[4];
#pragma unroll
  for (int k = 0; k < 4; ++k) sc[k] = __builtin_amdgcn_exp2f(red[k] - Gm - 8.f);
  {
    const f32x4* pp = (const f32x4*)part;
    f32x4 sv = pp[tid] * sc[0] + pp[896 + tid] * sc[1] + pp[1792 + tid] * sc[2] + pp[2688 + tid] * sc[3];
    *(f32x4*)(Fv + tid * 4) = sv;
    if (tid == 0) { Fv[1024] = red[4] * sc[0] + red[5] * sc[1] + red[6] * sc[2] + red[7] * sc[3]; Fv[1025] = 0.f; }
  }
  const float kinv = __builtin_amdgcn_exp2f(Gm - 6.f);
  if (wv == 0) {
    if (__all(fl == tag)) { have = true; load_x(); }
    else fl = ld_agent(flg);
  }
  __syncthreads();
  TR(a.ws, 3);
  {
    u32 e[8];
#pragma unroll
    for (int k = 0; k < 8; ++k)
      e[k] = pk_f16(*(const float*)((const char*)Fv + (slot2[k] & 0xffffu)), *(const float*)((const char*)Fv + (slot2[k] >> 16)));
    auto put = [&](int ch, const u32 (&ee)[8]) {
      u32 o[7];
#pragma unroll
      for (int i = 0; i < 7; ++i) o[i] = __builtin_amdgcn_alignbyte(ee[i + 1], ee[i], 2);
#pragma unroll
      for (int s = 0; s < 4; ++s) {
        *(u32x4*)(smem + (2 * s) * CPS + ch * 16) = u32x4{ee[s], ee[s + 1], ee[s + 2], ee[s + 3]};
        *(u32x4*)(smem + (2 * s + 1) * CPS + ch * 16) = u32x4{o[s], o[s + 1], o[s + 2], o[s + 3]};
      }
    };
    put(tid, e);
    if (tid == 255) { const u32 z[8] = {0, 0, 0, 0, 0, 0, 0, 0}; put(256, z); }
  }
  u16* su = (u16*)(smem + CH_SU);
  const u16* sx = (const u16*)(smem + CH_SX);
  if (wv == 0) {
    if (!have) {
      unsigned spins = 0;
      while (!__all(fl == tag)) { __builtin_amdgcn_s_sleep(2); fl = ld_agent(flg); if (++spins > SPIN_CAP) break; }
      load_x();
    }
    asm volatile("s_waitcnt vmcnt(0)" ::: "memory");
  }
  __syncthreads();
  TR(a.ws, 4);
  {
    const u32x4 xr = *(const u32x4*)(sx + tid * 8);
    const u32x4 sa = *(const u32x4*)(smem_all + CH_ST + tid * 32), sb = *(const u32x4*)(smem_all + CH_ST + tid * 32 + 16);
    u32x4 o;
#pragma unroll
    for (int k = 0; k < 4; ++k) {
      const u32 s0 = k < 2 ? sa[2 * k] : sb[2 * k - 4], s1 = k < 2 ? sa[2 * k + 1] : sb[2 * k - 3];
      const u32 dpk = pk_bf16(bflo(xr[k]) - bflo(s0), bfhi(xr[k]) - bflo(s1));
      const u32 upk = pk_bf16(bflo(dpk) * __builtin_amdgcn_rcpf(bfhi(s0)), bfhi(dpk) * __builtin_amdgcn_rcpf(bfhi(s1)));
      o[k] = pk_f16(bflo(upk), bfhi(upk));
    }
    *(u32x4*)(su + 256 + tid * 8) = o;
    if (tid < 32) *(u32x4*)(su + tid * 8) = u32x4{0, 0, 0, 0};
  }
  __syncthreads();
  TR(a.ws, 5);
  f32x4 cv[8];
#pragma unroll
  for (int q = 0; q < 8; ++q) cv[q] = f32x4{0.f, 0.f, 0.f, 0.f};
  const int sig = (7 - c) & 7;
  const char* abase = smem + sig * CPS + 16 * ((c < 8 ? 253 : 252) + g);
#pragma unroll
  for (int rr2 = 0; rr2 < 2; ++rr2) {
    const int rs2 = 2 * w + rr2;
    f16x8 B[8];
#pragma unroll
    for (int q = 0; q < 8; ++q) B[q] = *(const f16x8*)(su + 240 + 256 * q - 32 * rs2 + 16 * c + 8 * g);
    f16x8 A[8];
#pragma unroll
    for (int k = 0; k < 8; ++k) A[k] = *(const f16x8*)(abase - 64 * (rs2 + 8 * k));
#pragma unroll
    for (int k = 0; k < 8; ++k)
#pragma unroll
      for (int q = k; q < 8; ++q) cv[q] = __builtin_amdgcn_mfma_f32_16x16x32_f16(A[k], B[q - k], cv[q], 0, 0, 0);
  }
  TR(a.ws, 6);
  __syncthreads();
  f32x4* rb = (f32x4*)smem;
#pragma unroll
  for (int q = 0; q < 8; ++q)
    if ((q >> 1) != w) rb[(w * 8 + q) * 64 + lane] = cv[q];
  __syncthreads();
  const float dsk = bf2f(r_dsk);
  const rsrc_t rs = ws_rsrc(a.ws);
#pragma unroll
  for (int i = 0; i < 2; ++i) {
    f32x4 y = cv[0];
#pragma unroll
    for (int q = 0; q < 8; ++q) if (q == 2 * w + i) y = cv[q];
    const int q = 2 * w + i;
#pragma unroll
    for (int s = 1; s < 4; ++s) y += rb[(((w + s) & 3) * 8 + q) * 64 + lane];
    const int t = 256 * q + 16 * c + 4 * g;
    u32x2 uu = *(const u32x2*)(su + 256 + t);
    const u32x2 xx = *(const u32x2*)(sx + t);
    f16x2 ua = as_f16x2(uu[0]), ub = as_f16x2(uu[1]);
    float uf[4] = {(float)ua[0], (float)ua[1], (float)ub[0], (float)ub[1]};
    u32 y01 = pk_bf16(y[0] * kinv, y[1] * kinv), y23 = pk_bf16(y[2] * kinv, y[3] * kinv);
    u32 s01 = pk_bf16(uf[0] * dsk, uf[1] * dsk), s23 = pk_bf16(uf[2] * dsk, uf[3] * dsk);
    u32 r01 = pk_bf16(bflo(y01) + bflo(s01), bfhi(y01) + bfhi(s01));
    u32 r23 = pk_bf16(bflo(y23) + bflo(s23), bfhi(y23) + bfhi(s23));
    u32 h01 = pk_bf16(bflo(xx[0]) + bflo(r01), bfhi(xx[0]) + bfhi(r01));
    u32 h23 = pk_bf16(bflo(xx[1]) + bflo(r23), bfhi(xx[1]) + bfhi(r23));
    __builtin_amdgcn_raw_buffer_store_b64(u32x2{h01, h23}, rs, (u32)(OFF_HT + (size_t)d * LP * 2 + t * 2), 0, 16);
  }
  if (threadIdx.x == 0) { *(volatile u32*)(smem_all + LDS_SIG) = 0u; *(volatile u32*)(smem_all + LDS_SIG + 4) = 0u; *(volatile u64*)(smem_all + LDS_SIG + 8) = tag; }
  asm volatile("s_waitcnt vmcnt(0) lgkmcnt(0)" ::: "memory");
  __syncthreads();
  if (threadIdx.x < 8) st_agent(a.ws + OFF_SYNC + SY_CV + threadIdx.x * 2048 + m * 8, tag);
  TR(a.ws, 7);
  return *(volatile u64*)(smem_all + LDS_SIG + 8);
}

static constexpr int G1_R = 8;
static constexpr int G1_STG = 16384;
static constexpr int G1_ST = G1_R * G1_STG;

constexpr int g1_cnt(int n) {
  if (n < 0) return 0;
  const int ks = n >> 2, c = n & 3;
  return (n + 4 < 56 ? 2 : 0) + ((c < 2 && ks + 1 < 14) ? 4 : 0);
}
constexpr int g1_wait(int n) {
  const int c = n & 3;
  const int breq = (g1_cnt(n - 4) - 2) + g1_cnt(n - 3) + g1_cnt(n - 2) + g1_cnt(n - 1);
  const int areq = g1_cnt(n - 2) + g1_cnt(n - 1);
  return (c == 0 && areq < breq) ? areq : breq;
}

DI void phase_b(const Args& a, int m, char* smem, u64 tag) {
  const int tid = threadIdx.x, lane = tid & 63;
  const int wv = __builtin_amdgcn_readfirstlane(tid >> 6);
  const int w = wv & 3;
  const int xcd = m & 7, bi = m >> 3;
  const int tb = 2 * xcd + bi / 14, fb = bi % 14;
  const int c16 = lane & 15, g = lane >> 4, q = (lane & 15) >> 2, p = lane & 3;
  const u32 lds0 = (u32)(size_t)(LDS3 char*)smem;
  f32x2* st = (f32x2*)(smem + G1_ST);
  char* ws = a.ws;

  if (wv >= 4) {
    u32 vh[2], vw[2];
#pragma unroll
    for (int k = 0; k < 2; ++k) {
      const int row = 8 * w + 4 * k + g;
      vh[k] = (u32)(row * LP * 2) + (u32)((c16 ^ swz(row)) * 16);
      vw[k] = (u32)(row * F * 2) + (u32)((c16 ^ swz(row)) * 16);
    }
    const char* hb = ws + OFF_HT + tb * 256;
    const char* wb = (const char*)a.w1 + fb * 256;
    auto ldw = [&](auto kc) {
      constexpr int ks = decltype(kc)::value;
      constexpr int sl = (ks % G1_R) * G1_STG + 8192;
      glds16<false>(lds0 + sl + (8 * w) * 256, vw[0], wb + (size_t)ks * 32 * F * 2);
      glds16<false>(lds0 + sl + (8 * w + 4) * 256, vw[1], wb + (size_t)ks * 32 * F * 2);
    };
    auto ldh = [&](auto kc) {
      constexpr int ks = decltype(kc)::value;
      constexpr int sl = (ks % G1_R) * G1_STG;
      glds16<true>(lds0 + sl + (8 * w) * 256, vh[0], hb + (size_t)ks * 32 * LP * 2);
      glds16<true>(lds0 + sl + (8 * w + 4) * 256, vh[1], hb + (size_t)ks * 32 * LP * 2);
    };
    static_for<0, G1_R>(ldw);
    loaders_wait<4>(smem, 0, w, ws + OFF_SYNC + SY_CV + xcd * 2048, NMAIN, tag);
    TRW(ws, 8, tid == 256);
    static_for<0, G1_R>(ldh);
    auto stepl = [&](auto kc) {
      constexpr int ks = decltype(kc)::value;
      constexpr int refills = ks > 3 ? (ks - 3 < 14 - G1_R ? ks - 3 : 14 - G1_R) : 0;
      constexpr int need_after = ks < G1_R ? (2 * (G1_R - 1 - ks) + 4 * refills) : (4 * (G1_R + refills - 1 - ks));
      wait_vm<need_after>();
      bar();
      if constexpr (ks >= 3 && ks - 3 + G1_R < 14) { ldh(IC<ks - 3 + G1_R>{}); ldw(IC<ks - 3 + G1_R>{}); }
    };
    static_for<0, 14>(stepl);
    if (w == 0) wait_flags<1>(ws + OFF_SYNC + SY_W1S, NAUX, tag);
    bar();
    bar();
    bar();
    return;
  }
  const int fi = w >> 1, ti = w & 1;
  f32x4 acc[4][4];
  float s1[2] = {0.f, 0.f}, s2[2] = {0.f, 0.f};
  const bf16x2 ones = __builtin_bit_cast(bf16x2, 0x3f803f80u);
  u32 adB[2][4], adA[2][4], adBh[2][4], adAh[2][4];
#pragma unroll
  for (int t = 0; t < 2; ++t) {
    const int row = 8 * g + 4 * t + q;
    const u32 a0 = lds0 + (u32)(row * 256) + (((u32)(p >> 1) ^ swz(row)) << 4) + 8 * (p & 1);
#pragma unroll
    for (int c = 0; c < 4; ++c) {
      adB[t][c] = a0 ^ (u32)(32 * (4 * ti + ((2 * fi + c) & 3)));
      adA[t][c] = (a0 ^ (u32)(32 * (4 * fi + c))) + 8192;
      adBh[t][c] = adB[t][c] + 65536u; adAh[t][c] = adA[t][c] + 65536u;
    }
  }
  u32x2 B[8][2], A[2][4][2];
  auto rdB = [&](auto nc, auto tc) {
    constexpr int n = decltype(nc)::value, t = decltype(tc)::value, sl = (n >> 2) % G1_R;
    trread<(sl & 3) * G1_STG>(B[n & 7][t], sl < 4 ? adB[t][n & 3] : adBh[t][n & 3]);
  };
  auto rdA = [&](auto kc, auto ic, auto tc) {
    constexpr int ks = decltype(kc)::value, i = decltype(ic)::value, t = decltype(tc)::value, sl = ks % G1_R;
    trread<(sl & 3) * G1_STG>(A[ks & 1][i][t], sl < 4 ? adA[t][i] : adAh[t][i]);
  };
  auto group = [&](auto nc) {
    constexpr int n = decltype(nc)::value;
    constexpr int ks = n >> 2, c = n & 3, sl = n & 7;
    constexpr bool nb = n + 4 < 56, na = (c < 2) && (ks + 1 < 14);
    constexpr int wt = n < 4 ? (n == 0 ? 6 : n == 1 ? 10 : n == 2 ? 14 : 14) : g1_wait(n);
    asm volatile("s_waitcnt lgkmcnt(%10)" : "+v"(B[sl][0]), "+v"(B[sl][1]), "+v"(A[ks & 1][0][0]), "+v"(A[ks & 1][0][1]), "+v"(A[ks & 1][1][0]), "+v"(A[ks & 1][1][1]),
                 "+v"(A[ks & 1][2][0]), "+v"(A[ks & 1][2][1]), "+v"(A[ks & 1][3][0]), "+v"(A[ks & 1][3][1]) : "n"(wt) : "memory");
    const u32x4 bfr = cat(B[sl][0], B[sl][1]);
    mfma_bf16<ks == 0>(acc[0][c], cat(A[ks & 1][0][0], A[ks & 1][0][1]), bfr);
    __builtin_amdgcn_sched_barrier(0);
    if constexpr (c == 0 && ks + 1 < 14) bar();
    if constexpr (nb) rdB(IC<n + 4>{}, IC<0>{});
    __builtin_amdgcn_sched_barrier(0);
    mfma_bf16<ks == 0>(acc[1][c], cat(A[ks & 1][1][0], A[ks & 1][1][1]), bfr);
    __builtin_amdgcn_sched_barrier(0);
    if constexpr (nb) rdB(IC<n + 4>{}, IC<1>{});
    if constexpr (na) rdA(IC<ks + 1>{}, IC<2 * c>{}, IC<0>{});
    __builtin_amdgcn_sched_barrier(0);
    mfma_bf16<ks == 0>(acc[2][c], cat(A[ks & 1][2][0], A[ks & 1][2][1]), bfr);
    __builtin_amdgcn_sched_barrier(0);
    if constexpr (na) { rdA(IC<ks + 1>{}, IC<2 * c>{}, IC<1>{}); rdA(IC<ks + 1>{}, IC<2 * c + 1>{}, IC<0>{}); }
    if constexpr (c < 2) {
#pragma unroll
      for (int k = 0; k < 2; ++k) {
        bf16x2 x2 = as_bf16x2(bfr[k]);
        s1[c] = __builtin_amdgcn_fdot2_f32_bf16(x2, ones, s1[c], false);
        s2[c] = __builtin_amdgcn_fdot2_f32_bf16(x2, x2, s2[c], false);
      }
    }
    __builtin_amdgcn_sched_barrier(0);
    mfma_bf16<ks == 0>(acc[3][c], cat(A[ks & 1][3][0], A[ks & 1][3][1]), bfr);
    __builtin_amdgcn_sched_barrier(0);
    if constexpr (na) rdA(IC<ks + 1>{}, IC<2 * c + 1>{}, IC<1>{});
    if constexpr (c < 2) {
#pragma unroll
      for (int k = 2; k < 4; ++k) {
        bf16x2 x2 = as_bf16x2(bfr[k]);
        s1[c] = __builtin_amdgcn_fdot2_f32_bf16(x2, ones, s1[c], false);
        s2[c] = __builtin_amdgcn_fdot2_f32_bf16(x2, x2, s2[c], false);
      }
      if (ks == fb) {
        const int tt = 4 * ti + ((2 * fi + c) & 3);
        __builtin_amdgcn_raw_buffer_store_b128(bfr, ws_rsrc(ws), (u32)(OFF_HN + ((size_t)(tb * 128 + 16 * tt + c16) * D + 32 * fb + 8 * g) * 2), 0, 16);
      }
    }
    __builtin_amdgcn_sched_barrier(0);
  };
  bar();
  static_for<0, 4>([&](auto ic) { rdA(IC<0>{}, ic, IC<0>{}); rdA(IC<0>{}, ic, IC<1>{}); });
  rdB(IC<0>{}, IC<0>{}); rdB(IC<0>{}, IC<1>{}); rdB(IC<1>{}, IC<0>{}); rdB(IC<1>{}, IC<1>{});
  asm volatile("s_waitcnt lgkmcnt(4)" ::: "memory");
  rdB(IC<2>{}, IC<0>{}); rdB(IC<2>{}, IC<1>{}); rdB(IC<3>{}, IC<0>{}); rdB(IC<3>{}, IC<1>{});
  static_for<0, 56>(group);
  asm volatile("s_nop 7\n\ts_nop 7" ::: "memory");
  TR(ws, 9);
  bar();
  f32x4 wsv[4];
#pragma unroll
  for (int i = 0; i < 4; ++i)
    wsv[i] = __builtin_bit_cast(f32x4, __builtin_amdgcn_raw_buffer_load_b128(ws_rsrc(ws), (u32)(OFF_W1S + (fb * 128 + 64 * fi + 4 * g + 16 * i) * 4), 0, 16));
#pragma unroll
  for (int i = 0; i < 2; ++i) {
    float sa = s1[i], sb = s2[i];
    sa += __shfl_xor(sa, 16); sa += __shfl_xor(sa, 32);
    sb += __shfl_xor(sb, 16); sb += __shfl_xor(sb, 32);
    const float mean = sa * (1.0f / D);
    const float var = fmaxf(sb * (1.0f / D) - mean * mean, 0.f);
    const float mu = rbf(mean);
    const float sd = rbf(__builtin_amdgcn_sqrtf(rbf(rbf(var) + 1e-5f)));
    const int tt = 4 * ti + ((2 * fi + i) & 3);
    if (lane < 16) st[tt * 16 + lane] = f32x2{mu, __builtin_amdgcn_rcpf(sd)};
  }
  lds_bar();
#pragma unroll
  for (int c = 0; c < 4; ++c) {
    const int tt = 4 * ti + ((2 * fi + c) & 3);
    const f32x2 ms = st[tt * 16 + c16];
#pragma unroll
    for (int pp = 0; pp < 2; ++pp) {
      float v0[4], v1[4];
#pragma unroll
      for (int k = 0; k < 4; ++k) {
        v0[k] = fmaxf((acc[2 * pp][c][k] - ms[0] * wsv[2 * pp][k]) * ms[1], 0.f);
        v1[k] = fmaxf((acc[2 * pp + 1][c][k] - ms[0] * wsv[2 * pp + 1][k]) * ms[1], 0.f);
      }
      u32x4 o = {pk_bf16(v0[0], v0[1]), pk_bf16(v0[2], v0[3]), pk_bf16(v1[0], v1[1]), pk_bf16(v1[2], v1[3])};
      __builtin_amdgcn_raw_buffer_store_b128(o, ws_rsrc(ws), (u32)(OFF_H1F + (((size_t)(tb * 8 + tt) * 56 + fb * 4 + 2 * fi + pp) * 64 + lane) * 16), 0, 16);
    }
  }
  asm volatile("s_waitcnt vmcnt(0)" ::: "memory");
  bar();
  if (tid == 0) st_agent(ws + OFF_SYNC + SY_H1 + tb * 128 + fb * 8, tag);
  TR(ws, 10);
}

static constexpr int G2_R = 4;
static constexpr int G2_STG = 32768;
static constexpr int G2_RS = 272;

DI void phase_c(const Args& a, int m, char* smem, u64 tag) {
  const int tid = threadIdx.x, lane = tid & 63;
  const int wv = __builtin_amdgcn_readfirstlane(tid >> 6);
  const int w = wv & 3;
  const int xcd = m & 7, bi = m >> 3;
  const int tb = 2 * xcd + bi / 14, fb = bi % 14;
  const int t64 = 2 * tb + fb / 7, db = fb % 7;
  const u32 lds0 = (u32)(size_t)(LDS3 char*)smem;
  char* ws = a.ws;
  const int c16 = lane & 15, g = lane >> 4, q = (lane & 15) >> 2, p = lane & 3;
  if (wv >= 4) {
    const int rr = lane >> 3;
    u32 vw[4], vo[4];
#pragma unroll
    for (int i = 0; i < 4; ++i) {
      const int row = 8 * i + rr;
      vw[i] = (u32)(row * D * 2) + ((((u32)(lane & 7)) ^ (u32)(((row >> 1) & 3) << 1)) << 4);
      vo[i] = lane * 16 + i * 56 * 1024;
    }
    const char* wb = (const char*)a.w2 + ((size_t)(448 * w) * D + db * 64) * 2;
    const char* hb = ws + OFF_H1F + ((size_t)(t64 * 4) * 56 + 14 * w) * 1024;
    auto ldw = [&](auto kc) {
      constexpr int k = decltype(kc)::value;
      constexpr int sl = (k % G2_R) * G2_STG;
#pragma unroll
      for (int i = 0; i < 4; ++i) glds16<false>(lds0 + sl + w * 8192 + i * 1024, vw[i], wb + (size_t)k * 32 * D * 2);
    };
    auto ldh = [&](auto kc) {
      constexpr int k = decltype(kc)::value;
      constexpr int sl = (k % G2_R) * G2_STG;
#pragma unroll
      for (int i = 0; i < 4; ++i) glds16<true>(lds0 + sl + w * 8192 + 4096 + i * 1024, vo[i], hb + k * 1024);
    };
    static_for<0, G2_R>(ldw);
    loaders_wait<1>(smem, 1, w, ws + OFF_SYNC + SY_H1 + tb * 128, 14, tag);
    TRW(ws, 11, tid == 256);
    static_for<0, G2_R>(ldh);
    auto stepl = [&](auto kc) {
      constexpr int k = decltype(kc)::value;
      constexpr int refills = k > 1 ? (k - 1 < 14 - G2_R ? k - 1 : 14 - G2_R) : 0;
      constexpr int need_after = k < G2_R ? (4 * (G2_R - 1 - k) + 8 * refills) : (8 * (G2_R + refills - 1 - k));
      wait_vm<need_after>();
      bar();
      if constexpr (k >= 1 && k - 1 + G2_R < 14) { ldh(IC<k - 1 + G2_R>{}); ldw(IC<k - 1 + G2_R>{}); }
    };
    static_for<0, 14>(stepl);
    bar();
    bar();
    return;
  }
  u32x4 res[2];
  f32x4 acc[4][4];
  u32 adW[4];
  {
    const int row = 4 * g + q;
    const u32 f = (u32)((row >> 1) & 3);
    const u32 a0 = lds0 + w * 8192 + (u32)(row * 128) + (u32)((p >> 1) * 16 + 8 * (p & 1));
#pragma unroll
    for (int dl = 0; dl < 4; ++dl) adW[dl] = a0 + ((((u32)dl) ^ f) << 5);
  }
  const u32 adH = lds0 + w * 8192 + 4096 + lane * 16;
  u32x2 Wf[2][4][2];
  u32x4 Hf[2][4];
  auto rd = [&](auto kc, auto ic) {
    constexpr int k = decltype(kc)::value, i = decltype(ic)::value;
    constexpr int sl = (k % G2_R) * G2_STG;
    constexpr int off = (sl & 65535);
    constexpr u32 hi = (u32)(sl & ~65535);
    if constexpr (i < 8) trread<off + (i & 1) * 2048>(Wf[k & 1][i >> 1][i & 1], adW[i >> 1] + hi);
    else ldsread128<off + (i - 8) * 1024>(Hf[k & 1][i - 8], adH + hi);
  };
  auto step = [&](auto kc) {
    constexpr int k = decltype(kc)::value;
    asm volatile("s_waitcnt lgkmcnt(0)" : "+v"(Wf[k & 1][0][0]), "+v"(Wf[k & 1][0][1]), "+v"(Wf[k & 1][1][0]), "+v"(Wf[k & 1][1][1]), "+v"(Wf[k & 1][2][0]), "+v"(Wf[k & 1][2][1]),
                 "+v"(Wf[k & 1][3][0]), "+v"(Wf[k & 1][3][1]), "+v"(Hf[k & 1][0]), "+v"(Hf[k & 1][1]), "+v"(Hf[k & 1][2]), "+v"(Hf[k & 1][3]) :: "memory");
    if constexpr (k + 1 < 14) bar();
    static_for<0, 16>([&](auto jc) {
      constexpr int j = decltype(jc)::value, dl = j >> 2, ta = j & 3;
      mfma_bf16<k == 0>(acc[dl][ta], cat(Wf[k & 1][dl][0], Wf[k & 1][dl][1]), Hf[k & 1][ta]);
      __builtin_amdgcn_sched_barrier(0);
      if constexpr (k + 1 < 14 && j < 12) rd(IC<k + 1>{}, IC<j>{});
      __builtin_amdgcn_sched_barrier(0);
    });
  };
  bar();
  static_for<0, 12>([&](auto ic) { rd(IC<0>{}, ic); });
  {
    const rsrc_t rs = ws_rsrc(ws);
#pragma unroll
    for (int i = 0; i < 2; ++i)
      res[i] = __builtin_amdgcn_raw_buffer_load_b128(rs, (u32)(OFF_HN + ((size_t)(t64 * 64 + 32 * i + (tid >> 3)) * D + db * 64 + (tid & 7) * 8) * 2), 0, 16);
  }
  static_for<0, 14>(step);
  asm volatile("s_nop 7\n\ts_nop 7" ::: "memory");
  TR(ws, 12);
  bar();
#pragma unroll
  for (int dl = 0; dl < 4; ++dl)
#pragma unroll
    for (int ta = 0; ta < 4; ++ta)
      *(f32x4*)(smem + (w * 64 + 16 * ta + c16) * G2_RS + (16 * dl + 4 * g) * 4) = acc[dl][ta];
  lds_bar();
#pragma unroll
  for (int i = 0; i < 2; ++i) {
    const int t = (tid >> 3) + 32 * i, chn = tid & 7;
    const u32x4 rr = res[i];
    f32x4 s0 = {0.f, 0.f, 0.f, 0.f}, s1 = s0;
#pragma unroll
    for (int ww = 0; ww < 4; ++ww) {
      s0 += *(const f32x4*)(smem + (ww * 64 + t) * G2_RS + chn * 32);
      s1 += *(const f32x4*)(smem + (ww * 64 + t) * G2_RS + chn * 32 + 16);
    }
    u32 p0 = pk_bf16(s0[0], s0[1]), p1 = pk_bf16(s0[2], s0[3]), p2 = pk_bf16(s1[0], s1[1]), p3 = pk_bf16(s1[2], s1[3]);
    u32x4 o;
    o[0] = pk_bf16(bflo(rr[0]) + bflo(p0), bfhi(rr[0]) + bfhi(p0));
    o[1] = pk_bf16(bflo(rr[1]) + bflo(p1), bfhi(rr[1]) + bfhi(p1));
    o[2] = pk_bf16(bflo(rr[2]) + bflo(p2), bfhi(rr[2]) + bfhi(p2));
    o[3] = pk_bf16(bflo(rr[3]) + bflo(p3), bfhi(rr[3]) + bfhi(p3));
    *(u32x4*)(a.out + (size_t)(t64 * 64 + t) * D + db * 64 + chn * 8) = o;
  }
}

__global__ __launch_bounds__(512, 2) void s4_block_kernel(Args a) {
  extern __shared__ __attribute__((aligned(16))) char smem[];
  const int b = blockIdx.x;
  TR(a.ws, 0);
  if (b < NAUX) { aux_block(a, b, smem); TR(a.ws, 15); TRDUMP(a.ws); return; }
  const int m = b - NAUX;
  const u64 tag = phase_a(a, m, smem);
  phase_b(a, m, smem, tag);
  __syncthreads();
  phase_c(a, m, smem, tag);
  TR(a.ws, 15);
  TRDUMP(a.ws);
}

extern "C" void kernel_launch(void* const* d_in, const int* in_sizes, int n_in,
                              void* d_out, int out_size, void* d_ws, size_t ws_size,
                              hipStream_t stream) {
  Args a;
  a.x = (const u16*)d_in[0]; a.Are = (const u16*)d_in[1]; a.Aim = (const u16*)d_in[2];
  a.Bre = (const u16*)d_in[3]; a.Bim = (const u16*)d_in[4]; a.Cre = (const u16*)d_in[5];
  a.Cim = (const u16*)d_in[6]; a.logdt = (const u16*)d_in[7]; a.Dskip = (const u16*)d_in[8];
  a.w1 = (const u16*)d_in[10]; a.w2 = (const u16*)d_in[11]; a.ws = (char*)d_ws; a.out = (u16*)d_out; a.pad = nullptr;
  (void)hipFuncSetAttribute((const void*)s4_block_kernel, hipFuncAttributeMaxDynamicSharedMemorySize, SMEM_BYTES);
  s4_block_kernel<<<NAUX + NMAIN, 512, SMEM_BYTES, stream>>>(a);
}
```

```cpp
#define TRACE 1
#include <hip/hip_runtime.h>
#include <hip/hip_bf16.h>
#include <stdint.h>
#include <type_traits>

static constexpr int L = 2048, D = 448, NM = 256, F = 1792;
static constexpr int LP = 2176;
static constexpr size_t OFF_ST = 0;
static constexpr size_t OFF_XT = 8192;
static constexpr size_t OFF_HT = OFF_XT + (size_t)D * LP * 2;
static constexpr size_t OFF_W1S = OFF_HT + (size_t)D * LP * 2;
static constexpr size_t OFF_HN = OFF_W1S + 8192;
static constexpr size_t OFF_H1F = OFF_HN + (size_t)L * D * 2;
static constexpr size_t OFF_SYNC = OFF_H1F + (size_t)L * F * 2;
static constexpr size_t SY_EP = 0, SY_LN = 1024  , SY_W1S = 4096, SY_CV = 8192  , SY_H1 = 32768  ;
static constexpr size_t WS_USED = OFF_SYNC + 65536;

typedef unsigned short u16;
typedef unsigned int u32;
typedef unsigned long long u64;
typedef _Float16 f16;
typedef f16 f16x2 __attribute__((ext_vector_type(2)));
typedef f16 f16x8 __attribute__((ext_vector_type(8)));
typedef __bf16 bf16x2 __attribute__((ext_vector_type(2)));
typedef __bf16 bf16x8 __attribute__((ext_vector_type(8)));
typedef float f32x2 __attribute__((ext_vector_type(2)));
typedef float f32x4 __attribute__((ext_vector_type(4)));
typedef u32 u32x2 __attribute__((ext_vector_type(2)));
typedef u32 u32x4 __attribute__((ext_vector_type(4)));
typedef short s16x4 __attribute__((ext_vector_type(4)));
#define LDS3 __attribute__((address_space(3)))
typedef __attribute__((address_space(1))) u64 gu64;
typedef __amdgpu_buffer_rsrc_t rsrc_t;

#define DI __device__ __forceinline__
DI float bf2f(u16 v) { return __uint_as_float(((u32)v) << 16); }
DI float bflo(u32 v) { return __uint_as_float(v << 16); }
DI float bfhi(u32 v) { return __uint_as_float(v & 0xffff0000u); }
DI u32 pk_bf16(float a, float b) { f32x2 v = {a, b}; return __builtin_bit_cast(u32, __builtin_convertvector(v, bf16x2)); }
DI u32 pk_f16(float a, float b) { f32x2 v = {a, b}; return __builtin_bit_cast(u32, __builtin_convertvector(v, f16x2)); }
DI float rbf(float x) { return bflo(pk_bf16(x, 0.f)); }
DI f16x2 as_f16x2(u32 v) { return __builtin_bit_cast(f16x2, v); }
DI bf16x2 as_bf16x2(u32 v) { return __builtin_bit_cast(bf16x2, v); }
template <int CTRL, int RM> DI float dppf(float old, float v) {
  return __int_as_float(__builtin_amdgcn_update_dpp(__float_as_int(old), __float_as_int(v), CTRL, RM, 0xf, false));
}
DI float wave_sum(float v) {
  v += dppf<0x111, 0xf>(0.f, v); v += dppf<0x112, 0xf>(0.f, v); v += dppf<0x114, 0xf>(0.f, v); v += dppf<0x118, 0xf>(0.f, v);
  v += dppf<0x142, 0xa>(0.f, v); v += dppf<0x143, 0xc>(0.f, v);
  return __int_as_float(__builtin_amdgcn_readlane(__float_as_int(v), 63));
}
DI float wave_max(float v) {
  v = fmaxf(v, dppf<0x111, 0xf>(v, v)); v = fmaxf(v, dppf<0x112, 0xf>(v, v)); v = fmaxf(v, dppf<0x114, 0xf>(v, v)); v = fmaxf(v, dppf<0x118, 0xf>(v, v));
  v = fmaxf(v, dppf<0x142, 0xa>(v, v)); v = fmaxf(v, dppf<0x143, 0xc>(v, v));
  return __int_as_float(__builtin_amdgcn_readlane(__float_as_int(v), 63));
}
DI u64 ld_agent(const void* p) { return __hip_atomic_load((const gu64*)p, __ATOMIC_RELAXED, __HIP_MEMORY_SCOPE_AGENT); }
DI void st_agent(void* p, u64 v) { __hip_atomic_store((gu64*)p, v, __ATOMIC_RELAXED, __HIP_MEMORY_SCOPE_AGENT); }
static constexpr u64 TAG_INC = 0x9E3779B97F4A7C15ull;
DI rsrc_t ws_rsrc(char* ws) { return __builtin_amdgcn_make_buffer_rsrc(ws, 0, (int)WS_USED, 0x00020000); }
template <int B, int E, class Fn> DI void static_for(Fn&& f) {
  if constexpr (B < E) { f(std::integral_constant<int, B>{}); static_for<B + 1, E>(f); }
}
template <int N> using IC = std::integral_constant<int, N>;
DI s16x4 tr_read(const void* p) { return __builtin_amdgcn_ds_read_tr16_b64_v4i16((LDS3 s16x4*)p); }
template <int N> DI void wait_vm() { asm volatile("s_waitcnt vmcnt(%0)" :: "n"(N) : "memory"); }
DI void bar() { __builtin_amdgcn_s_barrier(); }
DI void lds_bar() { asm volatile("s_waitcnt lgkmcnt(0)" ::: "memory"); __builtin_amdgcn_s_barrier(); }
template <bool SC1> DI void glds16(u32 lds_dst, u32 voff, const void* sbase) {
  u32 keep;
  if constexpr (SC1)
    asm volatile("s_mov_b32 %0, m0\n\ts_mov_b32 m0, %1\n\ts_nop 0\n\tglobal_load_lds_dwordx4 %2, %3 sc1\n\ts_mov_b32 m0, %0"
                 : "=&s"(keep) : "s"(lds_dst), "v"(voff), "s"(sbase) : "memory");
  else
    asm volatile("s_mov_b32 %0, m0\n\ts_mov_b32 m0, %1\n\ts_nop 0\n\tglobal_load_lds_dwordx4 %2, %3\n\ts_mov_b32 m0, %0"
                 : "=&s"(keep) : "s"(lds_dst), "v"(voff), "s"(sbase) : "memory");
}
template <int OFF> DI void trread(u32x2& dst, u32 addr) {
  asm volatile("ds_read_b64_tr_b16 %0, %1 offset:%2" : "=v"(dst) : "v"(addr), "n"(OFF) : "memory");
}
template <int OFF> DI void ldsread128(u32x4& dst, u32 addr) {
  asm volatile("ds_read_b128 %0, %1 offset:%2" : "=v"(dst) : "v"(addr), "n"(OFF) : "memory");
}
template <bool FIRST> DI void mfma_bf16(f32x4& acc, const u32x4& a, const u32x4& b) {
  if constexpr (FIRST) asm volatile("s_nop 1\n\tv_mfma_f32_16x16x32_bf16 %0, %1, %2, 0" : "=v"(acc) : "v"(a), "v"(b));
  else asm volatile("s_nop 1\n\tv_mfma_f32_16x16x32_bf16 %0, %1, %2, %0" : "+v"(acc) : "v"(a), "v"(b));
}
DI u32x4 cat(const u32x2& a, const u32x2& b) { return u32x4{a[0], a[1], b[0], b[1]}; }
DI u32 swz(int row) { return (u32)(((row & 3) << 2) | ((row >> 2) & 3)); }
DI f32x2 cmul(f32x2 y, f32x2 e) {
  f32x2 t, r;
  asm("v_pk_mul_f32 %0, %1, %2 op_sel_hi:[1,0]" : "=v"(t) : "v"(y), "v"(e));
  asm("v_pk_fma_f32 %0, %1, %2, %3 op_sel:[1,1,0] op_sel_hi:[0,1,1] neg_lo:[0,1,0]" : "=v"(r) : "v"(y), "v"(e), "v"(t));
  return r;
}
DI f32x2 cmul_conj(f32x2 p, f32x2 q) {
  f32x2 t, r;
  asm("v_pk_mul_f32 %0, %1, %2 op_sel_hi:[1,0] neg_hi:[1,0]" : "=v"(t) : "v"(p), "v"(q));
  asm("v_pk_fma_f32 %0, %1, %2, %3 op_sel:[1,1,0] op_sel_hi:[0,1,1] neg_lo:[1,0,0] neg_hi:[1,0,0]" : "=v"(r) : "v"(p), "v"(q), "v"(t));
  return r;
}

static constexpr size_t OFF_TR = (size_t)128 << 20;
extern __shared__ __attribute__((aligned(16))) char smem_tr[];
#define TRW(ws, slot, cond) do { if (cond) { unsigned long long _t = __builtin_amdgcn_s_memrealtime(); *(volatile unsigned long long*)(smem_tr + 160 * 1024 - 256 + (slot) * 8) = _t; } } while (0)
#define TR(ws, slot) TRW(ws, slot, threadIdx.x == 0)
#define TRDUMP(ws) do { __syncthreads(); if (threadIdx.x == 0 && (blockIdx.x == 3 || blockIdx.x == 40 || blockIdx.x == 200)) { \
  volatile unsigned long long* q_ = (volatile unsigned long long*)(smem_tr + 160 * 1024 - 256); unsigned long long z_ = q_[0]; \
  printf("TRC b%d t0=%llu : %d %d %d %d %d %d %d | %d %d %d %d %d | %d\n", (int)blockIdx.x, z_ % 100000000ull, (int)(q_[1]-z_), (int)(q_[2]-z_), (int)(q_[3]-z_), (int)(q_[4]-z_), (int)(q_[5]-z_), (int)(q_[6]-z_), (int)(q_[7]-z_), (int)(q_[8]-z_), (int)(q_[9]-z_), (int)(q_[10]-z_), (int)(q_[11]-z_), (int)(q_[12]-z_), (int)(q_[15]-z_)); } } while (0)

struct Args {
  const u16 *x, *Are, *Aim, *Bre, *Bim, *Cre, *Cim, *logdt, *Dskip, *w1, *w2;
  char* ws;
  u16* out;
  const void* pad;
};

constexpr int rne_bf16_int(int l) {
  if (l < 256) return l;
  int sh = 0; while ((l >> sh) >= 256) ++sh;
  const int q = l >> sh, rem = l & ((1 << sh) - 1), half = 1 << (sh - 1);
  const int up = (rem > half) || (rem == half && (q & 1));
  return (q + up) << sh;
}
constexpr int kslot(int m) {
  const int l = 2047 - m;
  if (l < 0) return 1025;
  const int t = rne_bf16_int(l);
  if (t >= 2048) return 1024;
  const int seg = (t >= 256) + (t >= 512) + (t >= 1024);
  const int row = seg == 0 ? (t & 7) : seg == 1 ? 8 + ((t >> 1) & 3) : seg == 2 ? 12 + ((t >> 2) & 1) : 14 + ((t >> 9) & 1);
  const int b = (t >> 3) & (seg >= 2 ? 63 : 31);
  return b * 16 + row;
}
struct SlotTab { u16 v[2064]; };
constexpr SlotTab make_slot_tab() { SlotTab t{}; for (int m = 0; m < 2064; ++m) t.v[m] = (u16)(kslot(m) * 4); return t; }
__device__ __attribute__((aligned(16))) const SlotTab g_slot_tab = make_slot_tab();

static constexpr int NAUX = 32;
static constexpr int NMAIN = 224;
static constexpr int SMEM_BYTES = 160 * 1024;
static constexpr unsigned SPIN_CAP = 1u << 20;
static constexpr int LDS_SIG = SMEM_BYTES - 272;

template <int NPL> DI void wait_flags(const char* base, int n, u64 tag) {
  const int lane = threadIdx.x & 63;
  unsigned spins = 0;
  for (;;) {
    bool ok = true;
#pragma unroll
    for (int k = 0; k < NPL; ++k) { const int i = k * 64 + lane; if (i < n) ok &= (ld_agent(base + i * 8) == tag); }
    if (__all(ok)) break;
    __builtin_amdgcn_s_sleep(1);
    if (++spins > SPIN_CAP) break;
  }
}

template <int NPL> DI void loaders_wait(char* smem, int idx, int w, const char* base, int n, u64 tag) {
  volatile u32* sig = (volatile u32*)(smem + LDS_SIG) + idx;
  if (w == 0) {
    wait_flags<NPL>(base, n, tag);
    *sig = 1u;
  } else {
    unsigned spins = 0;
    while (*sig == 0u) { __builtin_amdgcn_s_sleep(1); if (++spins > (SPIN_CAP << 4)) break; }
  }
  asm volatile("" ::: "memory");
}

static constexpr int LNRS = 912;
DI void aux_block(const Args& a, int blk, char* smem) {
  const int tid = threadIdx.x, lane = tid & 63;
  const int w = __builtin_amdgcn_readfirstlane(tid >> 6);
  const int t0 = blk * 64;
  __builtin_amdgcn_s_setprio(2);
  u32x4 v[8];
#pragma unroll
  for (int i = 0; i < 8; ++i) {
    v[i] = u32x4{0, 0, 0, 0};
    if (lane < 56) v[i] = *(const u32x4*)(a.x + (size_t)(t0 + 8 * w + i) * D + lane * 8);
  }
#pragma unroll
  for (int i = 0; i < 8; ++i)
    if (lane < 56) *(u32x4*)(smem + (8 * w + i) * LNRS + lane * 16) = v[i];
  TR(a.ws, 1);
  __syncthreads();
  TR(a.ws, 5);
  const rsrc_t rs = ws_rsrc(a.ws);
  const int g = lane >> 4, i16 = lane & 15, q = i16 >> 2, p = lane & 3;
  if (w >= 4) {
    const char* src0 = smem + (16 * g + 8 * (p >> 1) + q) * LNRS + (4 * (p & 1)) * 2;
    const u32 off0 = (u32)(OFF_XT + (size_t)(i16 & 7) * LP * 2 + (t0 + 16 * g + 8 * (i16 >> 3)) * 2);
#pragma unroll
    for (int h2 = 0; h2 < 2; ++h2) {
      s16x4 lo[7], hi[7];
#pragma unroll
      for (int k = 0; k < 7; ++k) {
        const int cg = (w - 4) + 4 * (k + 7 * h2);
        lo[k] = tr_read(src0 + cg * 16); hi[k] = tr_read(src0 + cg * 16 + 4 * LNRS);
      }
      __builtin_amdgcn_sched_barrier(0);
#pragma unroll
      for (int k = 0; k < 7; ++k) {
        const int cg = (w - 4) + 4 * (k + 7 * h2);
        __builtin_amdgcn_raw_buffer_store_b128(cat(__builtin_bit_cast(u32x2, lo[k]), __builtin_bit_cast(u32x2, hi[k])), rs, off0 + (u32)(cg * 8 * LP * 2), 0, 16);
      }
      __builtin_amdgcn_sched_barrier(0);
    }
  }
  TR(a.ws, 4);
  if (w < 4) {
    f32x4 a1 = {0.f, 0.f, 0.f, 0.f}, a2 = a1;
    const u32x4 one4 = {0x3f803f80u, 0x3f803f80u, 0x3f803f80u, 0x3f803f80u};
    bf16x8 fr[14];
#pragma unroll
    for (int ks = 0; ks < 14; ++ks) fr[ks] = *(const bf16x8*)(smem + (16 * w + i16) * LNRS + (32 * ks + 8 * g) * 2);
    __builtin_amdgcn_sched_barrier(0);
#pragma unroll
    for (int ks = 0; ks < 14; ++ks) {
      a1 = __builtin_amdgcn_mfma_f32_16x16x32_bf16(fr[ks], __builtin_bit_cast(bf16x8, one4), a1, 0, 0, 0);
      a2 = __builtin_amdgcn_mfma_f32_16x16x32_bf16(fr[ks], fr[ks], a2, 0, 0, 0);
    }
    const float s = p == 0 ? a1[0] : p == 1 ? a1[1] : p == 2 ? a1[2] : a1[3];
    const float q2 = p == 0 ? a2[0] : p == 1 ? a2[1] : p == 2 ? a2[2] : a2[3];
    const float mean = s * (1.0f / D);
    const float var = fmaxf(q2 * (1.0f / D) - mean * mean, 0.f);
    const float sd = rbf(__builtin_amdgcn_sqrtf(rbf(rbf(var) + 1e-5f)));
    if (g == q) __builtin_amdgcn_raw_buffer_store_b32(pk_bf16(mean, sd), rs, (u32)(OFF_ST + (t0 + 16 * w + i16) * 4), 0, 16);
  }
  const u64 ep = ld_agent(w == 0 ? (const char*)(a.ws + OFF_SYNC + SY_EP) : (const char*)(a.x + (size_t)(t0 + w) * D));
  TR(a.ws, 2);
  asm volatile("s_waitcnt vmcnt(0)" ::: "memory");
  const u64 tag = ep + TAG_INC;
  __syncthreads();
  if (tid < 8) st_agent(a.ws + OFF_SYNC + SY_LN + tid * 256 + blk * 8, tag);
  __builtin_amdgcn_s_setprio(0);
  TR(a.ws, 3);
  {
    float* red = (float*)smem;
    const int cp = tid % 28, rg = tid / 28;
    float s0 = 0.f, s1 = 0.f;
    if (rg < 16) {
      u32 x[28];
#pragma unroll
      for (int i = 0; i < 28; ++i) x[i] = *(const u32*)(a.w1 + (size_t)(rg + 16 * i) * F + 56 * blk + 2 * cp);
#pragma unroll
      for (int i = 0; i < 28; ++i) { s0 += bflo(x[i]); s1 += bfhi(x[i]); }
      red[rg * 56 + 2 * cp] = s0; red[rg * 56 + 2 * cp + 1] = s1;
    }
    __syncthreads();
    if (tid < 56) {
      float s = 0.f;
#pragma unroll
      for (int i = 0; i < 16; ++i) s += red[i * 56 + tid];
      __builtin_amdgcn_raw_buffer_store_b32(__float_as_uint(s), rs, (u32)(OFF_W1S + (56 * blk + tid) * 4), 0, 16);
    }
    asm volatile("s_waitcnt vmcnt(0)" ::: "memory");
    __syncthreads();
    if (tid == 0) st_agent(a.ws + OFF_SYNC + SY_W1S + blk * 8, tag);
  }
  if (blk == 0 && w == 0) {
    wait_flags<1>(a.ws + OFF_SYNC + SY_LN, NAUX, tag);
    wait_flags<4>(a.ws + OFF_SYNC + SY_CV, NMAIN, tag);
    if (lane == 0) st_agent(a.ws + OFF_SYNC + SY_EP, tag);
  }
}

static constexpr int CPS = 4160;
static constexpr int CH_BYTES = 61440;
static constexpr int CH_TAB = 0;
static constexpr int CH_SU = 8 * CPS;
static constexpr int CH_F = CH_SU + 4608 + 256;
static constexpr int CH_SX = 47104;
static constexpr int CH_ST = CH_SX + 4096;
static constexpr int CH_RED = 59392;

DI u64 phase_a(const Args& a, int m, char* smem_all) {
  const int tid = threadIdx.x & 255, lane = threadIdx.x & 63;
  const int wv = __builtin_amdgcn_readfirstlane(threadIdx.x >> 6);
  const int w = wv & 3, hw = wv >> 2;
  const int d = 2 * m + hw;
  char* smem = smem_all + hw * CH_BYTES;
  const float LOG2E = 1.4426950408889634f, INV2PI = 0.15915494309189535f;
  const u32 lds0 = (u32)(size_t)(LDS3 char*)smem;
  const int n = tid;
  const u16 r_are = a.Are[d * NM + n], r_aim = a.Aim[d * NM + n], r_bre = a.Bre[d * NM + n], r_bim = a.Bim[d * NM + n];
  const u16 r_cre = a.Cre[d * NM + n], r_cim = a.Cim[d * NM + n], r_ldt = a.logdt[d], r_dsk = a.Dskip[d];
  u32 slot2[8];
  {
    const u32x4 s0 = *(const u32x4*)(g_slot_tab.v + 8 * tid), s1 = *(const u32x4*)(g_slot_tab.v + 8 * tid + 8);
#pragma unroll
    for (int k = 0; k < 4; ++k) { slot2[k] = s0[k]; slot2[4 + k] = s1[k]; }
  }
  const u64 ep = ld_agent(wv == 0 ? (const char*)(a.ws + OFF_SYNC + SY_EP) : (const char*)(a.Are + d * NM + 4 * w));
  float* red = (float*)(smem + CH_RED);
  const float dt = rbf(expf(bf2f(r_ldt)));
  const float bre = dt * bf2f(r_bre), bim = dt * bf2f(r_bim);
  const float cre = bf2f(r_cre), cim = bf2f(r_cim);
  const float zr = (dt * bf2f(r_are)) * LOG2E, zi = (dt * bf2f(r_aim)) * INV2PI;
  const float cr = cre * bre - cim * bim, ci = cre * bim + cim * bre;
  const float mx = fmaxf(fabsf(cr), fabsf(ci));
  const float lc = mx > 1e-36f ? __builtin_amdgcn_logf(mx) + 0.5f : -1000.f;
  const float sY = fmaxf(0.f, zr * 504.f);
  const float gm = fmaxf(lc + fmaxf(0.f, zr * 1536.f) + sY, lc + zr * 2048.f);
  const float G = ceilf(wave_max(gm));
  auto cexp = [&](float t, float lg) -> f32x2 {
    const float mm = __builtin_amdgcn_exp2f(__builtin_fmaf(zr, t, lg)), f = __builtin_amdgcn_fractf(zi * t);
    return f32x2{mm * __builtin_amdgcn_cosf(f), mm * __builtin_amdgcn_sinf(f)};
  };
  {
    f32x2* tb = (f32x2*)(smem + CH_TAB + n * 224);
    const f32x2 e1 = cexp(1.f, 0.f), e2 = cmul(e1, e1), e3 = cmul(e2, e1), e4 = cexp(4.f, 0.f), e6 = cmul(e3, e3);
    const f32x2 e8 = cexp(8.f, 0.f), e16 = cmul(e8, e8), e24 = cmul(e16, e8);
    const f32x2 e32 = cexp(32.f, 0.f), e64 = cmul(e32, e32), e96 = cmul(e64, e32);
    const f32x2 e128 = cexp(128.f, 0.f), e256 = cexp(256.f, 0.f), e384 = cmul(e256, e128);
    const f32x2 e512 = cexp(512.f, 0.f), e1024 = cexp(1024.f, 0.f);
    const float sy2 = __builtin_amdgcn_exp2f(-sY);
    const float cs = __builtin_amdgcn_exp2f(sY - G + 14.f);
    const f32x2 c0 = {cr * cs, ci * cs};
    *(f32x4*)(tb + 0) = f32x4{1.f, 0.f, e8[0], e8[1]};      *(f32x4*)(tb + 2) = f32x4{e16[0], e16[1], e24[0], e24[1]};
    *(f32x4*)(tb + 4) = f32x4{sy2, 0.f, e32[0] * sy2, e32[1] * sy2}; *(f32x4*)(tb + 6) = f32x4{e64[0] * sy2, e64[1] * sy2, e96[0] * sy2, e96[1] * sy2};
    const f32x2 p1 = cmul(c0, e4), p2 = cmul(c0, e256), p3 = cmul(c0, e512);
    *(f32x4*)(tb + 8) = f32x4{c0[0], c0[1], p1[0], p1[1]};  *(f32x4*)(tb + 10) = f32x4{p2[0], p2[1], p3[0], p3[1]};
    *(f32x4*)(tb + 12) = f32x4{1.f, 0.f, e1[0], e1[1]};     *(f32x4*)(tb + 14) = f32x4{e2[0], e2[1], e3[0], e3[1]};
    *(f32x4*)(tb + 16) = f32x4{1.f, 0.f, e2[0], e2[1]};     *(f32x4*)(tb + 18) = f32x4{e4[0], e4[1], e6[0], e6[1]};
    *(f32x4*)(tb + 20) = f32x4{1.f, 0.f, e4[0], e4[1]};     *(f32x4*)(tb + 22) = f32x4{e512[0], e512[1], e1024[0], e1024[1]};
    *(f32x4*)(tb + 24) = f32x4{e128[0], e128[1], e256[0], e256[1]}; *(f32x4*)(tb + 26) = f32x4{e384[0], e384[1], 0.f, 0.f};
    const f32x2 e2048 = cexp(2048.f, 14.f - G);
    float t2048 = cr * e2048[0] - ci * e2048[1];
    t2048 = wave_sum(t2048);
    if (lane == 0) { red[w] = G; red[4 + w] = t2048; }
  }
  __builtin_amdgcn_fence(__ATOMIC_RELEASE, "wavefront");
  __builtin_amdgcn_wave_barrier();
  TR(a.ws, 1);
  const u64 tag = ep + TAG_INC;
  const char* flg = a.ws + OFF_SYNC + SY_LN + (m & 7) * 256 + (lane & 31) * 8;
  u64 fl = 0;
  bool have = (wv != 0);
  const int c = lane & 15, g = lane >> 4;
  auto load_x = [&]() {
#pragma unroll
    for (int i = 0; i < 8; ++i) glds16<true>(lds0 + CH_ST + i * 1024, lane * 16 + i * 1024, a.ws + OFF_ST);
#pragma unroll
    for (int i = 0; i < 4; ++i) {
      glds16<true>(lds0 + CH_SX + i * 1024, lane * 16 + i * 1024, a.ws + OFF_XT + (size_t)(2 * m) * LP * 2);
      glds16<true>(lds0 + CH_BYTES + CH_SX + i * 1024, lane * 16 + i * 1024, a.ws + OFF_XT + (size_t)(2 * m + 1) * LP * 2);
    }
  };
  const int r = c;
  f32x4 acc[4];
#pragma unroll
  for (int k = 0; k < 4; ++k) acc[k] = f32x4{0.f, 0.f, 0.f, 0.f};
  {
    const char* tb = smem + CH_TAB + (64 * w + 4 * g) * 224;
    const char* pA = tb + (c & 3) * 8;
    const char* pB = tb + 32 + (c >> 2) * 8;
    const char* pP = tb + 64 + (r >> 2) * 8;
    const char* pQ = tb + 96 + ((r < 8 ? 0 : r < 12 ? 4 : 8) + (r & 3)) * 8;
    const char* pE = tb + 192;
    f32x2 tA[2][4], tB[2][4], tP[2][4], tQ[2][4], tE3[2][4];
    f32x4 tE[2][4];
    auto ldtab = [&](auto sc_) {
      constexpr int s = decltype(sc_)::value;
#pragma unroll
      for (int i = 0; i < 4; ++i) {
        const int o = (16 * s + i) * 224;
        tA[s & 1][i] = *(const f32x2*)(pA + o); tB[s & 1][i] = *(const f32x2*)(pB + o);
        tP[s & 1][i] = *(const f32x2*)(pP + o); tQ[s & 1][i] = *(const f32x2*)(pQ + o);
        tE[s & 1][i] = *(const f32x4*)(pE + o); tE3[s & 1][i] = *(const f32x2*)(pE + o + 16);
      }
    };
    auto kstep = [&](auto sc_) {
      constexpr int s = decltype(sc_)::value;
      if constexpr (s < 3) ldtab(IC<s + 1>{});
      __builtin_amdgcn_sched_barrier(0);
      u32x4 xf, y0, y1, y2, y3;
#pragma unroll
      for (int i = 0; i < 4; ++i) {
        const f32x2 yv = cmul(tA[s & 1][i], tB[s & 1][i]);
        const f32x2 xv = cmul_conj(tP[s & 1][i], tQ[s & 1][i]);
        y0[i] = pk_f16(yv[0], yv[1]);
        xf[i] = pk_f16(xv[0], xv[1]);
        const f32x4 E12 = tE[s & 1][i];
        const f32x2 v1 = cmul(yv, f32x2{E12[0], E12[1]}), v2 = cmul(yv, f32x2{E12[2], E12[3]}), v3 = cmul(yv, tE3[s & 1][i]);
        y1[i] = pk_f16(v1[0], v1[1]); y2[i] = pk_f16(v2[0], v2[1]); y3[i] = pk_f16(v3[0], v3[1]);
      }
      f16x8 xa = __builtin_bit_cast(f16x8, xf);
      __builtin_amdgcn_sched_barrier(0);
      acc[0] = __builtin_amdgcn_mfma_f32_16x16x32_f16(xa, __builtin_bit_cast(f16x8, y0), acc[0], 0, 0, 0);
      acc[1] = __builtin_amdgcn_mfma_f32_16x16x32_f16(xa, __builtin_bit_cast(f16x8, y1), acc[1], 0, 0, 0);
      acc[2] = __builtin_amdgcn_mfma_f32_16x16x32_f16(xa, __builtin_bit_cast(f16x8, y2), acc[2], 0, 0, 0);
      acc[3] = __builtin_amdgcn_mfma_f32_16x16x32_f16(xa, __builtin_bit_cast(f16x8, y3), acc[3], 0, 0, 0);
      __builtin_amdgcn_sched_barrier(0);
    };
    ldtab(IC<0>{});
    static_for<0, 4>(kstep);
  }
  if (wv == 0) fl = ld_agent(flg);
  TR(a.ws, 2);
  float* part = (float*)(smem + CH_TAB);
#pragma unroll
  for (int k = 0; k < 4; ++k) *(f32x4*)(part + w * 3584 + (16 * k + r) * 16 + 4 * g) = acc[k];
  __syncthreads();
  float* Fv = (float*)(smem + CH_F);
  const float Gm = fmaxf(fmaxf(red[0], red[1]), fmaxf(red[2], red[3]));
  float sc[4];
#pragma unroll
  for (int k = 0; k < 4; ++k) sc[k] = __builtin_amdgcn_exp2f(red[k] - Gm - 8.f);
  {
    const f32x4* pp = (const f32x4*)part;
    f32x4 sv = pp[tid] * sc[0] + pp[896 + tid] * sc[1] + pp[1792 + tid] * sc[2] + pp[2688 + tid] * sc[3];
    *(f32x4*)(Fv + tid * 4) = sv;
    if (tid == 0) { Fv[1024] = red[4] * sc[0] + red[5] * sc[1] + red[6] * sc[2] + red[7] * sc[3]; Fv[1025] = 0.f; }
  }
  const float kinv = __builtin_amdgcn_exp2f(Gm - 6.f);
  if (wv == 0) {
    if (__all(fl == tag)) { have = true; load_x(); }
    else fl = ld_agent(flg);
  }
  __syncthreads();
  TR(a.ws, 3);
  {
    u32 e[8];
#pragma unroll
    for (int k = 0; k < 8; ++k)
      e[k] = pk_f16(*(const float*)((const char*)Fv + (slot2[k] & 0xffffu)), *(const float*)((const char*)Fv + (slot2[k] >> 16)));
    auto put = [&](int ch, const u32 (&ee)[8]) {
      u32 o[7];
#pragma unroll
      for (int i = 0; i < 7; ++i) o[i] = __builtin_amdgcn_alignbyte(ee[i + 1], ee[i], 2);
#pragma unroll
      for (int s = 0; s < 4; ++s) {
        *(u32x4*)(smem + (2 * s) * CPS + ch * 16) = u32x4{ee[s], ee[s + 1], ee[s + 2], ee[s + 3]};
        *(u32x4*)(smem + (2 * s + 1) * CPS + ch * 16) = u32x4{o[s], o[s + 1], o[s + 2], o[s + 3]};
      }
    };
    put(tid, e);
    if (tid == 255) { const u32 z[8] = {0, 0, 0, 0, 0, 0, 0, 0}; put(256, z); }
  }
  u16* su = (u16*)(smem + CH_SU);
  const u16* sx = (const u16*)(smem + CH_SX);
  if (wv == 0) {
    if (!have) {
      unsigned spins = 0;
      while (!__all(fl == tag)) { __builtin_amdgcn_s_sleep(2); fl = ld_agent(flg); if (++spins > SPIN_CAP) break; }
      load_x();
    }
    asm volatile("s_waitcnt vmcnt(0)" ::: "memory");
  }
  __syncthreads();
  TR(a.ws, 4);
  {
    const u32x4 xr = *(const u32x4*)(sx + tid * 8);
    const u32x4 sa = *(const u32x4*)(smem_all + CH_ST + tid * 32), sb = *(const u32x4*)(smem_all + CH_ST + tid * 32 + 16);
    u32x4 o;
#pragma unroll
    for (int k = 0; k < 4; ++k) {
      const u32 s0 = k < 2 ? sa[2 * k] : sb[2 * k - 4], s1 = k < 2 ? sa[2 * k + 1] : sb[2 * k - 3];
      const u32 dpk = pk_bf16(bflo(xr[k]) - bflo(s0), bfhi(xr[k]) - bflo(s1));
      const u32 upk = pk_bf16(bflo(dpk) * __builtin_amdgcn_rcpf(bfhi(s0)), bfhi(dpk) * __builtin_amdgcn_rcpf(bfhi(s1)));
      o[k] = pk_f16(bflo(upk), bfhi(upk));
    }
    *(u32x4*)(su + 256 + tid * 8) = o;
    if (tid < 32) *(u32x4*)(su + tid * 8) = u32x4{0, 0, 0, 0};
  }
  __syncthreads();
  TR(a.ws, 5);
  f32x4 cv[8];
#pragma unroll
  for (int q = 0; q < 8; ++q) cv[q] = f32x4{0.f, 0.f, 0.f, 0.f};
  const int sig = (7 - c) & 7;
  const char* abase = smem + sig * CPS + 16 * ((c < 8 ? 253 : 252) + g);
#pragma unroll
  for (int rr2 = 0; rr2 < 2; ++rr2) {
    const int rs2 = 2 * w + rr2;
    f16x8 B[8];
#pragma unroll
    for (int q = 0; q < 8; ++q) B[q] = *(const f16x8*)(su + 240 + 256 * q - 32 * rs2 + 16 * c + 8 * g);
    f16x8 A[8];
#pragma unroll
    for (int k = 0; k < 8; ++k) A[k] = *(const f16x8*)(abase - 64 * (rs2 + 8 * k));
#pragma unroll
    for (int k = 0; k < 8; ++k)
#pragma unroll
      for (int q = k; q < 8; ++q) cv[q] = __builtin_amdgcn_mfma_f32_16x16x32_f16(A[k], B[q - k], cv[q], 0, 0, 0);
  }
  TR(a.ws, 6);
  __syncthreads();
  f32x4* rb = (f32x4*)smem;
#pragma unroll
  for (int q = 0; q < 8; ++q)
    if ((q >> 1) != w) rb[(w * 8 + q) * 64 + lane] = cv[q];
  __syncthreads();
  const float dsk = bf2f(r_dsk);
  const rsrc_t rs = ws_rsrc(a.ws);
#pragma unroll
  for (int i = 0; i < 2; ++i) {
    f32x4 y = cv[0];
#pragma unroll
    for (int q = 0; q < 8; ++q) if (q == 2 * w + i) y = cv[q];
    const int q = 2 * w + i;
#pragma unroll
    for (int s = 1; s < 4; ++s) y += rb[(((w + s) & 3) * 8 + q) * 64 + lane];
    const int t = 256 * q + 16 * c + 4 * g;
    u32x2 uu = *(const u32x2*)(su + 256 + t);
    const u32x2 xx = *(const u32x2*)(sx + t);
    f16x2 ua = as_f16x2(uu[0]), ub = as_f16x2(uu[1]);
    float uf[4] = {(float)ua[0], (float)ua[1], (float)ub[0], (float)ub[1]};
    u32 y01 = pk_bf16(y[0] * kinv, y[1] * kinv), y23 = pk_bf16(y[2] * kinv, y[3] * kinv);
    u32 s01 = pk_bf16(uf[0] * dsk, uf[1] * dsk), s23 = pk_bf16(uf[2] * dsk, uf[3] * dsk);
    u32 r01 = pk_bf16(bflo(y01) + bflo(s01), bfhi(y01) + bfhi(s01));
    u32 r23 = pk_bf16(bflo(y23) + bflo(s23), bfhi(y23) + bfhi(s23));
    u32 h01 = pk_bf16(bflo(xx[0]) + bflo(r01), bfhi(xx[0]) + bfhi(r01));
    u32 h23 = pk_bf16(bflo(xx[1]) + bflo(r23), bfhi(xx[1]) + bfhi(r23));
    __builtin_amdgcn_raw_buffer_store_b64(u32x2{h01, h23}, rs, (u32)(OFF_HT + (size_t)d * LP * 2 + t * 2), 0, 16);
  }
  if (threadIdx.x == 0) { *(volatile u32*)(smem_all + LDS_SIG) = 0u; *(volatile u32*)(smem_all + LDS_SIG + 4) = 0u; *(volatile u64*)(smem_all + LDS_SIG + 8) = tag; }
  asm volatile("s_waitcnt vmcnt(0) lgkmcnt(0)" ::: "memory");
  __syncthreads();
  if (threadIdx.x < 8) st_agent(a.ws + OFF_SYNC + SY_CV + threadIdx.x * 2048 + m * 8, tag);
  TR(a.ws, 7);
  return *(volatile u64*)(smem_all + LDS_SIG + 8);
}

static constexpr int G1_R = 8;
static constexpr int G1_STG = 16384;
static constexpr int G1_ST = G1_R * G1_STG;

constexpr int g1_cnt(int n) {
  if (n < 0) return 0;
  const int ks = n >> 2, c = n & 3;
  return (n + 4 < 56 ? 2 : 0) + ((c < 2 && ks + 1 < 14) ? 4 : 0);
}
constexpr int g1_wait(int n) {
  const int c = n & 3;
  const int breq = (g1_cnt(n - 4) - 2) + g1_cnt(n - 3) + g1_cnt(n - 2) + g1_cnt(n - 1);
  const int areq = g1_cnt(n - 2) + g1_cnt(n - 1);
  return (c == 0 && areq < breq) ? areq : breq;
}

DI void phase_b(const Args& a, int m, char* smem, u64 tag) {
  const int tid = threadIdx.x, lane = tid & 63;
  const int wv = __builtin_amdgcn_readfirstlane(tid >> 6);
  const int w = wv & 3;
  const int xcd = m & 7, bi = m >> 3;
  const int tb = 2 * xcd + bi / 14, fb = bi % 14;
  const int c16 = lane & 15, g = lane >> 4, q = (lane & 15) >> 2, p = lane & 3;
  const u32 lds0 = (u32)(size_t)(LDS3 char*)smem;
  f32x2* st = (f32x2*)(smem + G1_ST);
  char* ws = a.ws;

  if (wv >= 4) {
    u32 vh[2], vw[2];
#pragma unroll
    for (int k = 0; k < 2; ++k) {
      const int row = 8 * w + 4 * k + g;
      vh[k] = (u32)(row * LP * 2) + (u32)((c16 ^ swz(row)) * 16);
      vw[k] = (u32)(row * F * 2) + (u32)((c16 ^ swz(row)) * 16);
    }
    const char* hb = ws + OFF_HT + tb * 256;
    const char* wb = (const char*)a.w1 + fb * 256;
    auto ldw = [&](auto kc) {
      constexpr int ks = decltype(kc)::value;
      constexpr int sl = (ks % G1_R) * G1_STG + 8192;
      glds16<false>(lds0 + sl + (8 * w) * 256, vw[0], wb + (size_t)ks * 32 * F * 2);
      glds16<false>(lds0 + sl + (8 * w + 4) * 256, vw[1], wb + (size_t)ks * 32 * F * 2);
    };
    auto ldh = [&](auto kc) {
      constexpr int ks = decltype(kc)::value;
      constexpr int sl = (ks % G1_R) * G1_STG;
      glds16<true>(lds0 + sl + (8 * w) * 256, vh[0], hb + (size_t)ks * 32 * LP * 2);
      glds16<true>(lds0 + sl + (8 * w + 4) * 256, vh[1], hb + (size_t)ks * 32 * LP * 2);
    };
    static_for<0, G1_R>(ldw);
    loaders_wait<4>(smem, 0, w, ws + OFF_SYNC + SY_CV + xcd * 2048, NMAIN, tag);
    TRW(ws, 8, tid == 256);
    static_for<0, G1_R>(ldh);
    auto stepl = [&](auto kc) {
      constexpr int ks = decltype(kc)::value;
      constexpr int refills = ks > 3 ? (ks - 3 < 14 - G1_R ? ks - 3 : 14 - G1_R) : 0;
      constexpr int need_after = ks < G1_R ? (2 * (G1_R - 1 - ks) + 4 * refills) : (4 * (G1_R + refills - 1 - ks));
      wait_vm<need_after>();
      bar();
      if constexpr (ks >= 3 && ks - 3 + G1_R < 14) { ldh(IC<ks - 3 + G1_R>{}); ldw(IC<ks - 3 + G1_R>{}); }
    };
    static_for<0, 14>(stepl);
    if (w == 0) wait_flags<1>(ws + OFF_SYNC + SY_W1S, NAUX, tag);
    bar();
    bar();
    bar();
    return;
  }
  const int fi = w >> 1, ti = w & 1;
  f32x4 acc[4][4];
  float s1[2] = {0.f, 0.f}, s2[2] = {0.f, 0.f};
  const bf16x2 ones = __builtin_bit_cast(bf16x2, 0x3f803f80u);
  u32 adB[2][4], adA[2][4], adBh[2][4], adAh[2][4];
#pragma unroll
  for (int t = 0; t < 2; ++t) {
    const int row = 8 * g + 4 * t + q;
    const u32 a0 = lds0 + (u32)(row * 256) + (((u32)(p >> 1) ^ swz(row)) << 4) + 8 * (p & 1);
#pragma unroll
    for (int c = 0; c < 4; ++c) {
      adB[t][c] = a0 ^ (u32)(32 * (4 * ti + ((2 * fi + c) & 3)));
      adA[t][c] = (a0 ^ (u32)(32 * (4 * fi + c))) + 8192;
      adBh[t][c] = adB[t][c] + 65536u; adAh[t][c] = adA[t][c] + 65536u;
    }
  }
  u32x2 B[8][2], A[2][4][2];
  auto rdB = [&](auto nc, auto tc) {
    constexpr int n = decltype(nc)::value, t = decltype(tc)::value, sl = (n >> 2) % G1_R;
    trread<(sl & 3) * G1_STG>(B[n & 7][t], sl < 4 ? adB[t][n & 3] : adBh[t][n & 3]);
  };
  auto rdA = [&](auto kc, auto ic, auto tc) {
    constexpr int ks = decltype(kc)::value, i = decltype(ic)::value, t = decltype(tc)::value, sl = ks % G1_R;
    trread<(sl & 3) * G1_STG>(A[ks & 1][i][t], sl < 4 ? adA[t][i] : adAh[t][i]);
  };
  auto group = [&](auto nc) {
    constexpr int n = decltype(nc)::value;
    constexpr int ks = n >> 2, c = n & 3, sl = n & 7;
    constexpr bool nb = n + 4 < 56, na = (c < 2) && (ks + 1 < 14);
    constexpr int wt = n < 4 ? (n == 0 ? 6 : n == 1 ? 10 : n == 2 ? 14 : 14) : g1_wait(n);
    asm volatile("s_waitcnt lgkmcnt(%10)" : "+v"(B[sl][0]), "+v"(B[sl][1]), "+v"(A[ks & 1][0][0]), "+v"(A[ks & 1][0][1]), "+v"(A[ks & 1][1][0]), "+v"(A[ks & 1][1][1]),
                 "+v"(A[ks & 1][2][0]), "+v"(A[ks & 1][2][1]), "+v"(A[ks & 1][3][0]), "+v"(A[ks & 1][3][1]) : "n"(wt) : "memory");
    const u32x4 bfr = cat(B[sl][0], B[sl][1]);
    mfma_bf16<ks == 0>(acc[0][c], cat(A[ks & 1][0][0], A[ks & 1][0][1]), bfr);
    __builtin_amdgcn_sched_barrier(0);
    if constexpr (c == 0 && ks + 1 < 14) bar();
    if constexpr (nb) rdB(IC<n + 4>{}, IC<0>{});
    __builtin_amdgcn_sched_barrier(0);
    mfma_bf16<ks == 0>(acc[1][c], cat(A[ks & 1][1][0], A[ks & 1][1][1]), bfr);
    __builtin_amdgcn_sched_barrier(0);
    if constexpr (nb) rdB(IC<n + 4>{}, IC<1>{});
    if constexpr (na) rdA(IC<ks + 1>{}, IC<2 * c>{}, IC<0>{});
    __builtin_amdgcn_sched_barrier(0);
    mfma_bf16<ks == 0>(acc[2][c], cat(A[ks & 1][2][0], A[ks & 1][2][1]), bfr);
    __builtin_amdgcn_sched_barrier(0);
    if constexpr (na) { rdA(IC<ks + 1>{}, IC<2 * c>{}, IC<1>{}); rdA(IC<ks + 1>{}, IC<2 * c + 1>{}, IC<0>{}); }
    if constexpr (c < 2) {
#pragma unroll
      for (int k = 0; k < 2; ++k) {
        bf16x2 x2 = as_bf16x2(bfr[k]);
        s1[c] = __builtin_amdgcn_fdot2_f32_bf16(x2, ones, s1[c], false);
        s2[c] = __builtin_amdgcn_fdot2_f32_bf16(x2, x2, s2[c], false);
      }
    }
    __builtin_amdgcn_sched_barrier(0);
    mfma_bf16<ks == 0>(acc[3][c], cat(A[ks & 1][3][0], A[ks & 1][3][1]), bfr);
    __builtin_amdgcn_sched_barrier(0);
    if constexpr (na) rdA(IC<ks + 1>{}, IC<2 * c + 1>{}, IC<1>{});
    if constexpr (c < 2) {
#pragma unroll
      for (int k = 2; k < 4; ++k) {
        bf16x2 x2 = as_bf16x2(bfr[k]);
        s1[c] = __builtin_amdgcn_fdot2_f32_bf16(x2, ones, s1[c], false);
        s2[c] = __builtin_amdgcn_fdot2_f32_bf16(x2, x2, s2[c], false);
      }
      if (ks == fb) {
        const int tt = 4 * ti + ((2 * fi + c) & 3);
        __builtin_amdgcn_raw_buffer_store_b128(bfr, ws_rsrc(ws), (u32)(OFF_HN + ((size_t)(tb * 128 + 16 * tt + c16) * D + 32 * fb + 8 * g) * 2), 0, 16);
      }
    }
    __builtin_amdgcn_sched_barrier(0);
  };
  bar();
  static_for<0, 4>([&](auto ic) { rdA(IC<0>{}, ic, IC<0>{}); rdA(IC<0>{}, ic, IC<1>{}); });
  rdB(IC<0>{}, IC<0>{}); rdB(IC<0>{}, IC<1>{}); rdB(IC<1>{}, IC<0>{}); rdB(IC<1>{}, IC<1>{});
  asm volatile("s_waitcnt lgkmcnt(4)" ::: "memory");
  rdB(IC<2>{}, IC<0>{}); rdB(IC<2>{}, IC<1>{}); rdB(IC<3>{}, IC<0>{}); rdB(IC<3>{}, IC<1>{});
  static_for<0, 56>(group);
  asm volatile("s_nop 7\n\ts_nop 7" ::: "memory");
  TR(ws, 9);
  bar();
  f32x4 wsv[4];
#pragma unroll
  for (int i = 0; i < 4; ++i)
    wsv[i] = __builtin_bit_cast(f32x4, __builtin_amdgcn_raw_buffer_load_b128(ws_rsrc(ws), (u32)(OFF_W1S + (fb * 128 + 64 * fi + 4 * g + 16 * i) * 4), 0, 16));
#pragma unroll
  for (int i = 0; i < 2; ++i) {
    float sa = s1[i], sb = s2[i];
    sa += __shfl_xor(sa, 16); sa += __shfl_xor(sa, 32);
    sb += __shfl_xor(sb, 16); sb += __shfl_xor(sb, 32);
    const float mean = sa * (1.0f / D);
    const float var = fmaxf(sb * (1.0f / D) - mean * mean, 0.f);
    const float mu = rbf(mean);
    const float sd = rbf(__builtin_amdgcn_sqrtf(rbf(rbf(var) + 1e-5f)));
    const int tt = 4 * ti + ((2 * fi + i) & 3);
    if (lane < 16) st[tt * 16 + lane] = f32x2{mu, __builtin_amdgcn_rcpf(sd)};
  }
  lds_bar();
#pragma unroll
  for (int c = 0; c < 4; ++c) {
    const int tt = 4 * ti + ((2 * fi + c) & 3);
    const f32x2 ms = st[tt * 16 + c16];
#pragma unroll
    for (int pp = 0; pp < 2; ++pp) {
      float v0[4], v1[4];
#pragma unroll
      for (int k = 0; k < 4; ++k) {
        v0[k] = fmaxf((acc[2 * pp][c][k] - ms[0] * wsv[2 * pp][k]) * ms[1], 0.f);
        v1[k] = fmaxf((acc[2 * pp + 1][c][k] - ms[0] * wsv[2 * pp + 1][k]) * ms[1], 0.f);
      }
      u32x4 o = {pk_bf16(v0[0], v0[1]), pk_bf16(v0[2], v0[3]), pk_bf16(v1[0], v1[1]), pk_bf16(v1[2], v1[3])};
      __builtin_amdgcn_raw_buffer_store_b128(o, ws_rsrc(ws), (u32)(OFF_H1F + (((size_t)(tb * 8 + tt) * 56 + fb * 4 + 2 * fi + pp) * 64 + lane) * 16), 0, 16);
    }
  }
  asm volatile("s_waitcnt vmcnt(0)" ::: "memory");
  bar();
  if (tid == 0) st_agent(ws + OFF_SYNC + SY_H1 + tb * 128 + fb * 8, tag);
  TR(ws, 10);
}

static constexpr int G2_R = 4;
static constexpr int G2_STG = 32768;
static constexpr int G2_RS = 272;

DI void phase_c(const Args& a, int m, char* smem, u64 tag) {
  const int tid = threadIdx.x, lane = tid & 63;
  const int wv = __builtin_amdgcn_readfirstlane(tid >> 6);
  const int w = wv & 3;
  const int xcd = m & 7, bi = m >> 3;
  const int tb = 2 * xcd + bi / 14, fb = bi % 14;
  const int t64 = 2 * tb + fb / 7, db = fb % 7;
  const u32 lds0 = (u32)(size_t)(LDS3 char*)smem;
  char* ws = a.ws;
  const int c16 = lane & 15, g = lane >> 4, q = (lane & 15) >> 2, p = lane & 3;
  if (wv >= 4) {
    const int rr = lane >> 3;
    u32 vw[4], vo[4];
#pragma unroll
    for (int i = 0; i < 4; ++i) {
      const int row = 8 * i + rr;
      vw[i] = (u32)(row * D * 2) + ((((u32)(lane & 7)) ^ (u32)(((row >> 1) & 3) << 1)) << 4);
      vo[i] = lane * 16 + i * 56 * 1024;
    }
    const char* wb = (const char*)a.w2 + ((size_t)(448 * w) * D + db * 64) * 2;
    const char* hb = ws + OFF_H1F + ((size_t)(t64 * 4) * 56 + 14 * w) * 1024;
    auto ldw = [&](auto kc) {
      constexpr int k = decltype(kc)::value;
      constexpr int sl = (k % G2_R) * G2_STG;
#pragma unroll
      for (int i = 0; i < 4; ++i) glds16<false>(lds0 + sl + w * 8192 + i * 1024, vw[i], wb + (size_t)k * 32 * D * 2);
    };
    auto ldh = [&](auto kc) {
      constexpr int k = decltype(kc)::value;
      constexpr int sl = (k % G2_R) * G2_STG;
#pragma unroll
      for (int i = 0; i < 4; ++i) glds16<true>(lds0 + sl + w * 8192 + 4096 + i * 1024, vo[i], hb + k * 1024);
    };
    static_for<0, G2_R>(ldw);
    loaders_wait<1>(smem, 1, w, ws + OFF_SYNC + SY_H1 + tb * 128, 14, tag);
    TRW(ws, 11, tid == 256);
    static_for<0, G2_R>(ldh);
    auto stepl = [&](auto kc) {
      constexpr int k = decltype(kc)::value;
      constexpr int refills = k > 1 ? (k - 1 < 14 - G2_R ? k - 1 : 14 - G2_R) : 0;
      constexpr int need_after = k < G2_R ? (4 * (G2_R - 1 - k) + 8 * refills) : (8 * (G2_R + refills - 1 - k));
      wait_vm<need_after>();
      bar();
      if constexpr (k >= 1 && k - 1 + G2_R < 14) { ldh(IC<k - 1 + G2_R>{}); ldw(IC<k - 1 + G2_R>{}); }
    };
    static_for<0, 14>(stepl);
    bar();
    bar();
    return;
  }
  u32x4 res[2];
  f32x4 acc[4][4];
  u32 adW[4];
  {
    const int row = 4 * g + q;
    const u32 f = (u32)((row >> 1) & 3);
    const u32 a0 = lds0 + w * 8192 + (u32)(row * 128) + (u32)((p >> 1) * 16 + 8 * (p & 1));
#pragma unroll
    for (int dl = 0; dl < 4; ++dl) adW[dl] = a0 + ((((u32)dl) ^ f) << 5);
  }
  const u32 adH = lds0 + w * 8192 + 4096 + lane * 16;
  u32x2 Wf[2][4][2];
  u32x4 Hf[2][4];
  auto rd = [&](auto kc, auto ic) {
    constexpr int k = decltype(kc)::value, i = decltype(ic)::value;
    constexpr int sl = (k % G2_R) * G2_STG;
    constexpr int off = (sl & 65535);
    constexpr u32 hi = (u32)(sl & ~65535);
    if constexpr (i < 8) trread<off + (i & 1) * 2048>(Wf[k & 1][i >> 1][i & 1], adW[i >> 1] + hi);
    else ldsread128<off + (i - 8) * 1024>(Hf[k & 1][i - 8], adH + hi);
  };
  auto step = [&](auto kc) {
    constexpr int k = decltype(kc)::value;
    asm volatile("s_waitcnt lgkmcnt(0)" : "+v"(Wf[k & 1][0][0]), "+v"(Wf[k & 1][0][1]), "+v"(Wf[k & 1][1][0]), "+v"(Wf[k & 1][1][1]), "+v"(Wf[k & 1][2][0]), "+v"(Wf[k & 1][2][1]),
                 "+v"(Wf[k & 1][3][0]), "+v"(Wf[k & 1][3][1]), "+v"(Hf[k & 1][0]), "+v"(Hf[k & 1][1]), "+v"(Hf[k & 1][2]), "+v"(Hf[k & 1][3]) :: "memory");
    if constexpr (k + 1 < 14) bar();
    static_for<0, 16>([&](auto jc) {
      constexpr int j = decltype(jc)::value, dl = j >> 2, ta = j & 3;
      mfma_bf16<k == 0>(acc[dl][ta], cat(Wf[k & 1][dl][0], Wf[k & 1][dl][1]), Hf[k & 1][ta]);
      __builtin_amdgcn_sched_barrier(0);
      if constexpr (k + 1 < 14 && j < 12) rd(IC<k + 1>{}, IC<j>{});
      __builtin_amdgcn_sched_barrier(0);
    });
  };
  bar();
  static_for<0, 12>([&](auto ic) { rd(IC<0>{}, ic); });
  {
    const rsrc_t rs = ws_rsrc(ws);
#pragma unroll
    for (int i = 0; i < 2; ++i)
      res[i] = __builtin_amdgcn_raw_buffer_load_b128(rs, (u32)(OFF_HN + ((size_t)(t64 * 64 + 32 * i + (tid >> 3)) * D + db * 64 + (tid & 7) * 8) * 2), 0, 16);
  }
  static_for<0, 14>(step);
  asm volatile("s_nop 7\n\ts_nop 7" ::: "memory");
  TR(ws, 12);
  bar();
#pragma unroll
  for (int dl = 0; dl < 4; ++dl)
#pragma unroll
    for (int ta = 0; ta < 4; ++ta)
      *(f32x4*)(smem + (w * 64 + 16 * ta + c16) * G2_RS + (16 * dl + 4 * g) * 4) = acc[dl][ta];
  lds_bar();
#pragma unroll
  for (int i = 0; i < 2; ++i) {
    const int t = (tid >> 3) + 32 * i, chn = tid & 7;
    const u32x4 rr = res[i];
    f32x4 s0 = {0.f, 0.f, 0.f, 0.f}, s1 = s0;
#pragma unroll
    for (int ww = 0; ww < 4; ++ww) {
      s0 += *(const f32x4*)(smem + (ww * 64 + t) * G2_RS + chn * 32);
      s1 += *(const f32x4*)(smem + (ww * 64 + t) * G2_RS + chn * 32 + 16);
    }
    u32 p0 = pk_bf16(s0[0], s0[1]), p1 = pk_bf16(s0[2], s0[3]), p2 = pk_bf16(s1[0], s1[1]), p3 = pk_bf16(s1[2], s1[3]);
    u32x4 o;
    o[0] = pk_bf16(bflo(rr[0]) + bflo(p0), bfhi(rr[0]) + bfhi(p0));
    o[1] = pk_bf16(bflo(rr[1]) + bflo(p1), bfhi(rr[1]) + bfhi(p1));
    o[2] = pk_bf16(bflo(rr[2]) + bflo(p2), bfhi(rr[2]) + bfhi(p2));
    o[3] = pk_bf16(bflo(rr[3]) + bflo(p3), bfhi(rr[3]) + bfhi(p3));
    *(u32x4*)(a.out + (size_t)(t64 * 64 + t) * D + db * 64 + chn * 8) = o;
  }
}

__global__ __launch_bounds__(512, 2) void s4_block_kernel(Args a) {
  extern __shared__ __attribute__((aligned(16))) char smem[];
  const int b = blockIdx.x;
  TR(a.ws, 0);
  if (b < NAUX) { aux_block(a, b, smem); TR(a.ws, 15); TRDUMP(a.ws); return; }
  const int m = b - NAUX;
  const u64 tag = phase_a(a, m, smem);
  phase_b(a, m, smem, tag);
  __syncthreads();
  phase_c(a, m, smem, tag);
  TR(a.ws, 15);
  TRDUMP(a.ws);
}

extern "C" void kernel_launch(void* const* d_in, const int* in_sizes, int n_in,
                              void* d_out, int out_size, void* d_ws, size_t ws_size,
                              hipStream_t stream) {
  Args a;
  a.x = (const u16*)d_in[0]; a.Are = (const u16*)d_in[1]; a.Aim = (const u16*)d_in[2];
  a.Bre = (const u16*)d_in[3]; a.Bim = (const u16*)d_in[4]; a.Cre = (const u16*)d_in[5];
  a.Cim = (const u16*)d_in[6]; a.logdt = (const u16*)d_in[7]; a.Dskip = (const u16*)d_in[8];
  a.w1 = (const u16*)d_in[10]; a.w2 = (const u16*)d_in[11]; a.ws = (char*)d_ws; a.out = (u16*)d_out; a.pad = nullptr;
  (void)hipFuncSetAttribute((const void*)s4_block_kernel, hipFuncAttributeMaxDynamicSharedMemorySize, SMEM_BYTES);
  s4_block_kernel<<<NAUX + NMAIN, 512, SMEM_BYTES, stream>>>(a);
}
```

```cpp
#define PF_CODE 0
#define PF_W 1
#include <hip/hip_runtime.h>
#include <hip/hip_bf16.h>
#include <stdint.h>
#include <type_traits>

static constexpr int L = 2048, D = 448, NM = 256, F = 1792;
static constexpr int LP = 2176;
static constexpr size_t OFF_ST = 0;
static constexpr size_t OFF_XT = 8192;
static constexpr size_t OFF_HT = OFF_XT + (size_t)D * LP * 2;
static constexpr size_t OFF_W1S = OFF_HT + (size_t)D * LP * 2;
static constexpr size_t OFF_HN = OFF_W1S + 8192;
static constexpr size_t OFF_H1F = OFF_HN + (size_t)L * D * 2;
static constexpr size_t OFF_SYNC = OFF_H1F + (size_t)L * F * 2;
static constexpr size_t SY_EP = 0, SY_LN = 1024  , SY_W1S = 4096, SY_CV = 8192  , SY_H1 = 32768  ;
static constexpr size_t WS_USED = OFF_SYNC + 65536;

typedef unsigned short u16;
typedef unsigned int u32;
typedef unsigned long long u64;
typedef _Float16 f16;
typedef f16 f16x2 __attribute__((ext_vector_type(2)));
typedef f16 f16x8 __attribute__((ext_vector_type(8)));
typedef __bf16 bf16x2 __attribute__((ext_vector_type(2)));
typedef __bf16 bf16x8 __attribute__((ext_vector_type(8)));
typedef float f32x2 __attribute__((ext_vector_type(2)));
typedef float f32x4 __attribute__((ext_vector_type(4)));
typedef u32 u32x2 __attribute__((ext_vector_type(2)));
typedef u32 u32x4 __attribute__((ext_vector_type(4)));
typedef short s16x4 __attribute__((ext_vector_type(4)));
#define LDS3 __attribute__((address_space(3)))
typedef __attribute__((address_space(1))) u64 gu64;
typedef __amdgpu_buffer_rsrc_t rsrc_t;

#define DI __device__ __forceinline__
DI float bf2f(u16 v) { return __uint_as_float(((u32)v) << 16); }
DI float bflo(u32 v) { return __uint_as_float(v << 16); }
DI float bfhi(u32 v) { return __uint_as_float(v & 0xffff0000u); }
DI u32 pk_bf16(float a, float b) { f32x2 v = {a, b}; return __builtin_bit_cast(u32, __builtin_convertvector(v, bf16x2)); }
DI u32 pk_f16(float a, float b) { f32x2 v = {a, b}; return __builtin_bit_cast(u32, __builtin_convertvector(v, f16x2)); }
DI float rbf(float x) { return bflo(pk_bf16(x, 0.f)); }
DI f16x2 as_f16x2(u32 v) { return __builtin_bit_cast(f16x2, v); }
DI bf16x2 as_bf16x2(u32 v) { return __builtin_bit_cast(bf16x2, v); }
template <int CTRL, int RM> DI float dppf(float old, float v) {
  return __int_as_float(__builtin_amdgcn_update_dpp(__float_as_int(old), __float_as_int(v), CTRL, RM, 0xf, false));
}
DI float wave_sum(float v) {
  v += dppf<0x111, 0xf>(0.f, v); v += dppf<0x112, 0xf>(0.f, v); v += dppf<0x114, 0xf>(0.f, v); v += dppf<0x118, 0xf>(0.f, v);
  v += dppf<0x142, 0xa>(0.f, v); v += dppf<0x143, 0xc>(0.f, v);
  return __int_as_float(__builtin_amdgcn_readlane(__float_as_int(v), 63));
}
DI float wave_max(float v) {
  v = fmaxf(v, dppf<0x111, 0xf>(v, v)); v = fmaxf(v, dppf<0x112, 0xf>(v, v)); v = fmaxf(v, dppf<0x114, 0xf>(v, v)); v = fmaxf(v, dppf<0x118, 0xf>(v, v));
  v = fmaxf(v, dppf<0x142, 0xa>(v, v)); v = fmaxf(v, dppf<0x143, 0xc>(v, v));
  return __int_as_float(__builtin_amdgcn_readlane(__float_as_int(v), 63));
}
DI u64 ld_agent(const void* p) { return __hip_atomic_load((const gu64*)p, __ATOMIC_RELAXED, __HIP_MEMORY_SCOPE_AGENT); }
DI void st_agent(void* p, u64 v) { __hip_atomic_store((gu64*)p, v, __ATOMIC_RELAXED, __HIP_MEMORY_SCOPE_AGENT); }
static constexpr u64 TAG_INC = 0x9E3779B97F4A7C15ull;
DI rsrc_t ws_rsrc(char* ws) { return __builtin_amdgcn_make_buffer_rsrc(ws, 0, (int)WS_USED, 0x00020000); }
template <int B, int E, class Fn> DI void static_for(Fn&& f) {
  if constexpr (B < E) { f(std::integral_constant<int, B>{}); static_for<B + 1, E>(f); }
}
template <int N> using IC = std::integral_constant<int, N>;
DI s16x4 tr_read(const void* p) { return __builtin_amdgcn_ds_read_tr16_b64_v4i16((LDS3 s16x4*)p); }
template <int N> DI void wait_vm() { asm volatile("s_waitcnt vmcnt(%0)" :: "n"(N) : "memory"); }
DI void bar() { __builtin_amdgcn_s_barrier(); }
DI void lds_bar() { asm volatile("s_waitcnt lgkmcnt(0)" ::: "memory"); __builtin_amdgcn_s_barrier(); }
template <bool SC1> DI void glds16(u32 lds_dst, u32 voff, const void* sbase) {
  u32 keep;
  if constexpr (SC1)
    asm volatile("s_mov_b32 %0, m0\n\ts_mov_b32 m0, %1\n\ts_nop 0\n\tglobal_load_lds_dwordx4 %2, %3 sc1\n\ts_mov_b32 m0, %0"
                 : "=&s"(keep) : "s"(lds_dst), "v"(voff), "s"(sbase) : "memory");
  else
    asm volatile("s_mov_b32 %0, m0\n\ts_mov_b32 m0, %1\n\ts_nop 0\n\tglobal_load_lds_dwordx4 %2, %3\n\ts_mov_b32 m0, %0"
                 : "=&s"(keep) : "s"(lds_dst), "v"(voff), "s"(sbase) : "memory");
}
template <int OFF> DI void trread(u32x2& dst, u32 addr) {
  asm volatile("ds_read_b64_tr_b16 %0, %1 offset:%2" : "=v"(dst) : "v"(addr), "n"(OFF) : "memory");
}
template <int OFF> DI void ldsread128(u32x4& dst, u32 addr) {
  asm volatile("ds_read_b128 %0, %1 offset:%2" : "=v"(dst) : "v"(addr), "n"(OFF) : "memory");
}
template <bool FIRST> DI void mfma_bf16(f32x4& acc, const u32x4& a, const u32x4& b) {
  if constexpr (FIRST) asm volatile("s_nop 1\n\tv_mfma_f32_16x16x32_bf16 %0, %1, %2, 0" : "=v"(acc) : "v"(a), "v"(b));
  else asm volatile("s_nop 1\n\tv_mfma_f32_16x16x32_bf16 %0, %1, %2, %0" : "+v"(acc) : "v"(a), "v"(b));
}
DI u32x4 cat(const u32x2& a, const u32x2& b) { return u32x4{a[0], a[1], b[0], b[1]}; }
DI u32 swz(int row) { return (u32)(((row & 3) << 2) | ((row >> 2) & 3)); }
DI f32x2 cmul(f32x2 y, f32x2 e) {
  f32x2 t, r;
  asm("v_pk_mul_f32 %0, %1, %2 op_sel_hi:[1,0]" : "=v"(t) : "v"(y), "v"(e));
  asm("v_pk_fma_f32 %0, %1, %2, %3 op_sel:[1,1,0] op_sel_hi:[0,1,1] neg_lo:[0,1,0]" : "=v"(r) : "v"(y), "v"(e), "v"(t));
  return r;
}
DI f32x2 cmul_conj(f32x2 p, f32x2 q) {
  f32x2 t, r;
  asm("v_pk_mul_f32 %0, %1, %2 op_sel_hi:[1,0] neg_hi:[1,0]" : "=v"(t) : "v"(p), "v"(q));
  asm("v_pk_fma_f32 %0, %1, %2, %3 op_sel:[1,1,0] op_sel_hi:[0,1,1] neg_lo:[1,0,0] neg_hi:[1,0,0]" : "=v"(r) : "v"(p), "v"(q), "v"(t));
  return r;
}

#define TR(ws, slot) do {} while (0)
#define TRW(ws, slot, cond) do {} while (0)
#define TRDUMP(ws) do {} while (0)

struct Args {
  const u16 *x, *Are, *Aim, *Bre, *Bim, *Cre, *Cim, *logdt, *Dskip, *w1, *w2;
  char* ws;
  u16* out;
  const void* pad;
};

constexpr int rne_bf16_int(int l) {
  if (l < 256) return l;
  int sh = 0; while ((l >> sh) >= 256) ++sh;
  const int q = l >> sh, rem = l & ((1 << sh) - 1), half = 1 << (sh - 1);
  const int up = (rem > half) || (rem == half && (q & 1));
  return (q + up) << sh;
}
constexpr int kslot(int m) {
  const int l = 2047 - m;
  if (l < 0) return 1025;
  const int t = rne_bf16_int(l);
  if (t >= 2048) return 1024;
  const int seg = (t >= 256) + (t >= 512) + (t >= 1024);
  const int row = seg == 0 ? (t & 7) : seg == 1 ? 8 + ((t >> 1) & 3) : seg == 2 ? 12 + ((t >> 2) & 1) : 14 + ((t >> 9) & 1);
  const int b = (t >> 3) & (seg >= 2 ? 63 : 31);
  return b * 16 + row;
}
struct SlotTab { u16 v[2064]; };
constexpr SlotTab make_slot_tab() { SlotTab t{}; for (int m = 0; m < 2064; ++m) t.v[m] = (u16)(kslot(m) * 4); return t; }
__device__ __attribute__((aligned(16))) const SlotTab g_slot_tab = make_slot_tab();

static constexpr int NAUX = 32;
static constexpr int NMAIN = 224;
static constexpr int SMEM_BYTES = 160 * 1024;
static constexpr unsigned SPIN_CAP = 1u << 20;
static constexpr int LDS_SIG = SMEM_BYTES - 272;

template <int NPL> DI void wait_flags(const char* base, int n, u64 tag) {
  const int lane = threadIdx.x & 63;
  unsigned spins = 0;
  for (;;) {
    bool ok = true;
#pragma unroll
    for (int k = 0; k < NPL; ++k) { const int i = k * 64 + lane; if (i < n) ok &= (ld_agent(base + i * 8) == tag); }
    if (__all(ok)) break;
    __builtin_amdgcn_s_sleep(1);
    if (++spins > SPIN_CAP) break;
  }
}

template <int NPL> DI void loaders_wait(char* smem, int idx, int w, const char* base, int n, u64 tag) {
  volatile u32* sig = (volatile u32*)(smem + LDS_SIG) + idx;
  if (w == 0) {
    wait_flags<NPL>(base, n, tag);
    *sig = 1u;
  } else {
    unsigned spins = 0;
    while (*sig == 0u) { __builtin_amdgcn_s_sleep(1); if (++spins > (SPIN_CAP << 4)) break; }
  }
  asm volatile("" ::: "memory");
}

DI void l2_touch(const char* base, int kbytes, int part, int nparts, char* smem) {
  const int lane = threadIdx.x & 63;
  const int w = __builtin_amdgcn_readfirstlane(threadIdx.x >> 6);
  const u32 dst = (u32)(size_t)(LDS3 char*)smem + 65536 + w * 1024;
  const int per = (kbytes + nparts - 1) / nparts;
  const int k0 = per * part, k1 = (k0 + per < kbytes) ? k0 + per : kbytes;
  int cnt = 0;
  for (int k = k0 + w; k < k1; k += 8) {
    glds16<false>(dst, lane * 16, base + (size_t)k * 1024);
    if ((++cnt & 7) == 0) wait_vm<32>();
  }
  wait_vm<0>();
}
static constexpr int LNRS = 912;
DI void aux_block(const Args& a, int blk, char* smem) {
  const int tid = threadIdx.x, lane = tid & 63;
  const int w = __builtin_amdgcn_readfirstlane(tid >> 6);
  const int t0 = blk * 64;
  __builtin_amdgcn_s_setprio(2);
  u32x4 v[8];
#pragma unroll
  for (int i = 0; i < 8; ++i) {
    v[i] = u32x4{0, 0, 0, 0};
    if (lane < 56) v[i] = *(const u32x4*)(a.x + (size_t)(t0 + 8 * w + i) * D + lane * 8);
  }
#pragma unroll
  for (int i = 0; i < 8; ++i)
    if (lane < 56) *(u32x4*)(smem + (8 * w + i) * LNRS + lane * 16) = v[i];
  TR(a.ws, 1);
  __syncthreads();
  TR(a.ws, 5);
  const rsrc_t rs = ws_rsrc(a.ws);
  const int g = lane >> 4, i16 = lane & 15, q = i16 >> 2, p = lane & 3;
  if (w >= 4) {
    const char* src0 = smem + (16 * g + 8 * (p >> 1) + q) * LNRS + (4 * (p & 1)) * 2;
    const u32 off0 = (u32)(OFF_XT + (size_t)(i16 & 7) * LP * 2 + (t0 + 16 * g + 8 * (i16 >> 3)) * 2);
#pragma unroll
    for (int h2 = 0; h2 < 2; ++h2) {
      s16x4 lo[7], hi[7];
#pragma unroll
      for (int k = 0; k < 7; ++k) {
        const int cg = (w - 4) + 4 * (k + 7 * h2);
        lo[k] = tr_read(src0 + cg * 16); hi[k] = tr_read(src0 + cg * 16 + 4 * LNRS);
      }
      __builtin_amdgcn_sched_barrier(0);
#pragma unroll
      for (int k = 0; k < 7; ++k) {
        const int cg = (w - 4) + 4 * (k + 7 * h2);
        __builtin_amdgcn_raw_buffer_store_b128(cat(__builtin_bit_cast(u32x2, lo[k]), __builtin_bit_cast(u32x2, hi[k])), rs, off0 + (u32)(cg * 8 * LP * 2), 0, 16);
      }
      __builtin_amdgcn_sched_barrier(0);
    }
  }
  TR(a.ws, 4);
  if (w < 4) {
    f32x4 a1 = {0.f, 0.f, 0.f, 0.f}, a2 = a1;
    const u32x4 one4 = {0x3f803f80u, 0x3f803f80u, 0x3f803f80u, 0x3f803f80u};
    bf16x8 fr[14];
#pragma unroll
    for (int ks = 0; ks < 14; ++ks) fr[ks] = *(const bf16x8*)(smem + (16 * w + i16) * LNRS + (32 * ks + 8 * g) * 2);
    __builtin_amdgcn_sched_barrier(0);
#pragma unroll
    for (int ks = 0; ks < 14; ++ks) {
      a1 = __builtin_amdgcn_mfma_f32_16x16x32_bf16(fr[ks], __builtin_bit_cast(bf16x8, one4), a1, 0, 0, 0);
      a2 = __builtin_amdgcn_mfma_f32_16x16x32_bf16(fr[ks], fr[ks], a2, 0, 0, 0);
    }
    const float s = p == 0 ? a1[0] : p == 1 ? a1[1] : p == 2 ? a1[2] : a1[3];
    const float q2 = p == 0 ? a2[0] : p == 1 ? a2[1] : p == 2 ? a2[2] : a2[3];
    const float mean = s * (1.0f / D);
    const float var = fmaxf(q2 * (1.0f / D) - mean * mean, 0.f);
    const float sd = rbf(__builtin_amdgcn_sqrtf(rbf(rbf(var) + 1e-5f)));
    if (g == q) __builtin_amdgcn_raw_buffer_store_b32(pk_bf16(mean, sd), rs, (u32)(OFF_ST + (t0 + 16 * w + i16) * 4), 0, 16);
  }
  const u64 ep = ld_agent(w == 0 ? (const char*)(a.ws + OFF_SYNC + SY_EP) : (const char*)(a.x + (size_t)(t0 + w) * D));
  TR(a.ws, 2);
  asm volatile("s_waitcnt vmcnt(0)" ::: "memory");
  const u64 tag = ep + TAG_INC;
  __syncthreads();
  if (tid < 8) st_agent(a.ws + OFF_SYNC + SY_LN + tid * 256 + blk * 8, tag);
  __builtin_amdgcn_s_setprio(0);
  TR(a.ws, 3);
  {
    float* red = (float*)smem;
    const int cp = tid % 28, rg = tid / 28;
    float s0 = 0.f, s1 = 0.f;
    if (rg < 16) {
      u32 x[28];
#pragma unroll
      for (int i = 0; i < 28; ++i) x[i] = *(const u32*)(a.w1 + (size_t)(rg + 16 * i) * F + 56 * blk + 2 * cp);
#pragma unroll
      for (int i = 0; i < 28; ++i) { s0 += bflo(x[i]); s1 += bfhi(x[i]); }
      red[rg * 56 + 2 * cp] = s0; red[rg * 56 + 2 * cp + 1] = s1;
    }
    __syncthreads();
    if (tid < 56) {
      float s = 0.f;
#pragma unroll
      for (int i = 0; i < 16; ++i) s += red[i * 56 + tid];
      __builtin_amdgcn_raw_buffer_store_b32(__float_as_uint(s), rs, (u32)(OFF_W1S + (56 * blk + tid) * 4), 0, 16);
    }
    asm volatile("s_waitcnt vmcnt(0)" ::: "memory");
    __syncthreads();
    if (tid == 0) st_agent(a.ws + OFF_SYNC + SY_W1S + blk * 8, tag);
  }
  l2_touch((const char*)a.w1, D * F * 2 / 1024, blk >> 3, 4, smem);
  l2_touch((const char*)a.w2, D * F * 2 / 1024, blk >> 3, 4, smem);
  if (blk == 0 && w == 0) {
    wait_flags<1>(a.ws + OFF_SYNC + SY_LN, NAUX, tag);
    wait_flags<4>(a.ws + OFF_SYNC + SY_CV, NMAIN, tag);
    if (lane == 0) st_agent(a.ws + OFF_SYNC + SY_EP, tag);
  }
}

static constexpr int CPS = 4160;
static constexpr int CH_BYTES = 61440;
static constexpr int CH_TAB = 0;
static constexpr int CH_SU = 8 * CPS;
static constexpr int CH_F = CH_SU + 4608 + 256;
static constexpr int CH_SX = 47104;
static constexpr int CH_ST = CH_SX + 4096;
static constexpr int CH_RED = 59392;

DI u64 phase_a(const Args& a, int m, char* smem_all) {
  const int tid = threadIdx.x & 255, lane = threadIdx.x & 63;
  const int wv = __builtin_amdgcn_readfirstlane(threadIdx.x >> 6);
  const int w = wv & 3, hw = wv >> 2;
  const int d = 2 * m + hw;
  char* smem = smem_all + hw * CH_BYTES;
  const float LOG2E = 1.4426950408889634f, INV2PI = 0.15915494309189535f;
  const u32 lds0 = (u32)(size_t)(LDS3 char*)smem;
  const int n = tid;
  const u16 r_are = a.Are[d * NM + n], r_aim = a.Aim[d * NM + n], r_bre = a.Bre[d * NM + n], r_bim = a.Bim[d * NM + n];
  const u16 r_cre = a.Cre[d * NM + n], r_cim = a.Cim[d * NM + n], r_ldt = a.logdt[d], r_dsk = a.Dskip[d];
  u32 slot2[8];
  {
    const u32x4 s0 = *(const u32x4*)(g_slot_tab.v + 8 * tid), s1 = *(const u32x4*)(g_slot_tab.v + 8 * tid + 8);
#pragma unroll
    for (int k = 0; k < 4; ++k) { slot2[k] = s0[k]; slot2[4 + k] = s1[k]; }
  }
  const u64 ep = ld_agent(wv == 0 ? (const char*)(a.ws + OFF_SYNC + SY_EP) : (const char*)(a.Are + d * NM + 4 * w));
  float* red = (float*)(smem + CH_RED);
  const float dt = rbf(expf(bf2f(r_ldt)));
  const float bre = dt * bf2f(r_bre), bim = dt * bf2f(r_bim);
  const float cre = bf2f(r_cre), cim = bf2f(r_cim);
  const float zr = (dt * bf2f(r_are)) * LOG2E, zi = (dt * bf2f(r_aim)) * INV2PI;
  const float cr = cre * bre - cim * bim, ci = cre * bim + cim * bre;
  const float mx = fmaxf(fabsf(cr), fabsf(ci));
  const float lc = mx > 1e-36f ? __builtin_amdgcn_logf(mx) + 0.5f : -1000.f;
  const float sY = fmaxf(0.f, zr * 504.f);
  const float gm = fmaxf(lc + fmaxf(0.f, zr * 1536.f) + sY, lc + zr * 2048.f);
  const float G = ceilf(wave_max(gm));
  auto cexp = [&](float t, float lg) -> f32x2 {
    const float mm = __builtin_amdgcn_exp2f(__builtin_fmaf(zr, t, lg)), f = __builtin_amdgcn_fractf(zi * t);
    return f32x2{mm * __builtin_amdgcn_cosf(f), mm * __builtin_amdgcn_sinf(f)};
  };
  {
    f32x2* tb = (f32x2*)(smem + CH_TAB + n * 224);
    const f32x2 e1 = cexp(1.f, 0.f), e2 = cmul(e1, e1), e3 = cmul(e2, e1), e4 = cexp(4.f, 0.f), e6 = cmul(e3, e3);
    const f32x2 e8 = cexp(8.f, 0.f), e16 = cmul(e8, e8), e24 = cmul(e16, e8);
    const f32x2 e32 = cexp(32.f, 0.f), e64 = cmul(e32, e32), e96 = cmul(e64, e32);
    const f32x2 e128 = cexp(128.f, 0.f), e256 = cexp(256.f, 0.f), e384 = cmul(e256, e128);
    const f32x2 e512 = cexp(512.f, 0.f), e1024 = cexp(1024.f, 0.f);
    const float sy2 = __builtin_amdgcn_exp2f(-sY);
    const float cs = __builtin_amdgcn_exp2f(sY - G + 14.f);
    const f32x2 c0 = {cr * cs, ci * cs};
    *(f32x4*)(tb + 0) = f32x4{1.f, 0.f, e8[0], e8[1]};      *(f32x4*)(tb + 2) = f32x4{e16[0], e16[1], e24[0], e24[1]};
    *(f32x4*)(tb + 4) = f32x4{sy2, 0.f, e32[0] * sy2, e32[1] * sy2}; *(f32x4*)(tb + 6) = f32x4{e64[0] * sy2, e64[1] * sy2, e96[0] * sy2, e96[1] * sy2};
    const f32x2 p1 = cmul(c0, e4), p2 = cmul(c0, e256), p3 = cmul(c0, e512);
    *(f32x4*)(tb + 8) = f32x4{c0[0], c0[1], p1[0], p1[1]};  *(f32x4*)(tb + 10) = f32x4{p2[0], p2[1], p3[0], p3[1]};
    *(f32x4*)(tb + 12) = f32x4{1.f, 0.f, e1[0], e1[1]};     *(f32x4*)(tb + 14) = f32x4{e2[0], e2[1], e3[0], e3[1]};
    *(f32x4*)(tb + 16) = f32x4{1.f, 0.f, e2[0], e2[1]};     *(f32x4*)(tb + 18) = f32x4{e4[0], e4[1], e6[0], e6[1]};
    *(f32x4*)(tb + 20) = f32x4{1.f, 0.f, e4[0], e4[1]};     *(f32x4*)(tb + 22) = f32x4{e512[0], e512[1], e1024[0], e1024[1]};
    *(f32x4*)(tb + 24) = f32x4{e128[0], e128[1], e256[0], e256[1]}; *(f32x4*)(tb + 26) = f32x4{e384[0], e384[1], 0.f, 0.f};
    const f32x2 e2048 = cexp(2048.f, 14.f - G);
    float t2048 = cr * e2048[0] - ci * e2048[1];
    t2048 = wave_sum(t2048);
    if (lane == 0) { red[w] = G; red[4 + w] = t2048; }
  }
  __builtin_amdgcn_fence(__ATOMIC_RELEASE, "wavefront");
  __builtin_amdgcn_wave_barrier();
  TR(a.ws, 1);
  const u64 tag = ep + TAG_INC;
  const char* flg = a.ws + OFF_SYNC + SY_LN + (m & 7) * 256 + (lane & 31) * 8;
  u64 fl = 0;
  bool have = (wv != 0);
  const int c = lane & 15, g = lane >> 4;
  auto load_x = [&]() {
#pragma unroll
    for (int i = 0; i < 8; ++i) glds16<true>(lds0 + CH_ST + i * 1024, lane * 16 + i * 1024, a.ws + OFF_ST);
#pragma unroll
    for (int i = 0; i < 4; ++i) {
      glds16<true>(lds0 + CH_SX + i * 1024, lane * 16 + i * 1024, a.ws + OFF_XT + (size_t)(2 * m) * LP * 2);
      glds16<true>(lds0 + CH_BYTES + CH_SX + i * 1024, lane * 16 + i * 1024, a.ws + OFF_XT + (size_t)(2 * m + 1) * LP * 2);
    }
  };
  const int r = c;
  f32x4 acc[4];
#pragma unroll
  for (int k = 0; k < 4; ++k) acc[k] = f32x4{0.f, 0.f, 0.f, 0.f};
  {
    const char* tb = smem + CH_TAB + (64 * w + 4 * g) * 224;
    const char* pA = tb + (c & 3) * 8;
    const char* pB = tb + 32 + (c >> 2) * 8;
    const char* pP = tb + 64 + (r >> 2) * 8;
    const char* pQ = tb + 96 + ((r < 8 ? 0 : r < 12 ? 4 : 8) + (r & 3)) * 8;
    const char* pE = tb + 192;
    f32x2 tA[2][4], tB[2][4], tP[2][4], tQ[2][4], tE3[2][4];
    f32x4 tE[2][4];
    auto ldtab = [&](auto sc_) {
      constexpr int s = decltype(sc_)::value;
#pragma unroll
      for (int i = 0; i < 4; ++i) {
        const int o = (16 * s + i) * 224;
        tA[s & 1][i] = *(const f32x2*)(pA + o); tB[s & 1][i] = *(const f32x2*)(pB + o);
        tP[s & 1][i] = *(const f32x2*)(pP + o); tQ[s & 1][i] = *(const f32x2*)(pQ + o);
        tE[s & 1][i] = *(const f32x4*)(pE + o); tE3[s & 1][i] = *(const f32x2*)(pE + o + 16);
      }
    };
    auto kstep = [&](auto sc_) {
      constexpr int s = decltype(sc_)::value;
      if constexpr (s < 3) ldtab(IC<s + 1>{});
      __builtin_amdgcn_sched_barrier(0);
      u32x4 xf, y0, y1, y2, y3;
#pragma unroll
      for (int i = 0; i < 4; ++i) {
        const f32x2 yv = cmul(tA[s & 1][i], tB[s & 1][i]);
        const f32x2 xv = cmul_conj(tP[s & 1][i], tQ[s & 1][i]);
        y0[i] = pk_f16(yv[0], yv[1]);
        xf[i] = pk_f16(xv[0], xv[1]);
        const f32x4 E12 = tE[s & 1][i];
        const f32x2 v1 = cmul(yv, f32x2{E12[0], E12[1]}), v2 = cmul(yv, f32x2{E12[2], E12[3]}), v3 = cmul(yv, tE3[s & 1][i]);
        y1[i] = pk_f16(v1[0], v1[1]); y2[i] = pk_f16(v2[0], v2[1]); y3[i] = pk_f16(v3[0], v3[1]);
      }
      f16x8 xa = __builtin_bit_cast(f16x8, xf);
      __builtin_amdgcn_sched_barrier(0);
      acc[0] = __builtin_amdgcn_mfma_f32_16x16x32_f16(xa, __builtin_bit_cast(f16x8, y0), acc[0], 0, 0, 0);
      acc[1] = __builtin_amdgcn_mfma_f32_16x16x32_f16(xa, __builtin_bit_cast(f16x8, y1), acc[1], 0, 0, 0);
      acc[2] = __builtin_amdgcn_mfma_f32_16x16x32_f16(xa, __builtin_bit_cast(f16x8, y2), acc[2], 0, 0, 0);
      acc[3] = __builtin_amdgcn_mfma_f32_16x16x32_f16(xa, __builtin_bit_cast(f16x8, y3), acc[3], 0, 0, 0);
      __builtin_amdgcn_sched_barrier(0);
    };
    ldtab(IC<0>{});
    static_for<0, 4>(kstep);
  }
  if (wv == 0) fl = ld_agent(flg);
  TR(a.ws, 2);
  float* part = (float*)(smem + CH_TAB);
#pragma unroll
  for (int k = 0; k < 4; ++k) *(f32x4*)(part + w * 3584 + (16 * k + r) * 16 + 4 * g) = acc[k];
  __syncthreads();
  float* Fv = (float*)(smem + CH_F);
  const float Gm = fmaxf(fmaxf(red[0], red[1]), fmaxf(red[2], red[3]));
  float sc[4];
#pragma unroll
  for (int k = 0; k < 4; ++k) sc[k] = __builtin_amdgcn_exp2f(red[k] - Gm - 8.f);
  {
    const f32x4* pp = (const f32x4*)part;
    f32x4 sv = pp[tid] * sc[0] + pp[896 + tid] * sc[1] + pp[1792 + tid] * sc[2] + pp[2688 + tid] * sc[3];
    *(f32x4*)(Fv + tid * 4) = sv;
    if (tid == 0) { Fv[1024] = red[4] * sc[0] + red[5] * sc[1] + red[6] * sc[2] + red[7] * sc[3]; Fv[1025] = 0.f; }
  }
  const float kinv = __builtin_amdgcn_exp2f(Gm - 6.f);
  if (wv == 0) {
    if (__all(fl == tag)) { have = true; load_x(); }
    else fl = ld_agent(flg);
  }
  __syncthreads();
  TR(a.ws, 3);
  {
    u32 e[8];
#pragma unroll
    for (int k = 0; k < 8; ++k)
      e[k] = pk_f16(*(const float*)((const char*)Fv + (slot2[k] & 0xffffu)), *(const float*)((const char*)Fv + (slot2[k] >> 16)));
    auto put = [&](int ch, const u32 (&ee)[8]) {
      u32 o[7];
#pragma unroll
      for (int i = 0; i < 7; ++i) o[i] = __builtin_amdgcn_alignbyte(ee[i + 1], ee[i], 2);
#pragma unroll
      for (int s = 0; s < 4; ++s) {
        *(u32x4*)(smem + (2 * s) * CPS + ch * 16) = u32x4{ee[s], ee[s + 1], ee[s + 2], ee[s + 3]};
        *(u32x4*)(smem + (2 * s + 1) * CPS + ch * 16) = u32x4{o[s], o[s + 1], o[s + 2], o[s + 3]};
      }
    };
    put(tid, e);
    if (tid == 255) { const u32 z[8] = {0, 0, 0, 0, 0, 0, 0, 0}; put(256, z); }
  }
  u16* su = (u16*)(smem + CH_SU);
  const u16* sx = (const u16*)(smem + CH_SX);
  if (wv == 0) {
    if (!have) {
      unsigned spins = 0;
      while (!__all(fl == tag)) { __builtin_amdgcn_s_sleep(2); fl = ld_agent(flg); if (++spins > SPIN_CAP) break; }
      load_x();
    }
    asm volatile("s_waitcnt vmcnt(0)" ::: "memory");
  }
  __syncthreads();
  TR(a.ws, 4);
  {
    const u32x4 xr = *(const u32x4*)(sx + tid * 8);
    const u32x4 sa = *(const u32x4*)(smem_all + CH_ST + tid * 32), sb = *(const u32x4*)(smem_all + CH_ST + tid * 32 + 16);
    u32x4 o;
#pragma unroll
    for (int k = 0; k < 4; ++k) {
      const u32 s0 = k < 2 ? sa[2 * k] : sb[2 * k - 4], s1 = k < 2 ? sa[2 * k + 1] : sb[2 * k - 3];
      const u32 dpk = pk_bf16(bflo(xr[k]) - bflo(s0), bfhi(xr[k]) - bflo(s1));
      const u32 upk = pk_bf16(bflo(dpk) * __builtin_amdgcn_rcpf(bfhi(s0)), bfhi(dpk) * __builtin_amdgcn_rcpf(bfhi(s1)));
      o[k] = pk_f16(bflo(upk), bfhi(upk));
    }
    *(u32x4*)(su + 256 + tid * 8) = o;
    if (tid < 32) *(u32x4*)(su + tid * 8) = u32x4{0, 0, 0, 0};
  }
  __syncthreads();
  TR(a.ws, 5);
  f32x4 cv[8];
#pragma unroll
  for (int q = 0; q < 8; ++q) cv[q] = f32x4{0.f, 0.f, 0.f, 0.f};
  const int sig = (7 - c) & 7;
  const char* abase = smem + sig * CPS + 16 * ((c < 8 ? 253 : 252) + g);
#pragma unroll
  for (int rr2 = 0; rr2 < 2; ++rr2) {
    const int rs2 = 2 * w + rr2;
    f16x8 B[8];
#pragma unroll
    for (int q = 0; q < 8; ++q) B[q] = *(const f16x8*)(su + 240 + 256 * q - 32 * rs2 + 16 * c + 8 * g);
    f16x8 A[8];
#pragma unroll
    for (int k = 0; k < 8; ++k) A[k] = *(const f16x8*)(abase - 64 * (rs2 + 8 * k));
#pragma unroll
    for (int k = 0; k < 8; ++k)
#pragma unroll
      for (int q = k; q < 8; ++q) cv[q] = __builtin_amdgcn_mfma_f32_16x16x32_f16(A[k], B[q - k], cv[q], 0, 0, 0);
  }
  TR(a.ws, 6);
  __syncthreads();
  f32x4* rb = (f32x4*)smem;
#pragma unroll
  for (int q = 0; q < 8; ++q)
    if ((q >> 1) != w) rb[(w * 8 + q) * 64 + lane] = cv[q];
  __syncthreads();
  const float dsk = bf2f(r_dsk);
  const rsrc_t rs = ws_rsrc(a.ws);
#pragma unroll
  for (int i = 0; i < 2; ++i) {
    f32x4 y = cv[0];
#pragma unroll
    for (int q = 0; q < 8; ++q) if (q == 2 * w + i) y = cv[q];
    const int q = 2 * w + i;
#pragma unroll
    for (int s = 1; s < 4; ++s) y += rb[(((w + s) & 3) * 8 + q) * 64 + lane];
    const int t = 256 * q + 16 * c + 4 * g;
    u32x2 uu = *(const u32x2*)(su + 256 + t);
    const u32x2 xx = *(const u32x2*)(sx + t);
    f16x2 ua = as_f16x2(uu[0]), ub = as_f16x2(uu[1]);
    float uf[4] = {(float)ua[0], (float)ua[1], (float)ub[0], (float)ub[1]};
    u32 y01 = pk_bf16(y[0] * kinv, y[1] * kinv), y23 = pk_bf16(y[2] * kinv, y[3] * kinv);
    u32 s01 = pk_bf16(uf[0] * dsk, uf[1] * dsk), s23 = pk_bf16(uf[2] * dsk, uf[3] * dsk);
    u32 r01 = pk_bf16(bflo(y01) + bflo(s01), bfhi(y01) + bfhi(s01));
    u32 r23 = pk_bf16(bflo(y23) + bflo(s23), bfhi(y23) + bfhi(s23));
    u32 h01 = pk_bf16(bflo(xx[0]) + bflo(r01), bfhi(xx[0]) + bfhi(r01));
    u32 h23 = pk_bf16(bflo(xx[1]) + bflo(r23), bfhi(xx[1]) + bfhi(r23));
    __builtin_amdgcn_raw_buffer_store_b64(u32x2{h01, h23}, rs, (u32)(OFF_HT + (size_t)d * LP * 2 + t * 2), 0, 16);
  }
  if (threadIdx.x == 0) { *(volatile u32*)(smem_all + LDS_SIG) = 0u; *(volatile u32*)(smem_all + LDS_SIG + 4) = 0u; *(volatile u64*)(smem_all + LDS_SIG + 8) = tag; }
  asm volatile("s_waitcnt vmcnt(0) lgkmcnt(0)" ::: "memory");
  __syncthreads();
  if (threadIdx.x < 8) st_agent(a.ws + OFF_SYNC + SY_CV + threadIdx.x * 2048 + m * 8, tag);
  TR(a.ws, 7);
  return *(volatile u64*)(smem_all + LDS_SIG + 8);
}

static constexpr int G1_R = 8;
static constexpr int G1_STG = 16384;
static constexpr int G1_ST = G1_R * G1_STG;

constexpr int g1_cnt(int n) {
  if (n < 0) return 0;
  const int ks = n >> 2, c = n & 3;
  return (n + 4 < 56 ? 2 : 0) + ((c < 2 && ks + 1 < 14) ? 4 : 0);
}
constexpr int g1_wait(int n) {
  const int c = n & 3;
  const int breq = (g1_cnt(n - 4) - 2) + g1_cnt(n - 3) + g1_cnt(n - 2) + g1_cnt(n - 1);
  const int areq = g1_cnt(n - 2) + g1_cnt(n - 1);
  return (c == 0 && areq < breq) ? areq : breq;
}

DI void phase_b(const Args& a, int m, char* smem, u64 tag) {
  const int tid = threadIdx.x, lane = tid & 63;
  const int wv = __builtin_amdgcn_readfirstlane(tid >> 6);
  const int w = wv & 3;
  const int xcd = m & 7, bi = m >> 3;
  const int tb = 2 * xcd + bi / 14, fb = bi % 14;
  const int c16 = lane & 15, g = lane >> 4, q = (lane & 15) >> 2, p = lane & 3;
  const u32 lds0 = (u32)(size_t)(LDS3 char*)smem;
  f32x2* st = (f32x2*)(smem + G1_ST);
  char* ws = a.ws;

  if (wv >= 4) {
    u32 vh[2], vw[2];
#pragma unroll
    for (int k = 0; k < 2; ++k) {
      const int row = 8 * w + 4 * k + g;
      vh[k] = (u32)(row * LP * 2) + (u32)((c16 ^ swz(row)) * 16);
      vw[k] = (u32)(row * F * 2) + (u32)((c16 ^ swz(row)) * 16);
    }
    const char* hb = ws + OFF_HT + tb * 256;
    const char* wb = (const char*)a.w1 + fb * 256;
    auto ldw = [&](auto kc) {
      constexpr int ks = decltype(kc)::value;
      constexpr int sl = (ks % G1_R) * G1_STG + 8192;
      glds16<false>(lds0 + sl + (8 * w) * 256, vw[0], wb + (size_t)ks * 32 * F * 2);
      glds16<false>(lds0 + sl + (8 * w + 4) * 256, vw[1], wb + (size_t)ks * 32 * F * 2);
    };
    auto ldh = [&](auto kc) {
      constexpr int ks = decltype(kc)::value;
      constexpr int sl = (ks % G1_R) * G1_STG;
      glds16<true>(lds0 + sl + (8 * w) * 256, vh[0], hb + (size_t)ks * 32 * LP * 2);
      glds16<true>(lds0 + sl + (8 * w + 4) * 256, vh[1], hb + (size_t)ks * 32 * LP * 2);
    };
    static_for<0, G1_R>(ldw);
    loaders_wait<4>(smem, 0, w, ws + OFF_SYNC + SY_CV + xcd * 2048, NMAIN, tag);
    TRW(ws, 8, tid == 256);
    static_for<0, G1_R>(ldh);
    auto stepl = [&](auto kc) {
      constexpr int ks = decltype(kc)::value;
      constexpr int refills = ks > 3 ? (ks - 3 < 14 - G1_R ? ks - 3 : 14 - G1_R) : 0;
      constexpr int need_after = ks < G1_R ? (2 * (G1_R - 1 - ks) + 4 * refills) : (4 * (G1_R + refills - 1 - ks));
      wait_vm<need_after>();
      bar();
      if constexpr (ks >= 3 && ks - 3 + G1_R < 14) { ldh(IC<ks - 3 + G1_R>{}); ldw(IC<ks - 3 + G1_R>{}); }
    };
    static_for<0, 14>(stepl);
    if (w == 0) wait_flags<1>(ws + OFF_SYNC + SY_W1S, NAUX, tag);
    bar();
    bar();
    bar();
    return;
  }
  const int fi = w >> 1, ti = w & 1;
  f32x4 acc[4][4];
  float s1[2] = {0.f, 0.f}, s2[2] = {0.f, 0.f};
  const bf16x2 ones = __builtin_bit_cast(bf16x2, 0x3f803f80u);
  u32 adB[2][4], adA[2][4], adBh[2][4], adAh[2][4];
#pragma unroll
  for (int t = 0; t < 2; ++t) {
    const int row = 8 * g + 4 * t + q;
    const u32 a0 = lds0 + (u32)(row * 256) + (((u32)(p >> 1) ^ swz(row)) << 4) + 8 * (p & 1);
#pragma unroll
    for (int c = 0; c < 4; ++c) {
      adB[t][c] = a0 ^ (u32)(32 * (4 * ti + ((2 * fi + c) & 3)));
      adA[t][c] = (a0 ^ (u32)(32 * (4 * fi + c))) + 8192;
      adBh[t][c] = adB[t][c] + 65536u; adAh[t][c] = adA[t][c] + 65536u;
    }
  }
  u32x2 B[8][2], A[2][4][2];
  auto rdB = [&](auto nc, auto tc) {
    constexpr int n = decltype(nc)::value, t = decltype(tc)::value, sl = (n >> 2) % G1_R;
    trread<(sl & 3) * G1_STG>(B[n & 7][t], sl < 4 ? adB[t][n & 3] : adBh[t][n & 3]);
  };
  auto rdA = [&](auto kc, auto ic, auto tc) {
    constexpr int ks = decltype(kc)::value, i = decltype(ic)::value, t = decltype(tc)::value, sl = ks % G1_R;
    trread<(sl & 3) * G1_STG>(A[ks & 1][i][t], sl < 4 ? adA[t][i] : adAh[t][i]);
  };
  auto group = [&](auto nc) {
    constexpr int n = decltype(nc)::value;
    constexpr int ks = n >> 2, c = n & 3, sl = n & 7;
    constexpr bool nb = n + 4 < 56, na = (c < 2) && (ks + 1 < 14);
    constexpr int wt = n < 4 ? (n == 0 ? 6 : n == 1 ? 10 : n == 2 ? 14 : 14) : g1_wait(n);
    asm volatile("s_waitcnt lgkmcnt(%10)" : "+v"(B[sl][0]), "+v"(B[sl][1]), "+v"(A[ks & 1][0][0]), "+v"(A[ks & 1][0][1]), "+v"(A[ks & 1][1][0]), "+v"(A[ks & 1][1][1]),
                 "+v"(A[ks & 1][2][0]), "+v"(A[ks & 1][2][1]), "+v"(A[ks & 1][3][0]), "+v"(A[ks & 1][3][1]) : "n"(wt) : "memory");
    const u32x4 bfr = cat(B[sl][0], B[sl][1]);
    mfma_bf16<ks == 0>(acc[0][c], cat(A[ks & 1][0][0], A[ks & 1][0][1]), bfr);
    __builtin_amdgcn_sched_barrier(0);
    if constexpr (c == 0 && ks + 1 < 14) bar();
    if constexpr (nb) rdB(IC<n + 4>{}, IC<0>{});
    __builtin_amdgcn_sched_barrier(0);
    mfma_bf16<ks == 0>(acc[1][c], cat(A[ks & 1][1][0], A[ks & 1][1][1]), bfr);
    __builtin_amdgcn_sched_barrier(0);
    if constexpr (nb) rdB(IC<n + 4>{}, IC<1>{});
    if constexpr (na) rdA(IC<ks + 1>{}, IC<2 * c>{}, IC<0>{});
    __builtin_amdgcn_sched_barrier(0);
    mfma_bf16<ks == 0>(acc[2][c], cat(A[ks & 1][2][0], A[ks & 1][2][1]), bfr);
    __builtin_amdgcn_sched_barrier(0);
    if constexpr (na) { rdA(IC<ks + 1>{}, IC<2 * c>{}, IC<1>{}); rdA(IC<ks + 1>{}, IC<2 * c + 1>{}, IC<0>{}); }
    if constexpr (c < 2) {
#pragma unroll
      for (int k = 0; k < 2; ++k) {
        bf16x2 x2 = as_bf16x2(bfr[k]);
        s1[c] = __builtin_amdgcn_fdot2_f32_bf16(x2, ones, s1[c], false);
        s2[c] = __builtin_amdgcn_fdot2_f32_bf16(x2, x2, s2[c], false);
      }
    }
    __builtin_amdgcn_sched_barrier(0);
    mfma_bf16<ks == 0>(acc[3][c], cat(A[ks & 1][3][0], A[ks & 1][3][1]), bfr);
    __builtin_amdgcn_sched_barrier(0);
    if constexpr (na) rdA(IC<ks + 1>{}, IC<2 * c + 1>{}, IC<1>{});
    if constexpr (c < 2) {
#pragma unroll
      for (int k = 2; k < 4; ++k) {
        bf16x2 x2 = as_bf16x2(bfr[k]);
        s1[c] = __builtin_amdgcn_fdot2_f32_bf16(x2, ones, s1[c], false);
        s2[c] = __builtin_amdgcn_fdot2_f32_bf16(x2, x2, s2[c], false);
      }
      if (ks == fb) {
        const int tt = 4 * ti + ((2 * fi + c) & 3);
        __builtin_amdgcn_raw_buffer_store_b128(bfr, ws_rsrc(ws), (u32)(OFF_HN + ((size_t)(tb * 128 + 16 * tt + c16) * D + 32 * fb + 8 * g) * 2), 0, 16);
      }
    }
    __builtin_amdgcn_sched_barrier(0);
  };
  bar();
  static_for<0, 4>([&](auto ic) { rdA(IC<0>{}, ic, IC<0>{}); rdA(IC<0>{}, ic, IC<1>{}); });
  rdB(IC<0>{}, IC<0>{}); rdB(IC<0>{}, IC<1>{}); rdB(IC<1>{}, IC<0>{}); rdB(IC<1>{}, IC<1>{});
  asm volatile("s_waitcnt lgkmcnt(4)" ::: "memory");
  rdB(IC<2>{}, IC<0>{}); rdB(IC<2>{}, IC<1>{}); rdB(IC<3>{}, IC<0>{}); rdB(IC<3>{}, IC<1>{});
  static_for<0, 56>(group);
  asm volatile("s_nop 7\n\ts_nop 7" ::: "memory");
  TR(ws, 9);
  bar();
  f32x4 wsv[4];
#pragma unroll
  for (int i = 0; i < 4; ++i)
    wsv[i] = __builtin_bit_cast(f32x4, __builtin_amdgcn_raw_buffer_load_b128(ws_rsrc(ws), (u32)(OFF_W1S + (fb * 128 + 64 * fi + 4 * g + 16 * i) * 4), 0, 16));
#pragma unroll
  for (int i = 0; i < 2; ++i) {
    float sa = s1[i], sb = s2[i];
    sa += __shfl_xor(sa, 16); sa += __shfl_xor(sa, 32);
    sb += __shfl_xor(sb, 16); sb += __shfl_xor(sb, 32);
    const float mean = sa * (1.0f / D);
    const float var = fmaxf(sb * (1.0f / D) - mean * mean, 0.f);
    const float mu = rbf(mean);
    const float sd = rbf(__builtin_amdgcn_sqrtf(rbf(rbf(var) + 1e-5f)));
    const int tt = 4 * ti + ((2 * fi + i) & 3);
    if (lane < 16) st[tt * 16 + lane] = f32x2{mu, __builtin_amdgcn_rcpf(sd)};
  }
  lds_bar();
#pragma unroll
  for (int c = 0; c < 4; ++c) {
    const int tt = 4 * ti + ((2 * fi + c) & 3);
    const f32x2 ms = st[tt * 16 + c16];
#pragma unroll
    for (int pp = 0; pp < 2; ++pp) {
      float v0[4], v1[4];
#pragma unroll
      for (int k = 0; k < 4; ++k) {
        v0[k] = fmaxf((acc[2 * pp][c][k] - ms[0] * wsv[2 * pp][k]) * ms[1], 0.f);
        v1[k] = fmaxf((acc[2 * pp + 1][c][k] - ms[0] * wsv[2 * pp + 1][k]) * ms[1], 0.f);
      }
      u32x4 o = {pk_bf16(v0[0], v0[1]), pk_bf16(v0[2], v0[3]), pk_bf16(v1[0], v1[1]), pk_bf16(v1[2], v1[3])};
      __builtin_amdgcn_raw_buffer_store_b128(o, ws_rsrc(ws), (u32)(OFF_H1F + (((size_t)(tb * 8 + tt) * 56 + fb * 4 + 2 * fi + pp) * 64 + lane) * 16), 0, 16);
    }
  }
  asm volatile("s_waitcnt vmcnt(0)" ::: "memory");
  bar();
  if (tid == 0) st_agent(ws + OFF_SYNC + SY_H1 + tb * 128 + fb * 8, tag);
  TR(ws, 10);
}

static constexpr int G2_R = 4;
static constexpr int G2_STG = 32768;
static constexpr int G2_RS = 272;

DI void phase_c(const Args& a, int m, char* smem, u64 tag) {
  const int tid = threadIdx.x, lane = tid & 63;
  const int wv = __builtin_amdgcn_readfirstlane(tid >> 6);
  const int w = wv & 3;
  const int xcd = m & 7, bi = m >> 3;
  const int tb = 2 * xcd + bi / 14, fb = bi % 14;
  const int t64 = 2 * tb + fb / 7, db = fb % 7;
  const u32 lds0 = (u32)(size_t)(LDS3 char*)smem;
  char* ws = a.ws;
  const int c16 = lane & 15, g = lane >> 4, q = (lane & 15) >> 2, p = lane & 3;
  if (wv >= 4) {
    const int rr = lane >> 3;
    u32 vw[4], vo[4];
#pragma unroll
    for (int i = 0; i < 4; ++i) {
      const int row = 8 * i + rr;
      vw[i] = (u32)(row * D * 2) + ((((u32)(lane & 7)) ^ (u32)(((row >> 1) & 3) << 1)) << 4);
      vo[i] = lane * 16 + i * 56 * 1024;
    }
    const char* wb = (const char*)a.w2 + ((size_t)(448 * w) * D + db * 64) * 2;
    const char* hb = ws + OFF_H1F + ((size_t)(t64 * 4) * 56 + 14 * w) * 1024;
    auto ldw = [&](auto kc) {
      constexpr int k = decltype(kc)::value;
      constexpr int sl = (k % G2_R) * G2_STG;
#pragma unroll
      for (int i = 0; i < 4; ++i) glds16<false>(lds0 + sl + w * 8192 + i * 1024, vw[i], wb + (size_t)k * 32 * D * 2);
    };
    auto ldh = [&](auto kc) {
      constexpr int k = decltype(kc)::value;
      constexpr int sl = (k % G2_R) * G2_STG;
#pragma unroll
      for (int i = 0; i < 4; ++i) glds16<true>(lds0 + sl + w * 8192 + 4096 + i * 1024, vo[i], hb + k * 1024);
    };
    static_for<0, G2_R>(ldw);
    loaders_wait<1>(smem, 1, w, ws + OFF_SYNC + SY_H1 + tb * 128, 14, tag);
    TRW(ws, 11, tid == 256);
    static_for<0, G2_R>(ldh);
    auto stepl = [&](auto kc) {
      constexpr int k = decltype(kc)::value;
      constexpr int refills = k > 1 ? (k - 1 < 14 - G2_R ? k - 1 : 14 - G2_R) : 0;
      constexpr int need_after = k < G2_R ? (4 * (G2_R - 1 - k) + 8 * refills) : (8 * (G2_R + refills - 1 - k));
      wait_vm<need_after>();
      bar();
      if constexpr (k >= 1 && k - 1 + G2_R < 14) { ldh(IC<k - 1 + G2_R>{}); ldw(IC<k - 1 + G2_R>{}); }
    };
    static_for<0, 14>(stepl);
    bar();
    bar();
    return;
  }
  u32x4 res[2];
  f32x4 acc[4][4];
  u32 adW[4];
  {
    const int row = 4 * g + q;
    const u32 f = (u32)((row >> 1) & 3);
    const u32 a0 = lds0 + w * 8192 + (u32)(row * 128) + (u32)((p >> 1) * 16 + 8 * (p & 1));
#pragma unroll
    for (int dl = 0; dl < 4; ++dl) adW[dl] = a0 + ((((u32)dl) ^ f) << 5);
  }
  const u32 adH = lds0 + w * 8192 + 4096 + lane * 16;
  u32x2 Wf[2][4][2];
  u32x4 Hf[2][4];
  auto rd = [&](auto kc, auto ic) {
    constexpr int k = decltype(kc)::value, i = decltype(ic)::value;
    constexpr int sl = (k % G2_R) * G2_STG;
    constexpr int off = (sl & 65535);
    constexpr u32 hi = (u32)(sl & ~65535);
    if constexpr (i < 8) trread<off + (i & 1) * 2048>(Wf[k & 1][i >> 1][i & 1], adW[i >> 1] + hi);
    else ldsread128<off + (i - 8) * 1024>(Hf[k & 1][i - 8], adH + hi);
  };
  auto step = [&](auto kc) {
    constexpr int k = decltype(kc)::value;
    asm volatile("s_waitcnt lgkmcnt(0)" : "+v"(Wf[k & 1][0][0]), "+v"(Wf[k & 1][0][1]), "+v"(Wf[k & 1][1][0]), "+v"(Wf[k & 1][1][1]), "+v"(Wf[k & 1][2][0]), "+v"(Wf[k & 1][2][1]),
                 "+v"(Wf[k & 1][3][0]), "+v"(Wf[k & 1][3][1]), "+v"(Hf[k & 1][0]), "+v"(Hf[k & 1][1]), "+v"(Hf[k & 1][2]), "+v"(Hf[k & 1][3]) :: "memory");
    if constexpr (k + 1 < 14) bar();
    static_for<0, 16>([&](auto jc) {
      constexpr int j = decltype(jc)::value, dl = j >> 2, ta = j & 3;
      mfma_bf16<k == 0>(acc[dl][ta], cat(Wf[k & 1][dl][0], Wf[k & 1][dl][1]), Hf[k & 1][ta]);
      __builtin_amdgcn_sched_barrier(0);
      if constexpr (k + 1 < 14 && j < 12) rd(IC<k + 1>{}, IC<j>{});
      __builtin_amdgcn_sched_barrier(0);
    });
  };
  bar();
  static_for<0, 12>([&](auto ic) { rd(IC<0>{}, ic); });
  {
    const rsrc_t rs = ws_rsrc(ws);
#pragma unroll
    for (int i = 0; i < 2; ++i)
      res[i] = __builtin_amdgcn_raw_buffer_load_b128(rs, (u32)(OFF_HN + ((size_t)(t64 * 64 + 32 * i + (tid >> 3)) * D + db * 64 + (tid & 7) * 8) * 2), 0, 16);
  }
  static_for<0, 14>(step);
  asm volatile("s_nop 7\n\ts_nop 7" ::: "memory");
  TR(ws, 12);
  bar();
#pragma unroll
  for (int dl = 0; dl < 4; ++dl)
#pragma unroll
    for (int ta = 0; ta < 4; ++ta)
      *(f32x4*)(smem + (w * 64 + 16 * ta + c16) * G2_RS + (16 * dl + 4 * g) * 4) = acc[dl][ta];
  lds_bar();
#pragma unroll
  for (int i = 0; i < 2; ++i) {
    const int t = (tid >> 3) + 32 * i, chn = tid & 7;
    const u32x4 rr = res[i];
    f32x4 s0 = {0.f, 0.f, 0.f, 0.f}, s1 = s0;
#pragma unroll
    for (int ww = 0; ww < 4; ++ww) {
      s0 += *(const f32x4*)(smem + (ww * 64 + t) * G2_RS + chn * 32);
      s1 += *(const f32x4*)(smem + (ww * 64 + t) * G2_RS + chn * 32 + 16);
    }
    u32 p0 = pk_bf16(s0[0], s0[1]), p1 = pk_bf16(s0[2], s0[3]), p2 = pk_bf16(s1[0], s1[1]), p3 = pk_bf16(s1[2], s1[3]);
    u32x4 o;
    o[0] = pk_bf16(bflo(rr[0]) + bflo(p0), bfhi(rr[0]) + bfhi(p0));
    o[1] = pk_bf16(bflo(rr[1]) + bflo(p1), bfhi(rr[1]) + bfhi(p1));
    o[2] = pk_bf16(bflo(rr[2]) + bflo(p2), bfhi(rr[2]) + bfhi(p2));
    o[3] = pk_bf16(bflo(rr[3]) + bflo(p3), bfhi(rr[3]) + bfhi(p3));
    *(u32x4*)(a.out + (size_t)(t64 * 64 + t) * D + db * 64 + chn * 8) = o;
  }
}

__global__ __launch_bounds__(512, 2) void s4_block_kernel(Args a) {
  extern __shared__ __attribute__((aligned(16))) char smem[];
  const int b = blockIdx.x;
  TR(a.ws, 0);
  if (b < NAUX) { aux_block(a, b, smem); TR(a.ws, 15); TRDUMP(a.ws); return; }
  const int m = b - NAUX;
  const u64 tag = phase_a(a, m, smem);
  phase_b(a, m, smem, tag);
  __syncthreads();
  phase_c(a, m, smem, tag);
  TR(a.ws, 15);
  TRDUMP(a.ws);
}

extern "C" void kernel_launch(void* const* d_in, const int* in_sizes, int n_in,
                              void* d_out, int out_size, void* d_ws, size_t ws_size,
                              hipStream_t stream) {
  Args a;
  a.x = (const u16*)d_in[0]; a.Are = (const u16*)d_in[1]; a.Aim = (const u16*)d_in[2];
  a.Bre = (const u16*)d_in[3]; a.Bim = (const u16*)d_in[4]; a.Cre = (const u16*)d_in[5];
  a.Cim = (const u16*)d_in[6]; a.logdt = (const u16*)d_in[7]; a.Dskip = (const u16*)d_in[8];
  a.w1 = (const u16*)d_in[10]; a.w2 = (const u16*)d_in[11]; a.ws = (char*)d_ws; a.out = (u16*)d_out; a.pad = nullptr;
  (void)hipFuncSetAttribute((const void*)s4_block_kernel, hipFuncAttributeMaxDynamicSharedMemorySize, SMEM_BYTES);
  s4_block_kernel<<<NAUX + NMAIN, 512, SMEM_BYTES, stream>>>(a);
}
```

```cpp
#define PF_CODE 0
#define PF_W 1
#define XSC1 true
#include <hip/hip_runtime.h>
#include <hip/hip_bf16.h>
#include <stdint.h>
#include <type_traits>

static constexpr int L = 2048, D = 448, NM = 256, F = 1792;
static constexpr int LP = 2176;
static constexpr size_t OFF_ST = 0;
static constexpr size_t OFF_XT = 16384;
static constexpr size_t OFF_HT = OFF_XT + (size_t)D * LP * 2;
static constexpr size_t OFF_W1S = OFF_HT + (size_t)D * LP * 2;
static constexpr size_t OFF_HN = OFF_W1S + 8192;
static constexpr size_t OFF_H1F = OFF_HN + (size_t)L * D * 2;
static constexpr size_t OFF_SYNC = OFF_H1F + (size_t)L * F * 2;
static constexpr size_t SY_EP = 0, SY_LN = 1024  , SY_W1S = 4096, SY_CV = 8192  , SY_H1 = 32768  ;
static constexpr size_t WS_USED = OFF_SYNC + 65536;

typedef unsigned short u16;
typedef unsigned int u32;
typedef unsigned long long u64;
typedef _Float16 f16;
typedef f16 f16x2 __attribute__((ext_vector_type(2)));
typedef f16 f16x8 __attribute__((ext_vector_type(8)));
typedef __bf16 bf16x2 __attribute__((ext_vector_type(2)));
typedef __bf16 bf16x8 __attribute__((ext_vector_type(8)));
typedef float f32x2 __attribute__((ext_vector_type(2)));
typedef float f32x4 __attribute__((ext_vector_type(4)));
typedef u32 u32x2 __attribute__((ext_vector_type(2)));
typedef u32 u32x4 __attribute__((ext_vector_type(4)));
typedef short s16x4 __attribute__((ext_vector_type(4)));
#define LDS3 __attribute__((address_space(3)))
typedef __attribute__((address_space(1))) u64 gu64;
typedef __amdgpu_buffer_rsrc_t rsrc_t;

#define DI __device__ __forceinline__
DI float bf2f(u16 v) { return __uint_as_float(((u32)v) << 16); }
DI float bflo(u32 v) { return __uint_as_float(v << 16); }
DI float bfhi(u32 v) { return __uint_as_float(v & 0xffff0000u); }
DI u32 pk_bf16(float a, float b) { f32x2 v = {a, b}; return __builtin_bit_cast(u32, __builtin_convertvector(v, bf16x2)); }
DI u32 pk_f16(float a, float b) { f32x2 v = {a, b}; return __builtin_bit_cast(u32, __builtin_convertvector(v, f16x2)); }
DI float rbf(float x) { return bflo(pk_bf16(x, 0.f)); }
DI f16x2 as_f16x2(u32 v) { return __builtin_bit_cast(f16x2, v); }
DI bf16x2 as_bf16x2(u32 v) { return __builtin_bit_cast(bf16x2, v); }
template <int CTRL, int RM> DI float dppf(float old, float v) {
  return __int_as_float(__builtin_amdgcn_update_dpp(__float_as_int(old), __float_as_int(v), CTRL, RM, 0xf, false));
}
DI float wave_sum(float v) {
  v += dppf<0x111, 0xf>(0.f, v); v += dppf<0x112, 0xf>(0.f, v); v += dppf<0x114, 0xf>(0.f, v); v += dppf<0x118, 0xf>(0.f, v);
  v += dppf<0x142, 0xa>(0.f, v); v += dppf<0x143, 0xc>(0.f, v);
  return __int_as_float(__builtin_amdgcn_readlane(__float_as_int(v), 63));
}
DI float wave_max(float v) {
  v = fmaxf(v, dppf<0x111, 0xf>(v, v)); v = fmaxf(v, dppf<0x112, 0xf>(v, v)); v = fmaxf(v, dppf<0x114, 0xf>(v, v)); v = fmaxf(v, dppf<0x118, 0xf>(v, v));
  v = fmaxf(v, dppf<0x142, 0xa>(v, v)); v = fmaxf(v, dppf<0x143, 0xc>(v, v));
  return __int_as_float(__builtin_amdgcn_readlane(__float_as_int(v), 63));
}
DI u64 ld_agent(const void* p) { return __hip_atomic_load((const gu64*)p, __ATOMIC_RELAXED, __HIP_MEMORY_SCOPE_AGENT); }
DI void st_agent(void* p, u64 v) { __hip_atomic_store((gu64*)p, v, __ATOMIC_RELAXED, __HIP_MEMORY_SCOPE_AGENT); }
static constexpr u64 TAG_INC = 0x9E3779B97F4A7C15ull;
DI rsrc_t ws_rsrc(char* ws) { return __builtin_amdgcn_make_buffer_rsrc(ws, 0, (int)WS_USED, 0x00020000); }
template <int B, int E, class Fn> DI void static_for(Fn&& f) {
  if constexpr (B < E) { f(std::integral_constant<int, B>{}); static_for<B + 1, E>(f); }
}
template <int N> using IC = std::integral_constant<int, N>;
DI s16x4 tr_read(const void* p) { return __builtin_amdgcn_ds_read_tr16_b64_v4i16((LDS3 s16x4*)p); }
template <int N> DI void wait_vm() { asm volatile("s_waitcnt vmcnt(%0)" :: "n"(N) : "memory"); }
DI void bar() { __builtin_amdgcn_s_barrier(); }
DI bool vm_idle_unused() {
  u32 v; asm volatile("s_getreg_b32 %0, hwreg(HW_REG_IB_STS)" : "=s"(v));
  return ((v & 0xfu) | ((v >> 22) & 0x3u)) == 0u;
}
DI void lds_bar() { asm volatile("s_waitcnt lgkmcnt(0)" ::: "memory"); __builtin_amdgcn_s_barrier(); }
template <bool SC1> DI void glds16(u32 lds_dst, u32 voff, const void* sbase) {
  u32 keep;
  if constexpr (SC1)
    asm volatile("s_mov_b32 %0, m0\n\ts_mov_b32 m0, %1\n\ts_nop 0\n\tglobal_load_lds_dwordx4 %2, %3 sc1\n\ts_mov_b32 m0, %0"
                 : "=&s"(keep) : "s"(lds_dst), "v"(voff), "s"(sbase) : "memory");
  else
    asm volatile("s_mov_b32 %0, m0\n\ts_mov_b32 m0, %1\n\ts_nop 0\n\tglobal_load_lds_dwordx4 %2, %3\n\ts_mov_b32 m0, %0"
                 : "=&s"(keep) : "s"(lds_dst), "v"(voff), "s"(sbase) : "memory");
}
template <int OFF> DI void trread(u32x2& dst, u32 addr) {
  asm volatile("ds_read_b64_tr_b16 %0, %1 offset:%2" : "=v"(dst) : "v"(addr), "n"(OFF) : "memory");
}
template <int OFF> DI void ldsread128(u32x4& dst, u32 addr) {
  asm volatile("ds_read_b128 %0, %1 offset:%2" : "=v"(dst) : "v"(addr), "n"(OFF) : "memory");
}
template <bool FIRST, bool NOP> DI void mfma_bf16(f32x4& acc, const u32x4& a, const u32x4& b) {
  if constexpr (NOP) {
    if constexpr (FIRST) asm volatile("s_nop 1\n\tv_mfma_f32_16x16x32_bf16 %0, %1, %2, 0" : "=&v"(acc) : "v"(a), "v"(b));
    else asm volatile("s_nop 1\n\tv_mfma_f32_16x16x32_bf16 %0, %1, %2, %0" : "+v"(acc) : "v"(a), "v"(b));
  } else {
    if constexpr (FIRST) asm volatile("v_mfma_f32_16x16x32_bf16 %0, %1, %2, 0" : "=&v"(acc) : "v"(a), "v"(b));
    else asm volatile("v_mfma_f32_16x16x32_bf16 %0, %1, %2, %0" : "+v"(acc) : "v"(a), "v"(b));
  }
}
DI u32x4 cat(const u32x2& a, const u32x2& b) { return u32x4{a[0], a[1], b[0], b[1]}; }
DI u32 swz(int row) { return (u32)(((row & 3) << 2) | ((row >> 2) & 3)); }
DI f32x2 cmul(f32x2 y, f32x2 e) {
  f32x2 t, r;
  asm("v_pk_mul_f32 %0, %1, %2 op_sel_hi:[1,0]" : "=v"(t) : "v"(y), "v"(e));
  asm("v_pk_fma_f32 %0, %1, %2, %3 op_sel:[1,1,0] op_sel_hi:[0,1,1] neg_lo:[0,1,0]" : "=v"(r) : "v"(y), "v"(e), "v"(t));
  return r;
}
DI f32x2 cmul_conj(f32x2 p, f32x2 q) {
  f32x2 t, r;
  asm("v_pk_mul_f32 %0, %1, %2 op_sel_hi:[1,0] neg_hi:[1,0]" : "=v"(t) : "v"(p), "v"(q));
  asm("v_pk_fma_f32 %0, %1, %2, %3 op_sel:[1,1,0] op_sel_hi:[0,1,1] neg_lo:[1,0,0] neg_hi:[1,0,0]" : "=v"(r) : "v"(p), "v"(q), "v"(t));
  return r;
}

#define TR(ws, slot) do {} while (0)
#define TRW(ws, slot, cond) do {} while (0)
#define TRDUMP(ws) do {} while (0)

struct Args {
  const u16 *x, *Are, *Aim, *Bre, *Bim, *Cre, *Cim, *logdt, *Dskip, *w1, *w2;
  char* ws;
  u16* out;
  const void* pad;
};

constexpr int rne_bf16_int(int l) {
  if (l < 256) return l;
  int sh = 0; while ((l >> sh) >= 256) ++sh;
  const int q = l >> sh, rem = l & ((1 << sh) - 1), half = 1 << (sh - 1);
  const int up = (rem > half) || (rem == half && (q & 1));
  return (q + up) << sh;
}
constexpr int kslot(int m) {
  const int l = 2047 - m;
  if (l < 0) return 1089;
  const int t = rne_bf16_int(l);
  if (t >= 2048) return 1088;
  const int seg = (t >= 256) + (t >= 512) + (t >= 1024);
  const int row = seg == 0 ? (t & 7) : seg == 1 ? 8 + ((t >> 1) & 3) : seg == 2 ? 12 + ((t >> 2) & 1) : 14 + ((t >> 9) & 1);
  const int b = (t >> 3) & (seg >= 2 ? 63 : 31);
  return b * 17 + row;
}
struct SlotTab { u16 v[2064]; };
constexpr SlotTab make_slot_tab() { SlotTab t{}; for (int m = 0; m < 2064; ++m) t.v[m] = (u16)(kslot(m) * 4); return t; }
__device__ __attribute__((aligned(16))) const SlotTab g_slot_tab = make_slot_tab();

static constexpr int NAUX = 32;
static constexpr int NMAIN = 224;
static constexpr int SMEM_BYTES = 160 * 1024;
static constexpr unsigned SPIN_CAP = 1u << 20;
static constexpr int LDS_SIG = SMEM_BYTES - 320;

template <int NPL> DI void wait_flags(const char* base, int n, u64 tag) {
  const int lane = threadIdx.x & 63;
  unsigned spins = 0;
  for (;;) {
    bool ok = true;
#pragma unroll
    for (int k = 0; k < NPL; ++k) { const int i = k * 64 + lane; if (i < n) ok &= (ld_agent(base + i * 8) == tag); }
    if (__all(ok)) break;
    __builtin_amdgcn_s_sleep(1);
    if (++spins > SPIN_CAP) break;
  }
}

template <int NPL> DI void loaders_wait(char* smem, int idx, int w, const char* base, int n, u64 tag) {
  volatile u32* sig = (volatile u32*)(smem + LDS_SIG) + idx;
  if (w == 0) {
    wait_flags<NPL>(base, n, tag);
    *sig = 1u;
  } else {
    unsigned spins = 0;
    while (*sig == 0u) { __builtin_amdgcn_s_sleep(1); if (++spins > (SPIN_CAP << 4)) break; }
  }
  asm volatile("" ::: "memory");
}

DI void l2_touch(const char* base, int kbytes, int part, int nparts, char* smem) {
  const int lane = threadIdx.x & 63;
  const int w = __builtin_amdgcn_readfirstlane(threadIdx.x >> 6);
  const u32 dst = (u32)(size_t)(LDS3 char*)smem + 65536 + w * 1024;
  const int per = (kbytes + nparts - 1) / nparts;
  const int k0 = per * part, k1 = (k0 + per < kbytes) ? k0 + per : kbytes;
  int cnt = 0;
  for (int k = k0 + w; k < k1; k += 8) {
    glds16<false>(dst, lane * 16, base + (size_t)k * 1024);
    if ((++cnt & 7) == 0) wait_vm<32>();
  }
  wait_vm<0>();
}
static constexpr int XT_RS = 144;
static constexpr int LDS_X8 = 124 * 1024;
struct Pro { u32x4 row, tile; };
DI Pro pro_load(const Args& a, int b) {
  const int lane = threadIdx.x & 63, wv = threadIdx.x >> 6;
  Pro p; p.row = u32x4{0, 0, 0, 0}; p.tile = p.row;
  if (lane < 56) {
    p.tile = *(const u32x4*)(a.x + (size_t)(64 * (b >> 3) + 8 * wv + lane / 7) * D + 56 * (b & 7) + 8 * (lane % 7));
    p.row = *(const u32x4*)(a.x + (size_t)(8 * b + wv) * D + lane * 8);
  }
  return p;
}
DI u64 pro_store(const Args& a, int b, const Pro& p, u64 ep, char* smem) {
  const int lane = threadIdx.x & 63;
  const int wv = __builtin_amdgcn_readfirstlane(threadIdx.x >> 6);
  if (lane < 56) *(u32x4*)(smem + LDS_X8 + (8 * wv + lane / 7) * XT_RS + (lane % 7) * 16) = p.tile;
  if (threadIdx.x == 0) { *(volatile u32*)(smem + LDS_SIG) = 0u; *(volatile u32*)(smem + LDS_SIG + 4) = 0u; *(volatile u64*)(smem + LDS_SIG + 8) = ep + TAG_INC; }
  __syncthreads();
  const u64 tag = *(volatile u64*)(smem + LDS_SIG + 8);
  if (wv < 7) {
    const int g = lane >> 4, i16 = lane & 15, q = i16 >> 2, pp = lane & 3;
    const char* src = smem + LDS_X8 + (16 * g + 8 * (pp >> 1) + q) * XT_RS + (4 * (pp & 1)) * 2 + wv * 16;
    s16x4 lo = tr_read(src), hi = tr_read(src + 4 * XT_RS);
    __builtin_amdgcn_raw_buffer_store_b128(cat(__builtin_bit_cast(u32x2, lo), __builtin_bit_cast(u32x2, hi)), ws_rsrc(a.ws),
        (u32)(OFF_XT + (size_t)(56 * (b & 7) + 8 * wv + (i16 & 7)) * LP * 2 + (64 * (b >> 3) + 16 * g + 8 * (i16 >> 3)) * 2), 0, 16);
  }
  return tag;
}
DI void pro_stats(const Args& a, int b, const Pro& p, u64 tag) {
  const int lane = threadIdx.x & 63, wv = threadIdx.x >> 6;
  float s = 0.f, q2 = 0.f;
#pragma unroll
  for (int k = 0; k < 4; ++k) {
    const float lo = bflo(p.row[k]), hi = bfhi(p.row[k]);
    s += lo; s += hi; q2 = __builtin_fmaf(lo, lo, q2); q2 = __builtin_fmaf(hi, hi, q2);
  }
  const float mean = wave_sum(s) * (1.0f / D);
  const float var = fmaxf(wave_sum(q2) * (1.0f / D) - mean * mean, 0.f);
  const float sd = rbf(__builtin_amdgcn_sqrtf(rbf(rbf(var) + 1e-5f)));
  if (lane == 0) st_agent(a.ws + OFF_ST + (8 * b + wv) * 8, ((u64)(u32)tag << 32) | pk_bf16(mean, sd));
}
DI void pro_flag(const Args& a, int b, u64 tag) {
  if (threadIdx.x == 0) st_agent(a.ws + OFF_SYNC + SY_LN + (b & 7) * 256 + (b >> 3) * 8, tag);
}
DI void aux_block(const Args& a, int blk, char* smem) {
  const int tid = threadIdx.x, lane = tid & 63;
  const int w = __builtin_amdgcn_readfirstlane(tid >> 6);
  const rsrc_t rs = ws_rsrc(a.ws);
  const Pro pr = pro_load(a, blk);
  const u64 ep = ld_agent(w == 0 ? (const char*)(a.ws + OFF_SYNC + SY_EP) : (const char*)(a.x + (size_t)(8 * blk + w) * D));
  const u64 tag = pro_store(a, blk, pr, ep, smem);
  pro_stats(a, blk, pr, tag);
  asm volatile("s_waitcnt vmcnt(0)" ::: "memory");
  __syncthreads();
  pro_flag(a, blk, tag);
  TR(a.ws, 2);
  {
    float* red = (float*)smem;
    const int cp = tid % 28, rg = tid / 28;
    float s0 = 0.f, s1 = 0.f;
    if (rg < 16) {
      u32 x[28];
#pragma unroll
      for (int i = 0; i < 28; ++i) x[i] = *(const u32*)(a.w1 + (size_t)(rg + 16 * i) * F + 56 * blk + 2 * cp);
#pragma unroll
      for (int i = 0; i < 28; ++i) { s0 += bflo(x[i]); s1 += bfhi(x[i]); }
      red[rg * 56 + 2 * cp] = s0; red[rg * 56 + 2 * cp + 1] = s1;
    }
    __syncthreads();
    if (tid < 56) {
      float s = 0.f;
#pragma unroll
      for (int i = 0; i < 16; ++i) s += red[i * 56 + tid];
      __builtin_amdgcn_raw_buffer_store_b32(__float_as_uint(s), rs, (u32)(OFF_W1S + (56 * blk + tid) * 4), 0, 16);
    }
    asm volatile("s_waitcnt vmcnt(0)" ::: "memory");
    __syncthreads();
    if (tid == 0) st_agent(a.ws + OFF_SYNC + SY_W1S + blk * 8, tag);
  }
  l2_touch((const char*)a.w1, D * F * 2 / 1024, blk >> 3, 4, smem);
  l2_touch((const char*)a.w2, D * F * 2 / 1024, blk >> 3, 4, smem);
  if (blk == 0 && w == 0) {
    wait_flags<1>(a.ws + OFF_SYNC + SY_W1S, NAUX, tag);
    wait_flags<4>(a.ws + OFF_SYNC + SY_CV, NMAIN, tag);
    if (lane == 0) st_agent(a.ws + OFF_SYNC + SY_EP, tag);
  }
}

static constexpr int CPS = 4160;
static constexpr int CH_BYTES = 61440;
static constexpr int CH_TAB = 0;
static constexpr int CH_SU = 8 * CPS;
static constexpr int CH_F = CH_SU + 4608 + 256;
static constexpr int LDS_SX = 136 * 1024;
static constexpr int CH_RED = 59392;

DI u64 phase_a(const Args& a, int m, char* smem_all) {
  const int tid = threadIdx.x & 255, lane = threadIdx.x & 63;
  const int wv = __builtin_amdgcn_readfirstlane(threadIdx.x >> 6);
  const int w = wv & 3, hw = wv >> 2;
  const int d = 2 * m + hw;
  char* smem = smem_all + hw * CH_BYTES;
  const float LOG2E = 1.4426950408889634f, INV2PI = 0.15915494309189535f;
  const u32 lds0 = (u32)(size_t)(LDS3 char*)smem;
  const Pro pr = pro_load(a, m + NAUX);
  const u64 ep = ld_agent(wv == 0 ? (const char*)(a.ws + OFF_SYNC + SY_EP) : (const char*)(a.Are + d * NM + 4 * w));
  const int n = tid;
  const u16 r_are = a.Are[d * NM + n], r_aim = a.Aim[d * NM + n], r_bre = a.Bre[d * NM + n], r_bim = a.Bim[d * NM + n];
  const u16 r_cre = a.Cre[d * NM + n], r_cim = a.Cim[d * NM + n], r_ldt = a.logdt[d], r_dsk = a.Dskip[d];
  u32 slot2[8];
  {
    const u32x4 s0 = *(const u32x4*)(g_slot_tab.v + 8 * tid), s1 = *(const u32x4*)(g_slot_tab.v + 8 * tid + 8);
#pragma unroll
    for (int k = 0; k < 4; ++k) { slot2[k] = s0[k]; slot2[4 + k] = s1[k]; }
  }
  __builtin_amdgcn_sched_barrier(0);
  const u64 tag = pro_store(a, m + NAUX, pr, ep, smem_all);
  float* red = (float*)(smem + CH_RED);
  const float dt = rbf(expf(bf2f(r_ldt)));
  const float bre = dt * bf2f(r_bre), bim = dt * bf2f(r_bim);
  const float cre = bf2f(r_cre), cim = bf2f(r_cim);
  const float zr = (dt * bf2f(r_are)) * LOG2E, zi = (dt * bf2f(r_aim)) * INV2PI;
  const float cr = cre * bre - cim * bim, ci = cre * bim + cim * bre;
  const float mx = fmaxf(fabsf(cr), fabsf(ci));
  const float lc = mx > 1e-36f ? __builtin_amdgcn_logf(mx) + 0.5f : -1000.f;
  const float sY = fmaxf(0.f, zr * 504.f);
  const float gm = fmaxf(lc + fmaxf(0.f, zr * 1536.f) + sY, lc + zr * 2048.f);
  const float G = ceilf(wave_max(gm));
  auto cexp = [&](float t, float lg) -> f32x2 {
    const float mm = __builtin_amdgcn_exp2f(__builtin_fmaf(zr, t, lg)), f = __builtin_amdgcn_fractf(zi * t);
    return f32x2{mm * __builtin_amdgcn_cosf(f), mm * __builtin_amdgcn_sinf(f)};
  };
  {
    f32x2* tb = (f32x2*)(smem + CH_TAB + n * 224);
    const f32x2 e1 = cexp(1.f, 0.f), e2 = cmul(e1, e1), e3 = cmul(e2, e1), e4 = cexp(4.f, 0.f), e6 = cmul(e3, e3);
    const f32x2 e8 = cexp(8.f, 0.f), e16 = cmul(e8, e8), e24 = cmul(e16, e8);
    const f32x2 e32 = cexp(32.f, 0.f), e64 = cmul(e32, e32), e96 = cmul(e64, e32);
    const f32x2 e128 = cexp(128.f, 0.f), e256 = cexp(256.f, 0.f), e384 = cmul(e256, e128);
    const f32x2 e512 = cexp(512.f, 0.f), e1024 = cexp(1024.f, 0.f);
    const float sy2 = __builtin_amdgcn_exp2f(-sY);
    const float cs = __builtin_amdgcn_exp2f(sY - G + 14.f);
    const f32x2 c0 = {cr * cs, ci * cs};
    *(f32x4*)(tb + 0) = f32x4{1.f, 0.f, e8[0], e8[1]};      *(f32x4*)(tb + 2) = f32x4{e16[0], e16[1], e24[0], e24[1]};
    *(f32x4*)(tb + 4) = f32x4{sy2, 0.f, e32[0] * sy2, e32[1] * sy2}; *(f32x4*)(tb + 6) = f32x4{e64[0] * sy2, e64[1] * sy2, e96[0] * sy2, e96[1] * sy2};
    const f32x2 p1 = cmul(c0, e4), p2 = cmul(c0, e256), p3 = cmul(c0, e512);
    *(f32x4*)(tb + 8) = f32x4{c0[0], c0[1], p1[0], p1[1]};  *(f32x4*)(tb + 10) = f32x4{p2[0], p2[1], p3[0], p3[1]};
    *(f32x4*)(tb + 12) = f32x4{1.f, 0.f, e1[0], e1[1]};     *(f32x4*)(tb + 14) = f32x4{e2[0], e2[1], e3[0], e3[1]};
    *(f32x4*)(tb + 16) = f32x4{1.f, 0.f, e2[0], e2[1]};     *(f32x4*)(tb + 18) = f32x4{e4[0], e4[1], e6[0], e6[1]};
    *(f32x4*)(tb + 20) = f32x4{1.f, 0.f, e4[0], e4[1]};     *(f32x4*)(tb + 22) = f32x4{e512[0], e512[1], e1024[0], e1024[1]};
    *(f32x4*)(tb + 24) = f32x4{e128[0], e128[1], e256[0], e256[1]}; *(f32x4*)(tb + 26) = f32x4{e384[0], e384[1], 0.f, 0.f};
    const f32x2 e2048 = cexp(2048.f, 14.f - G);
    float t2048 = cr * e2048[0] - ci * e2048[1];
    t2048 = wave_sum(t2048);
    if (lane == 0) { red[w] = G; red[4 + w] = t2048; }
  }
  asm volatile("s_waitcnt vmcnt(0) lgkmcnt(0)" ::: "memory");
  __syncthreads();
  pro_flag(a, m + NAUX, tag);
  TR(a.ws, 1);
  pro_stats(a, m + NAUX, pr, tag);
  const int c = lane & 15, g = lane >> 4;
  const rsrc_t rs = ws_rsrc(a.ws);
  u32x4 gr[4];
  const char* flg = a.ws + OFF_SYNC + SY_LN + ((2 * m) / 56) * 256 + (lane & 31) * 8;
  u64 fl = 0;
  bool have = (wv != 0);
  auto load_x = [&]() {
    TR(a.ws, 13);
#pragma unroll
    for (int i = 0; i < 4; ++i) {
      glds16<XSC1>(lds0 + LDS_SX + i * 1024, lane * 16 + i * 1024, a.ws + OFF_XT + (size_t)(2 * m) * LP * 2);
      glds16<XSC1>(lds0 + LDS_SX + 4096 + i * 1024, lane * 16 + i * 1024, a.ws + OFF_XT + (size_t)(2 * m + 1) * LP * 2);
    }
  };
  auto poll = [&]() { if (wv == 0 && !have) fl = ld_agent(flg); };
  auto check = [&]() {
    if (wv == 0 && !have) {
      if (__all(fl == tag)) { have = true; load_x(); }
    }
  };
  auto load_st = [&]() {
#pragma unroll
    for (int k = 0; k < 4; ++k) gr[k] = __builtin_amdgcn_raw_buffer_load_b128(rs, (u32)(OFF_ST + tid * 64 + k * 16), 0, 16);
  };
  auto load_st_fast = [&]() {
#pragma unroll
    for (int k = 0; k < 4; ++k) gr[k] = __builtin_amdgcn_raw_buffer_load_b128(rs, (u32)(OFF_ST + tid * 64 + k * 16), 0, 0);
  };
  const int r = c;
  f32x4 acc[4];
#pragma unroll
  for (int k = 0; k < 4; ++k) acc[k] = f32x4{0.f, 0.f, 0.f, 0.f};
  {
    const char* tb = smem + CH_TAB + (64 * w + 4 * g) * 224;
    const char* pA = tb + (c & 3) * 8;
    const char* pB = tb + 32 + (c >> 2) * 8;
    const char* pP = tb + 64 + (r >> 2) * 8;
    const char* pQ = tb + 96 + ((r < 8 ? 0 : r < 12 ? 4 : 8) + (r & 3)) * 8;
    const char* pE = tb + 192;
    f32x2 tA[2][4], tB[2][4], tP[2][4], tQ[2][4], tE3[2][4];
    f32x4 tE[2][4];
    auto ldtab = [&](auto sc_) {
      constexpr int s = decltype(sc_)::value;
#pragma unroll
      for (int i = 0; i < 4; ++i) {
        const int o = (16 * s + i) * 224;
        tA[s & 1][i] = *(const f32x2*)(pA + o); tB[s & 1][i] = *(const f32x2*)(pB + o);
        tP[s & 1][i] = *(const f32x2*)(pP + o); tQ[s & 1][i] = *(const f32x2*)(pQ + o);
        tE[s & 1][i] = *(const f32x4*)(pE + o); tE3[s & 1][i] = *(const f32x2*)(pE + o + 16);
      }
    };
    auto kstep = [&](auto sc_) {
      constexpr int s = decltype(sc_)::value;
      if constexpr (s < 3) ldtab(IC<s + 1>{});
      __builtin_amdgcn_sched_barrier(0);
      u32x4 xf, y0, y1, y2, y3;
#pragma unroll
      for (int i = 0; i < 4; ++i) {
        const f32x2 yv = cmul(tA[s & 1][i], tB[s & 1][i]);
        const f32x2 xv = cmul_conj(tP[s & 1][i], tQ[s & 1][i]);
        y0[i] = pk_f16(yv[0], yv[1]);
        xf[i] = pk_f16(xv[0], xv[1]);
        const f32x4 E12 = tE[s & 1][i];
        const f32x2 v1 = cmul(yv, f32x2{E12[0], E12[1]}), v2 = cmul(yv, f32x2{E12[2], E12[3]}), v3 = cmul(yv, tE3[s & 1][i]);
        y1[i] = pk_f16(v1[0], v1[1]); y2[i] = pk_f16(v2[0], v2[1]); y3[i] = pk_f16(v3[0], v3[1]);
      }
      f16x8 xa = __builtin_bit_cast(f16x8, xf);
      __builtin_amdgcn_sched_barrier(0);
      acc[0] = __builtin_amdgcn_mfma_f32_16x16x32_f16(xa, __builtin_bit_cast(f16x8, y0), acc[0], 0, 0, 0);
      acc[1] = __builtin_amdgcn_mfma_f32_16x16x32_f16(xa, __builtin_bit_cast(f16x8, y1), acc[1], 0, 0, 0);
      acc[2] = __builtin_amdgcn_mfma_f32_16x16x32_f16(xa, __builtin_bit_cast(f16x8, y2), acc[2], 0, 0, 0);
      acc[3] = __builtin_amdgcn_mfma_f32_16x16x32_f16(xa, __builtin_bit_cast(f16x8, y3), acc[3], 0, 0, 0);
      __builtin_amdgcn_sched_barrier(0);
      if constexpr (s == 1) poll();
      __builtin_amdgcn_sched_barrier(0);
    };
    ldtab(IC<0>{});
    static_for<0, 4>(kstep);
  }
  check();
  poll();
  TR(a.ws, 2);
  float* part = (float*)(smem + CH_TAB);
#pragma unroll
  for (int k = 0; k < 4; ++k) *(f32x4*)(part + w * 3584 + (16 * k + r) * 16 + 4 * g) = acc[k];
  __syncthreads();
  load_st_fast();
  float* Fv = (float*)(smem + CH_F);
  const float Gm = fmaxf(fmaxf(red[0], red[1]), fmaxf(red[2], red[3]));
  float sc[4];
#pragma unroll
  for (int k = 0; k < 4; ++k) sc[k] = __builtin_amdgcn_exp2f(red[k] - Gm - 8.f);
  {
    const f32x4* pp = (const f32x4*)part;
    f32x4 sv = pp[tid] * sc[0] + pp[896 + tid] * sc[1] + pp[1792 + tid] * sc[2] + pp[2688 + tid] * sc[3];
    float* fo = Fv + (tid >> 2) * 17 + 4 * (tid & 3);
    fo[0] = sv[0]; fo[1] = sv[1]; fo[2] = sv[2]; fo[3] = sv[3];
    if (tid == 0) { Fv[1088] = red[4] * sc[0] + red[5] * sc[1] + red[6] * sc[2] + red[7] * sc[3]; Fv[1089] = 0.f; }
  }
  const float kinv = __builtin_amdgcn_exp2f(Gm - 6.f);
  check();
  __syncthreads();
  TR(a.ws, 3);
  {
    u32 e[8];
#pragma unroll
    for (int k = 0; k < 8; ++k)
      e[k] = pk_f16(*(const float*)((const char*)Fv + (slot2[k] & 0xffffu)), *(const float*)((const char*)Fv + (slot2[k] >> 16)));
    auto put = [&](int ch, const u32 (&ee)[8]) {
      u32 o[7];
#pragma unroll
      for (int i = 0; i < 7; ++i) o[i] = __builtin_amdgcn_alignbyte(ee[i + 1], ee[i], 2);
#pragma unroll
      for (int s = 0; s < 4; ++s) {
        *(u32x4*)(smem + (2 * s) * CPS + ch * 16) = u32x4{ee[s], ee[s + 1], ee[s + 2], ee[s + 3]};
        *(u32x4*)(smem + (2 * s + 1) * CPS + ch * 16) = u32x4{o[s], o[s + 1], o[s + 2], o[s + 3]};
      }
    };
    put(tid, e);
    if (tid == 255) { const u32 z[8] = {0, 0, 0, 0, 0, 0, 0, 0}; put(256, z); }
  }
  u16* su = (u16*)(smem + CH_SU);
  const u16* sx = (const u16*)(smem_all + LDS_SX + hw * 4096);
  if (wv == 0) {
    if (!have) {
      unsigned spins = 0;
      while (!__all(fl == tag)) { __builtin_amdgcn_s_sleep(1); fl = ld_agent(flg); if (++spins > SPIN_CAP) break; }
      load_x();
    }
    asm volatile("s_waitcnt vmcnt(0)" ::: "memory");
    TR(a.ws, 14);
  }
  {
    const u32 tg = (u32)tag;
    unsigned spins = 0;
    for (;;) {
      bool ok = true;
#pragma unroll
      for (int k = 0; k < 4; ++k) ok &= (gr[k][1] == tg) && (gr[k][3] == tg);
      if (__all(ok)) break;
      __builtin_amdgcn_s_sleep(1);
      load_st();
      if (++spins > SPIN_CAP) break;
    }
  }
  __syncthreads();
  TR(a.ws, 4);
  {
    const u32x4 xr = *(const u32x4*)(sx + tid * 8);
    u32x4 o;
#pragma unroll
    for (int k = 0; k < 4; ++k) {
      const u32 s0 = gr[k][0], s1 = gr[k][2];
      const u32 dpk = pk_bf16(bflo(xr[k]) - bflo(s0), bfhi(xr[k]) - bflo(s1));
      const u32 upk = pk_bf16(bflo(dpk) * __builtin_amdgcn_rcpf(bfhi(s0)), bfhi(dpk) * __builtin_amdgcn_rcpf(bfhi(s1)));
      o[k] = pk_f16(bflo(upk), bfhi(upk));
    }
    *(u32x4*)(su + 256 + tid * 8) = o;
    if (tid < 32) *(u32x4*)(su + tid * 8) = u32x4{0, 0, 0, 0};
  }
  __syncthreads();
  TR(a.ws, 5);
  f32x4 cv[8];
#pragma unroll
  for (int q = 0; q < 8; ++q) cv[q] = f32x4{0.f, 0.f, 0.f, 0.f};
  const int sig = (7 - c) & 7;
  const char* abase = smem + sig * CPS + 16 * ((c < 8 ? 253 : 252) + g);
#pragma unroll
  for (int rr2 = 0; rr2 < 2; ++rr2) {
    const int rs2 = 2 * w + rr2;
    f16x8 B[8];
#pragma unroll
    for (int q = 0; q < 8; ++q) B[q] = *(const f16x8*)(su + 240 + 256 * q - 32 * rs2 + 16 * c + 8 * g);
    f16x8 A[8];
#pragma unroll
    for (int k = 0; k < 8; ++k) A[k] = *(const f16x8*)(abase - 64 * (rs2 + 8 * k));
#pragma unroll
    for (int k = 0; k < 8; ++k)
#pragma unroll
      for (int q = k; q < 8; ++q) cv[q] = __builtin_amdgcn_mfma_f32_16x16x32_f16(A[k], B[q - k], cv[q], 0, 0, 0);
  }
  TR(a.ws, 6);
  __syncthreads();
  f32x4* rb = (f32x4*)smem;
#pragma unroll
  for (int q = 0; q < 8; ++q)
    if ((q >> 1) != w) rb[(w * 8 + q) * 64 + lane] = cv[q];
  __syncthreads();
  const float dsk = bf2f(r_dsk);
#pragma unroll
  for (int i = 0; i < 2; ++i) {
    f32x4 y = cv[0];
#pragma unroll
    for (int q = 0; q < 8; ++q) if (q == 2 * w + i) y = cv[q];
    const int q = 2 * w + i;
#pragma unroll
    for (int s = 1; s < 4; ++s) y += rb[(((w + s) & 3) * 8 + q) * 64 + lane];
    const int t = 256 * q + 16 * c + 4 * g;
    u32x2 uu = *(const u32x2*)(su + 256 + t);
    const u32x2 xx = *(const u32x2*)(sx + t);
    f16x2 ua = as_f16x2(uu[0]), ub = as_f16x2(uu[1]);
    float uf[4] = {(float)ua[0], (float)ua[1], (float)ub[0], (float)ub[1]};
    u32 y01 = pk_bf16(y[0] * kinv, y[1] * kinv), y23 = pk_bf16(y[2] * kinv, y[3] * kinv);
    u32 s01 = pk_bf16(uf[0] * dsk, uf[1] * dsk), s23 = pk_bf16(uf[2] * dsk, uf[3] * dsk);
    u32 r01 = pk_bf16(bflo(y01) + bflo(s01), bfhi(y01) + bfhi(s01));
    u32 r23 = pk_bf16(bflo(y23) + bflo(s23), bfhi(y23) + bfhi(s23));
    u32 h01 = pk_bf16(bflo(xx[0]) + bflo(r01), bfhi(xx[0]) + bfhi(r01));
    u32 h23 = pk_bf16(bflo(xx[1]) + bflo(r23), bfhi(xx[1]) + bfhi(r23));
    __builtin_amdgcn_raw_buffer_store_b64(u32x2{h01, h23}, rs, (u32)(OFF_HT + (size_t)d * LP * 2 + t * 2), 0, 16);
  }
  asm volatile("s_waitcnt vmcnt(0) lgkmcnt(0)" ::: "memory");
  __syncthreads();
  if (threadIdx.x < 8) st_agent(a.ws + OFF_SYNC + SY_CV + threadIdx.x * 2048 + m * 8, tag);
  TR(a.ws, 7);
  return tag;
}

static constexpr int G1_R = 8;
static constexpr int G1_STG = 16384;
static constexpr int G1_ST = G1_R * G1_STG;

constexpr int g1_cnt(int n) {
  if (n < 0) return 0;
  const int ks = n >> 2, c = n & 3;
  return (n + 4 < 56 ? 2 : 0) + ((c < 2 && ks + 1 < 14) ? 4 : 0);
}
constexpr int g1_wait(int n) {
  const int c = n & 3;
  const int breq = (g1_cnt(n - 4) - 2) + g1_cnt(n - 3) + g1_cnt(n - 2) + g1_cnt(n - 1);
  const int areq = g1_cnt(n - 2) + g1_cnt(n - 1);
  return (c == 0 && areq < breq) ? areq : breq;
}

DI void phase_b(const Args& a, int m, char* smem, u64 tag) {
  const int tid = threadIdx.x, lane = tid & 63;
  const int wv = __builtin_amdgcn_readfirstlane(tid >> 6);
  const int w = wv & 3;
  const int xcd = m & 7, bi = m >> 3;
  const int tb = 2 * xcd + bi / 14, fb = bi % 14;
  const int c16 = lane & 15, g = lane >> 4, q = (lane & 15) >> 2, p = lane & 3;
  const u32 lds0 = (u32)(size_t)(LDS3 char*)smem;
  f32x2* st = (f32x2*)(smem + G1_ST);
  char* ws = a.ws;

  if (wv >= 4) {
    u32 vh[2], vw[2];
#pragma unroll
    for (int k = 0; k < 2; ++k) {
      const int row = 8 * w + 4 * k + g;
      vh[k] = (u32)(row * LP * 2) + (u32)((c16 ^ swz(row)) * 16);
      vw[k] = (u32)(row * F * 2) + (u32)((c16 ^ swz(row)) * 16);
    }
    const char* hb = ws + OFF_HT + tb * 256;
    const char* wb = (const char*)a.w1 + fb * 256;
    auto ldw = [&](auto kc) {
      constexpr int ks = decltype(kc)::value;
      constexpr int sl = (ks % G1_R) * G1_STG + 8192;
      glds16<false>(lds0 + sl + (8 * w) * 256, vw[0], wb + (size_t)ks * 32 * F * 2);
      glds16<false>(lds0 + sl + (8 * w + 4) * 256, vw[1], wb + (size_t)ks * 32 * F * 2);
    };
    auto ldh = [&](auto kc) {
      constexpr int ks = decltype(kc)::value;
      constexpr int sl = (ks % G1_R) * G1_STG;
      glds16<true>(lds0 + sl + (8 * w) * 256, vh[0], hb + (size_t)ks * 32 * LP * 2);
      glds16<true>(lds0 + sl + (8 * w + 4) * 256, vh[1], hb + (size_t)ks * 32 * LP * 2);
    };
    static_for<0, G1_R>(ldw);
    loaders_wait<4>(smem, 0, w, ws + OFF_SYNC + SY_CV + xcd * 2048, NMAIN, tag);
    TRW(ws, 8, tid == 256);
    static_for<0, G1_R>(ldh);
    auto stepl = [&](auto kc) {
      constexpr int ks = decltype(kc)::value;
      constexpr int refills = ks > 3 ? (ks - 3 < 14 - G1_R ? ks - 3 : 14 - G1_R) : 0;
      constexpr int need_after = ks < G1_R ? (2 * (G1_R - 1 - ks) + 4 * refills) : (4 * (G1_R + refills - 1 - ks));
      wait_vm<need_after>();
      bar();
      if constexpr (ks >= 3 && ks - 3 + G1_R < 14) { ldh(IC<ks - 3 + G1_R>{}); ldw(IC<ks - 3 + G1_R>{}); }
    };
    static_for<0, 14>(stepl);
    if (w == 0) wait_flags<1>(ws + OFF_SYNC + SY_W1S, NAUX, tag);
    bar();
    bar();
    bar();
    return;
  }
  const int fi = w >> 1, ti = w & 1;
  f32x4 acc[4][4];
  float s1[2] = {0.f, 0.f}, s2[2] = {0.f, 0.f};
  const bf16x2 ones = __builtin_bit_cast(bf16x2, 0x3f803f80u);
  u32 adB[2][4], adA[2][4], adBh[2][4], adAh[2][4];
#pragma unroll
  for (int t = 0; t < 2; ++t) {
    const int row = 8 * g + 4 * t + q;
    const u32 a0 = lds0 + (u32)(row * 256) + (((u32)(p >> 1) ^ swz(row)) << 4) + 8 * (p & 1);
#pragma unroll
    for (int c = 0; c < 4; ++c) {
      adB[t][c] = a0 ^ (u32)(32 * (4 * ti + ((2 * fi + c) & 3)));
      adA[t][c] = (a0 ^ (u32)(32 * (4 * fi + c))) + 8192;
      adBh[t][c] = adB[t][c] + 65536u; adAh[t][c] = adA[t][c] + 65536u;
    }
  }
  u32x2 B[8][2], A[2][4][2];
  u32x4 Af[4];
  auto rdB = [&](auto nc, auto tc) {
    constexpr int n = decltype(nc)::value, t = decltype(tc)::value, sl = (n >> 2) % G1_R;
    trread<(sl & 3) * G1_STG>(B[n & 7][t], sl < 4 ? adB[t][n & 3] : adBh[t][n & 3]);
  };
  auto rdA = [&](auto kc, auto ic, auto tc) {
    constexpr int ks = decltype(kc)::value, i = decltype(ic)::value, t = decltype(tc)::value, sl = ks % G1_R;
    trread<(sl & 3) * G1_STG>(A[ks & 1][i][t], sl < 4 ? adA[t][i] : adAh[t][i]);
  };
  auto group = [&](auto nc) {
    constexpr int n = decltype(nc)::value;
    constexpr int ks = n >> 2, c = n & 3, sl = n & 7;
    constexpr bool nb = n + 4 < 56, na = (c < 2) && (ks + 1 < 14);
    constexpr int wt = n < 4 ? (n == 0 ? 6 : n == 1 ? 10 : n == 2 ? 14 : 14) : g1_wait(n);
    if constexpr (c == 0) {
      asm volatile("s_waitcnt lgkmcnt(%10)" : "+v"(B[sl][0]), "+v"(B[sl][1]), "+v"(A[ks & 1][0][0]), "+v"(A[ks & 1][0][1]), "+v"(A[ks & 1][1][0]), "+v"(A[ks & 1][1][1]),
                   "+v"(A[ks & 1][2][0]), "+v"(A[ks & 1][2][1]), "+v"(A[ks & 1][3][0]), "+v"(A[ks & 1][3][1]) : "n"(wt) : "memory");
#pragma unroll
      for (int i = 0; i < 4; ++i) Af[i] = cat(A[ks & 1][i][0], A[ks & 1][i][1]);
    } else {
      asm volatile("s_waitcnt lgkmcnt(%2)" : "+v"(B[sl][0]), "+v"(B[sl][1]) : "n"(wt) : "memory");
    }
    const u32x4 bfr = cat(B[sl][0], B[sl][1]);
    mfma_bf16<ks == 0, true>(acc[0][c], Af[0], bfr);
    __builtin_amdgcn_sched_barrier(0);
    if constexpr (c == 0 && ks + 1 < 14) bar();
    if constexpr (nb) rdB(IC<n + 4>{}, IC<0>{});
    __builtin_amdgcn_sched_barrier(0);
    mfma_bf16<ks == 0, false>(acc[1][c], Af[1], bfr);
    __builtin_amdgcn_sched_barrier(0);
    if constexpr (nb) rdB(IC<n + 4>{}, IC<1>{});
    if constexpr (na) rdA(IC<ks + 1>{}, IC<2 * c>{}, IC<0>{});
    __builtin_amdgcn_sched_barrier(0);
    mfma_bf16<ks == 0, false>(acc[2][c], Af[2], bfr);
    __builtin_amdgcn_sched_barrier(0);
    if constexpr (na) { rdA(IC<ks + 1>{}, IC<2 * c>{}, IC<1>{}); rdA(IC<ks + 1>{}, IC<2 * c + 1>{}, IC<0>{}); }
    if constexpr (c < 2) {
#pragma unroll
      for (int k = 0; k < 2; ++k) {
        bf16x2 x2 = as_bf16x2(bfr[k]);
        s1[c] = __builtin_amdgcn_fdot2_f32_bf16(x2, ones, s1[c], false);
        s2[c] = __builtin_amdgcn_fdot2_f32_bf16(x2, x2, s2[c], false);
      }
    }
    __builtin_amdgcn_sched_barrier(0);
    mfma_bf16<ks == 0, false>(acc[3][c], Af[3], bfr);
    __builtin_amdgcn_sched_barrier(0);
    if constexpr (na) rdA(IC<ks + 1>{}, IC<2 * c + 1>{}, IC<1>{});
    if constexpr (c < 2) {
#pragma unroll
      for (int k = 2; k < 4; ++k) {
        bf16x2 x2 = as_bf16x2(bfr[k]);
        s1[c] = __builtin_amdgcn_fdot2_f32_bf16(x2, ones, s1[c], false);
        s2[c] = __builtin_amdgcn_fdot2_f32_bf16(x2, x2, s2[c], false);
      }
      if (ks == fb) {
        const int tt = 4 * ti + ((2 * fi + c) & 3);
        __builtin_amdgcn_raw_buffer_store_b128(bfr, ws_rsrc(ws), (u32)(OFF_HN + ((size_t)(tb * 128 + 16 * tt + c16) * D + 32 * fb + 8 * g) * 2), 0, 16);
      }
    }
    __builtin_amdgcn_sched_barrier(0);
  };
  bar();
  static_for<0, 4>([&](auto ic) { rdA(IC<0>{}, ic, IC<0>{}); rdA(IC<0>{}, ic, IC<1>{}); });
  rdB(IC<0>{}, IC<0>{}); rdB(IC<0>{}, IC<1>{}); rdB(IC<1>{}, IC<0>{}); rdB(IC<1>{}, IC<1>{});
  asm volatile("s_waitcnt lgkmcnt(4)" ::: "memory");
  rdB(IC<2>{}, IC<0>{}); rdB(IC<2>{}, IC<1>{}); rdB(IC<3>{}, IC<0>{}); rdB(IC<3>{}, IC<1>{});
  static_for<0, 56>(group);
  asm volatile("s_nop 7\n\ts_nop 7" ::: "memory");
  TR(ws, 9);
  bar();
  f32x4 wsv[4];
#pragma unroll
  for (int i = 0; i < 4; ++i)
    wsv[i] = __builtin_bit_cast(f32x4, __builtin_amdgcn_raw_buffer_load_b128(ws_rsrc(ws), (u32)(OFF_W1S + (fb * 128 + 64 * fi + 4 * g + 16 * i) * 4), 0, 16));
#pragma unroll
  for (int i = 0; i < 2; ++i) {
    float sa = s1[i], sb = s2[i];
    sa += __shfl_xor(sa, 16); sa += __shfl_xor(sa, 32);
    sb += __shfl_xor(sb, 16); sb += __shfl_xor(sb, 32);
    const float mean = sa * (1.0f / D);
    const float var = fmaxf(sb * (1.0f / D) - mean * mean, 0.f);
    const float mu = rbf(mean);
    const float sd = rbf(__builtin_amdgcn_sqrtf(rbf(rbf(var) + 1e-5f)));
    const int tt = 4 * ti + ((2 * fi + i) & 3);
    if (lane < 16) st[tt * 16 + lane] = f32x2{mu, __builtin_amdgcn_rcpf(sd)};
  }
  lds_bar();
#pragma unroll
  for (int c = 0; c < 4; ++c) {
    const int tt = 4 * ti + ((2 * fi + c) & 3);
    const f32x2 ms = st[tt * 16 + c16];
#pragma unroll
    for (int pp = 0; pp < 2; ++pp) {
      float v0[4], v1[4];
#pragma unroll
      for (int k = 0; k < 4; ++k) {
        v0[k] = fmaxf((acc[2 * pp][c][k] - ms[0] * wsv[2 * pp][k]) * ms[1], 0.f);
        v1[k] = fmaxf((acc[2 * pp + 1][c][k] - ms[0] * wsv[2 * pp + 1][k]) * ms[1], 0.f);
      }
      u32x4 o = {pk_bf16(v0[0], v0[1]), pk_bf16(v0[2], v0[3]), pk_bf16(v1[0], v1[1]), pk_bf16(v1[2], v1[3])};
      __builtin_amdgcn_raw_buffer_store_b128(o, ws_rsrc(ws), (u32)(OFF_H1F + (((size_t)(tb * 8 + tt) * 56 + fb * 4 + 2 * fi + pp) * 64 + lane) * 16), 0, 16);
    }
  }
  asm volatile("s_waitcnt vmcnt(0)" ::: "memory");
  bar();
  if (tid == 0) st_agent(ws + OFF_SYNC + SY_H1 + tb * 128 + fb * 8, tag);
  TR(ws, 10);
}

static constexpr int G2_R = 4;
static constexpr int G2_STG = 32768;
static constexpr int G2_RS = 272;

DI void phase_c(const Args& a, int m, char* smem, u64 tag) {
  const int tid = threadIdx.x, lane = tid & 63;
  const int wv = __builtin_amdgcn_readfirstlane(tid >> 6);
  const int w = wv & 3;
  const int xcd = m & 7, bi = m >> 3;
  const int tb = 2 * xcd + bi / 14, fb = bi % 14;
  const int t64 = 2 * tb + fb / 7, db = fb % 7;
  const u32 lds0 = (u32)(size_t)(LDS3 char*)smem;
  char* ws = a.ws;
  const int c16 = lane & 15, g = lane >> 4, q = (lane & 15) >> 2, p = lane & 3;
  if (wv >= 4) {
    const int rr = lane >> 3;
    u32 vw[4], vo[4];
#pragma unroll
    for (int i = 0; i < 4; ++i) {
      const int row = 8 * i + rr;
      vw[i] = (u32)(row * D * 2) + ((((u32)(lane & 7)) ^ (u32)(((row >> 1) & 3) << 1)) << 4);
      vo[i] = lane * 16 + i * 56 * 1024;
    }
    const char* wb = (const char*)a.w2 + ((size_t)(448 * w) * D + db * 64) * 2;
    const char* hb = ws + OFF_H1F + ((size_t)(t64 * 4) * 56 + 14 * w) * 1024;
    auto ldw = [&](auto kc) {
      constexpr int k = decltype(kc)::value;
      constexpr int sl = (k % G2_R) * G2_STG;
#pragma unroll
      for (int i = 0; i < 4; ++i) glds16<false>(lds0 + sl + w * 8192 + i * 1024, vw[i], wb + (size_t)k * 32 * D * 2);
    };
    auto ldh = [&](auto kc) {
      constexpr int k = decltype(kc)::value;
      constexpr int sl = (k % G2_R) * G2_STG;
#pragma unroll
      for (int i = 0; i < 4; ++i) glds16<true>(lds0 + sl + w * 8192 + 4096 + i * 1024, vo[i], hb + k * 1024);
    };
    static_for<0, G2_R>(ldw);
    loaders_wait<1>(smem, 1, w, ws + OFF_SYNC + SY_H1 + tb * 128, 14, tag);
    TRW(ws, 11, tid == 256);
    static_for<0, G2_R>(ldh);
    auto stepl = [&](auto kc) {
      constexpr int k = decltype(kc)::value;
      constexpr int refills = k > 1 ? (k - 1 < 14 - G2_R ? k - 1 : 14 - G2_R) : 0;
      constexpr int need_after = k < G2_R ? (4 * (G2_R - 1 - k) + 8 * refills) : (8 * (G2_R + refills - 1 - k));
      wait_vm<need_after>();
      bar();
      if constexpr (k >= 1 && k - 1 + G2_R < 14) { ldh(IC<k - 1 + G2_R>{}); ldw(IC<k - 1 + G2_R>{}); }
    };
    static_for<0, 14>(stepl);
    bar();
    bar();
    return;
  }
  u32x4 res[2];
  f32x4 acc[4][4];
  u32 adW[4];
  {
    const int row = 4 * g + q;
    const u32 f = (u32)((row >> 1) & 3);
    const u32 a0 = lds0 + w * 8192 + (u32)(row * 128) + (u32)((p >> 1) * 16 + 8 * (p & 1));
#pragma unroll
    for (int dl = 0; dl < 4; ++dl) adW[dl] = a0 + ((((u32)dl) ^ f) << 5);
  }
  const u32 adH = lds0 + w * 8192 + 4096 + lane * 16;
  u32x2 Wf[2][4][2];
  u32x4 Hf[2][4];
  auto rd = [&](auto kc, auto ic) {
    constexpr int k = decltype(kc)::value, i = decltype(ic)::value;
    constexpr int sl = (k % G2_R) * G2_STG;
    constexpr int off = (sl & 65535);
    constexpr u32 hi = (u32)(sl & ~65535);
    if constexpr (i < 8) trread<off + (i & 1) * 2048>(Wf[k & 1][i >> 1][i & 1], adW[i >> 1] + hi);
    else ldsread128<off + (i - 8) * 1024>(Hf[k & 1][i - 8], adH + hi);
  };
  auto step = [&](auto kc) {
    constexpr int k = decltype(kc)::value;
    asm volatile("s_waitcnt lgkmcnt(0)" : "+v"(Wf[k & 1][0][0]), "+v"(Wf[k & 1][0][1]), "+v"(Wf[k & 1][1][0]), "+v"(Wf[k & 1][1][1]), "+v"(Wf[k & 1][2][0]), "+v"(Wf[k & 1][2][1]),
                 "+v"(Wf[k & 1][3][0]), "+v"(Wf[k & 1][3][1]), "+v"(Hf[k & 1][0]), "+v"(Hf[k & 1][1]), "+v"(Hf[k & 1][2]), "+v"(Hf[k & 1][3]) :: "memory");
    if constexpr (k + 1 < 14) bar();
    u32x4 Wq[4];
#pragma unroll
    for (int i = 0; i < 4; ++i) Wq[i] = cat(Wf[k & 1][i][0], Wf[k & 1][i][1]);
    static_for<0, 16>([&](auto jc) {
      constexpr int j = decltype(jc)::value, dl = j >> 2, ta = j & 3;
      mfma_bf16<k == 0, j == 0>(acc[dl][ta], Wq[dl], Hf[k & 1][ta]);
      __builtin_amdgcn_sched_barrier(0);
      if constexpr (k + 1 < 14 && j < 12) rd(IC<k + 1>{}, IC<j>{});
      __builtin_amdgcn_sched_barrier(0);
    });
  };
  bar();
  static_for<0, 12>([&](auto ic) { rd(IC<0>{}, ic); });
  {
    const rsrc_t rs = ws_rsrc(ws);
#pragma unroll
    for (int i = 0; i < 2; ++i)
      res[i] = __builtin_amdgcn_raw_buffer_load_b128(rs, (u32)(OFF_HN + ((size_t)(t64 * 64 + 32 * i + (tid >> 3)) * D + db * 64 + (tid & 7) * 8) * 2), 0, 16);
  }
  static_for<0, 14>(step);
  asm volatile("s_nop 7\n\ts_nop 7" ::: "memory");
  TR(ws, 12);
  bar();
#pragma unroll
  for (int dl = 0; dl < 4; ++dl)
#pragma unroll
    for (int ta = 0; ta < 4; ++ta)
      *(f32x4*)(smem + (w * 64 + 16 * ta + c16) * G2_RS + (16 * dl + 4 * g) * 4) = acc[dl][ta];
  lds_bar();
#pragma unroll
  for (int i = 0; i < 2; ++i) {
    const int t = (tid >> 3) + 32 * i, chn = tid & 7;
    const u32x4 rr = res[i];
    f32x4 s0 = {0.f, 0.f, 0.f, 0.f}, s1 = s0;
#pragma unroll
    for (int ww = 0; ww < 4; ++ww) {
      s0 += *(const f32x4*)(smem + (ww * 64 + t) * G2_RS + chn * 32);
      s1 += *(const f32x4*)(smem + (ww * 64 + t) * G2_RS + chn * 32 + 16);
    }
    u32 p0 = pk_bf16(s0[0], s0[1]), p1 = pk_bf16(s0[2], s0[3]), p2 = pk_bf16(s1[0], s1[1]), p3 = pk_bf16(s1[2], s1[3]);
    u32x4 o;
    o[0] = pk_bf16(bflo(rr[0]) + bflo(p0), bfhi(rr[0]) + bfhi(p0));
    o[1] = pk_bf16(bflo(rr[1]) + bflo(p1), bfhi(rr[1]) + bfhi(p1));
    o[2] = pk_bf16(bflo(rr[2]) + bflo(p2), bfhi(rr[2]) + bfhi(p2));
    o[3] = pk_bf16(bflo(rr[3]) + bflo(p3), bfhi(rr[3]) + bfhi(p3));
    *(u32x4*)(a.out + (size_t)(t64 * 64 + t) * D + db * 64 + chn * 8) = o;
  }
}

__global__ __launch_bounds__(512, 2) void s4_block_kernel(Args a) {
  extern __shared__ __attribute__((aligned(16))) char smem[];
  const int b = blockIdx.x;
  TR(a.ws, 0);
  if (b < NAUX) { aux_block(a, b, smem); TR(a.ws, 15); TRDUMP(a.ws); return; }
  const int m = b - NAUX;
  const u64 tag = phase_a(a, m, smem);
  phase_b(a, m, smem, tag);
  __syncthreads();
  phase_c(a, m, smem, tag);
  TR(a.ws, 15);
  TRDUMP(a.ws);
}

extern "C" void kernel_launch(void* const* d_in, const int* in_sizes, int n_in,
                              void* d_out, int out_size, void* d_ws, size_t ws_size,
                              hipStream_t stream) {
  Args a;
  a.x = (const u16*)d_in[0]; a.Are = (const u16*)d_in[1]; a.Aim = (const u16*)d_in[2];
  a.Bre = (const u16*)d_in[3]; a.Bim = (const u16*)d_in[4]; a.Cre = (const u16*)d_in[5];
  a.Cim = (const u16*)d_in[6]; a.logdt = (const u16*)d_in[7]; a.Dskip = (const u16*)d_in[8];
  a.w1 = (const u16*)d_in[10]; a.w2 = (const u16*)d_in[11]; a.ws = (char*)d_ws; a.out = (u16*)d_out; a.pad = nullptr;
  (void)hipFuncSetAttribute((const void*)s4_block_kernel, hipFuncAttributeMaxDynamicSharedMemorySize, SMEM_BYTES);
  s4_block_kernel<<<NAUX + NMAIN, 512, SMEM_BYTES, stream>>>(a);
}
```

```cpp
#define TLBT 1
#define PF_CODE 0
#define PF_W 1
#define XSC1 true
#include <hip/hip_runtime.h>
#include <hip/hip_bf16.h>
#include <stdint.h>
#include <type_traits>

static constexpr int L = 2048, D = 448, NM = 256, F = 1792;
static constexpr int LP = 2176;
static constexpr size_t OFF_ST = 0;
static constexpr size_t OFF_XT = 16384;
static constexpr size_t OFF_HT = OFF_XT + (size_t)D * LP * 2;
static constexpr size_t OFF_W1S = OFF_HT + (size_t)D * LP * 2;
static constexpr size_t OFF_HN = OFF_W1S + 8192;
static constexpr size_t OFF_H1F = OFF_HN + (size_t)L * D * 2;
static constexpr size_t OFF_SYNC = OFF_H1F + (size_t)L * F * 2;
static constexpr size_t SY_EP = 0, SY_LN = 1024  , SY_W1S = 4096, SY_CV = 8192  , SY_H1 = 32768  ;
static constexpr size_t WS_USED = OFF_SYNC + 65536;

typedef unsigned short u16;
typedef unsigned int u32;
typedef unsigned long long u64;
typedef _Float16 f16;
typedef f16 f16x2 __attribute__((ext_vector_type(2)));
typedef f16 f16x8 __attribute__((ext_vector_type(8)));
typedef __bf16 bf16x2 __attribute__((ext_vector_type(2)));
typedef __bf16 bf16x8 __attribute__((ext_vector_type(8)));
typedef float f32x2 __attribute__((ext_vector_type(2)));
typedef float f32x4 __attribute__((ext_vector_type(4)));
typedef u32 u32x2 __attribute__((ext_vector_type(2)));
typedef u32 u32x4 __attribute__((ext_vector_type(4)));
typedef short s16x4 __attribute__((ext_vector_type(4)));
#define LDS3 __attribute__((address_space(3)))
typedef __attribute__((address_space(1))) u64 gu64;
typedef __amdgpu_buffer_rsrc_t rsrc_t;

#define DI __device__ __forceinline__
DI float bf2f(u16 v) { return __uint_as_float(((u32)v) << 16); }
DI float bflo(u32 v) { return __uint_as_float(v << 16); }
DI float bfhi(u32 v) { return __uint_as_float(v & 0xffff0000u); }
DI u32 pk_bf16(float a, float b) { f32x2 v = {a, b}; return __builtin_bit_cast(u32, __builtin_convertvector(v, bf16x2)); }
DI u32 pk_f16(float a, float b) { f32x2 v = {a, b}; return __builtin_bit_cast(u32, __builtin_convertvector(v, f16x2)); }
DI float rbf(float x) { return bflo(pk_bf16(x, 0.f)); }
DI f16x2 as_f16x2(u32 v) { return __builtin_bit_cast(f16x2, v); }
DI bf16x2 as_bf16x2(u32 v) { return __builtin_bit_cast(bf16x2, v); }
template <int CTRL, int RM> DI float dppf(float old, float v) {
  return __int_as_float(__builtin_amdgcn_update_dpp(__float_as_int(old), __float_as_int(v), CTRL, RM, 0xf, false));
}
DI float wave_sum(float v) {
  v += dppf<0x111, 0xf>(0.f, v); v += dppf<0x112, 0xf>(0.f, v); v += dppf<0x114, 0xf>(0.f, v); v += dppf<0x118, 0xf>(0.f, v);
  v += dppf<0x142, 0xa>(0.f, v); v += dppf<0x143, 0xc>(0.f, v);
  return __int_as_float(__builtin_amdgcn_readlane(__float_as_int(v), 63));
}
DI float wave_max(float v) {
  v = fmaxf(v, dppf<0x111, 0xf>(v, v)); v = fmaxf(v, dppf<0x112, 0xf>(v, v)); v = fmaxf(v, dppf<0x114, 0xf>(v, v)); v = fmaxf(v, dppf<0x118, 0xf>(v, v));
  v = fmaxf(v, dppf<0x142, 0xa>(v, v)); v = fmaxf(v, dppf<0x143, 0xc>(v, v));
  return __int_as_float(__builtin_amdgcn_readlane(__float_as_int(v), 63));
}
DI u64 ld_agent(const void* p) { return __hip_atomic_load((const gu64*)p, __ATOMIC_RELAXED, __HIP_MEMORY_SCOPE_AGENT); }
DI void st_agent(void* p, u64 v) { __hip_atomic_store((gu64*)p, v, __ATOMIC_RELAXED, __HIP_MEMORY_SCOPE_AGENT); }
static constexpr u64 TAG_INC = 0x9E3779B97F4A7C15ull;
DI rsrc_t ws_rsrc(char* ws) { return __builtin_amdgcn_make_buffer_rsrc(ws, 0, (int)WS_USED, 0x00020000); }
template <int B, int E, class Fn> DI void static_for(Fn&& f) {
  if constexpr (B < E) { f(std::integral_constant<int, B>{}); static_for<B + 1, E>(f); }
}
template <int N> using IC = std::integral_constant<int, N>;
DI s16x4 tr_read(const void* p) { return __builtin_amdgcn_ds_read_tr16_b64_v4i16((LDS3 s16x4*)p); }
template <int N> DI void wait_vm() { asm volatile("s_waitcnt vmcnt(%0)" :: "n"(N) : "memory"); }
DI void bar() { __builtin_amdgcn_s_barrier(); }
DI int fresh_tid() { int t = threadIdx.x; asm volatile("" : "+v"(t)); return t; }
DI bool vm_idle_unused() {
  u32 v; asm volatile("s_getreg_b32 %0, hwreg(HW_REG_IB_STS)" : "=s"(v));
  return ((v & 0xfu) | ((v >> 22) & 0x3u)) == 0u;
}
DI void lds_bar() { asm volatile("s_waitcnt lgkmcnt(0)" ::: "memory"); __builtin_amdgcn_s_barrier(); }
template <bool SC1> DI void glds16(u32 lds_dst, u32 voff, const void* sbase) {
  u32 keep;
  if constexpr (SC1)
    asm volatile("s_mov_b32 %0, m0\n\ts_mov_b32 m0, %1\n\ts_nop 0\n\tglobal_load_lds_dwordx4 %2, %3 sc1\n\ts_mov_b32 m0, %0"
                 : "=&s"(keep) : "s"(lds_dst), "v"(voff), "s"(sbase) : "memory");
  else
    asm volatile("s_mov_b32 %0, m0\n\ts_mov_b32 m0, %1\n\ts_nop 0\n\tglobal_load_lds_dwordx4 %2, %3\n\ts_mov_b32 m0, %0"
                 : "=&s"(keep) : "s"(lds_dst), "v"(voff), "s"(sbase) : "memory");
}
template <int OFF> DI void trread(u32x2& dst, u32 addr) {
  asm volatile("ds_read_b64_tr_b16 %0, %1 offset:%2" : "=v"(dst) : "v"(addr), "n"(OFF) : "memory");
}
template <int OFF> DI void ldsread128(u32x4& dst, u32 addr) {
  asm volatile("ds_read_b128 %0, %1 offset:%2" : "=v"(dst) : "v"(addr), "n"(OFF) : "memory");
}
template <bool FIRST, bool NOP> DI void mfma_bf16(f32x4& acc, const u32x4& a, const u32x4& b) {
  if constexpr (NOP) {
    if constexpr (FIRST) asm volatile("s_nop 1\n\tv_mfma_f32_16x16x32_bf16 %0, %1, %2, 0" : "=&v"(acc) : "v"(a), "v"(b));
    else asm volatile("s_nop 1\n\tv_mfma_f32_16x16x32_bf16 %0, %1, %2, %0" : "+v"(acc) : "v"(a), "v"(b));
  } else {
    if constexpr (FIRST) asm volatile("v_mfma_f32_16x16x32_bf16 %0, %1, %2, 0" : "=&v"(acc) : "v"(a), "v"(b));
    else asm volatile("v_mfma_f32_16x16x32_bf16 %0, %1, %2, %0" : "+v"(acc) : "v"(a), "v"(b));
  }
}
DI u32x4 cat(const u32x2& a, const u32x2& b) { return u32x4{a[0], a[1], b[0], b[1]}; }
DI u32 swz(int row) { return (u32)(((row & 3) << 2) | ((row >> 2) & 3)); }
DI f32x2 cmul(f32x2 y, f32x2 e) {
  f32x2 t, r;
  asm("v_pk_mul_f32 %0, %1, %2 op_sel_hi:[1,0]" : "=v"(t) : "v"(y), "v"(e));
  asm("v_pk_fma_f32 %0, %1, %2, %3 op_sel:[1,1,0] op_sel_hi:[0,1,1] neg_lo:[0,1,0]" : "=v"(r) : "v"(y), "v"(e), "v"(t));
  return r;
}
DI f32x2 cmul_conj(f32x2 p, f32x2 q) {
  f32x2 t, r;
  asm("v_pk_mul_f32 %0, %1, %2 op_sel_hi:[1,0] neg_hi:[1,0]" : "=v"(t) : "v"(p), "v"(q));
  asm("v_pk_fma_f32 %0, %1, %2, %3 op_sel:[1,1,0] op_sel_hi:[0,1,1] neg_lo:[1,0,0] neg_hi:[1,0,0]" : "=v"(r) : "v"(p), "v"(q), "v"(t));
  return r;
}

#define TR(ws, slot) do {} while (0)
#define TRW(ws, slot, cond) do {} while (0)
#define TRDUMP(ws) do {} while (0)

struct Args {
  const u16 *x, *Are, *Aim, *Bre, *Bim, *Cre, *Cim, *logdt, *Dskip, *w1, *w2;
  char* ws;
  u16* out;
  const void* pad;
};

constexpr int rne_bf16_int(int l) {
  if (l < 256) return l;
  int sh = 0; while ((l >> sh) >= 256) ++sh;
  const int q = l >> sh, rem = l & ((1 << sh) - 1), half = 1 << (sh - 1);
  const int up = (rem > half) || (rem == half && (q & 1));
  return (q + up) << sh;
}
constexpr int kslot(int m) {
  const int l = 2047 - m;
  if (l < 0) return 1089;
  const int t = rne_bf16_int(l);
  if (t >= 2048) return 1088;
  const int seg = (t >= 256) + (t >= 512) + (t >= 1024);
  const int row = seg == 0 ? (t & 7) : seg == 1 ? 8 + ((t >> 1) & 3) : seg == 2 ? 12 + ((t >> 2) & 1) : 14 + ((t >> 9) & 1);
  const int b = (t >> 3) & (seg >= 2 ? 63 : 31);
  return b * 17 + row;
}
struct SlotTab { u16 v[2064]; };
constexpr SlotTab make_slot_tab() { SlotTab t{}; for (int m = 0; m < 2064; ++m) t.v[m] = (u16)(kslot(m) * 4); return t; }
__device__ __attribute__((aligned(16))) const SlotTab g_slot_tab = make_slot_tab();

static constexpr int NAUX = 32;
static constexpr int NMAIN = 224;
static constexpr int SMEM_BYTES = 160 * 1024;
static constexpr unsigned SPIN_CAP = 1u << 20;
static constexpr int LDS_SIG = SMEM_BYTES - 320;

template <int NPL> DI void wait_flags(const char* base, int n, u64 tag) {
  const int lane = threadIdx.x & 63;
  unsigned spins = 0;
  for (;;) {
    bool ok = true;
#pragma unroll
    for (int k = 0; k < NPL; ++k) { const int i = k * 64 + lane; if (i < n) ok &= (ld_agent(base + i * 8) == tag); }
    if (__all(ok)) break;
    __builtin_amdgcn_s_sleep(1);
    if (++spins > SPIN_CAP) break;
  }
}

template <int NPL> DI void loaders_wait(char* smem, int idx, int w, const char* base, int n, u64 tag) {
  volatile u32* sig = (volatile u32*)(smem + LDS_SIG) + idx;
  if (w == 0) {
    wait_flags<NPL>(base, n, tag);
    *sig = 1u;
  } else {
    unsigned spins = 0;
    while (*sig == 0u) { __builtin_amdgcn_s_sleep(1); if (++spins > (SPIN_CAP << 4)) break; }
  }
  asm volatile("" ::: "memory");
}

DI u32 tlb_touch_async(const Args& a) {
  const int lane = threadIdx.x & 63;
  const char* p;
  if (lane < 20) p = a.ws + (size_t)lane * (1u << 20);
  else if (lane < 22) p = (const char*)a.out + (size_t)(lane - 20) * (1u << 20);
  else if (lane < 24) p = (const char*)a.w1 + (size_t)(lane - 22) * (1u << 20);
  else if (lane < 26) p = (const char*)a.w2 + (size_t)(lane - 24) * (1u << 20);
  else p = (const char*)a.x;
  u32 v;
  asm volatile("global_load_dword %0, %1, off" : "=v"(v) : "v"(p) : "memory");
  return v;
}
DI void l2_touch(const char* base, int kbytes, int part, int nparts, char* smem) {
  const int lane = threadIdx.x & 63;
  const int w = __builtin_amdgcn_readfirstlane(threadIdx.x >> 6);
  const u32 dst = (u32)(size_t)(LDS3 char*)smem + 65536 + w * 1024;
  const int per = (kbytes + nparts - 1) / nparts;
  const int k0 = per * part, k1 = (k0 + per < kbytes) ? k0 + per : kbytes;
  int cnt = 0;
  for (int k = k0 + w; k < k1; k += 8) {
    glds16<false>(dst, lane * 16, base + (size_t)k * 1024);
    if ((++cnt & 7) == 0) wait_vm<32>();
  }
  wait_vm<0>();
}
static constexpr int XT_RS = 144;
static constexpr int LDS_X8 = 124 * 1024;
struct Pro { u32x4 row, tile; };
DI Pro pro_load(const Args& a, int b) {
  const int lane = threadIdx.x & 63, wv = threadIdx.x >> 6;
  Pro p; p.row = u32x4{0, 0, 0, 0}; p.tile = p.row;
  if (lane < 56) {
    p.tile = *(const u32x4*)(a.x + (size_t)(64 * (b >> 3) + 8 * wv + lane / 7) * D + 56 * (b & 7) + 8 * (lane % 7));
    p.row = *(const u32x4*)(a.x + (size_t)(8 * b + wv) * D + lane * 8);
  }
  return p;
}
DI u64 pro_store(const Args& a, int b, const Pro& p, u64 ep, char* smem) {
  const int lane = threadIdx.x & 63;
  const int wv = __builtin_amdgcn_readfirstlane(threadIdx.x >> 6);
  if (lane < 56) *(u32x4*)(smem + LDS_X8 + (8 * wv + lane / 7) * XT_RS + (lane % 7) * 16) = p.tile;
  if (threadIdx.x == 0) { *(volatile u32*)(smem + LDS_SIG) = 0u; *(volatile u32*)(smem + LDS_SIG + 4) = 0u; *(volatile u64*)(smem + LDS_SIG + 8) = ep + TAG_INC; *(volatile u32*)(smem + LDS_SIG + 48) = 0u; }
  __syncthreads();
  const u64 tag = *(volatile u64*)(smem + LDS_SIG + 8);
  if (wv < 7) {
    const int g = lane >> 4, i16 = lane & 15, q = i16 >> 2, pp = lane & 3;
    const char* src = smem + LDS_X8 + (16 * g + 8 * (pp >> 1) + q) * XT_RS + (4 * (pp & 1)) * 2 + wv * 16;
    s16x4 lo = tr_read(src), hi = tr_read(src + 4 * XT_RS);
    __builtin_amdgcn_raw_buffer_store_b128(cat(__builtin_bit_cast(u32x2, lo), __builtin_bit_cast(u32x2, hi)), ws_rsrc(a.ws),
        (u32)(OFF_XT + (size_t)(56 * (b & 7) + 8 * wv + (i16 & 7)) * LP * 2 + (64 * (b >> 3) + 16 * g + 8 * (i16 >> 3)) * 2), 0, 16);
  }
  return tag;
}
DI void pro_stats(const Args& a, int b, const Pro& p, u64 tag) {
  const int lane = threadIdx.x & 63, wv = threadIdx.x >> 6;
  float s = 0.f, q2 = 0.f;
#pragma unroll
  for (int k = 0; k < 4; ++k) {
    const float lo = bflo(p.row[k]), hi = bfhi(p.row[k]);
    s += lo; s += hi; q2 = __builtin_fmaf(lo, lo, q2); q2 = __builtin_fmaf(hi, hi, q2);
  }
  const float mean = wave_sum(s) * (1.0f / D);
  const float var = fmaxf(wave_sum(q2) * (1.0f / D) - mean * mean, 0.f);
  const float sd = rbf(__builtin_amdgcn_sqrtf(rbf(rbf(var) + 1e-5f)));
  if (lane == 0) st_agent(a.ws + OFF_ST + (8 * b + wv) * 8, ((u64)(u32)tag << 32) | pk_bf16(mean, sd));
}
DI void pro_flag(const Args& a, int b, u64 tag) {
  if (threadIdx.x == 0) st_agent(a.ws + OFF_SYNC + SY_LN + (b & 7) * 256 + (b >> 3) * 8, tag);
}
DI void aux_block(const Args& a, int blk, char* smem) {
  const int tid = threadIdx.x, lane = tid & 63;
  const int w = __builtin_amdgcn_readfirstlane(tid >> 6);
  const rsrc_t rs = ws_rsrc(a.ws);
  const Pro pr = pro_load(a, blk);
  const u64 ep = ld_agent(w == 0 ? (const char*)(a.ws + OFF_SYNC + SY_EP) : (const char*)(a.x + (size_t)(8 * blk + w) * D));
  const u64 tag = pro_store(a, blk, pr, ep, smem);
  pro_stats(a, blk, pr, tag);
  asm volatile("s_waitcnt vmcnt(0)" ::: "memory");
  __syncthreads();
  pro_flag(a, blk, tag);
  TR(a.ws, 2);
  {
    float* red = (float*)smem;
    const int cp = tid % 28, rg = tid / 28;
    float s0 = 0.f, s1 = 0.f;
    if (rg < 16) {
      u32 x[28];
#pragma unroll
      for (int i = 0; i < 28; ++i) x[i] = *(const u32*)(a.w1 + (size_t)(rg + 16 * i) * F + 56 * blk + 2 * cp);
#pragma unroll
      for (int i = 0; i < 28; ++i) { s0 += bflo(x[i]); s1 += bfhi(x[i]); }
      red[rg * 56 + 2 * cp] = s0; red[rg * 56 + 2 * cp + 1] = s1;
    }
    __syncthreads();
    if (tid < 56) {
      float s = 0.f;
#pragma unroll
      for (int i = 0; i < 16; ++i) s += red[i * 56 + tid];
      __builtin_amdgcn_raw_buffer_store_b32(__float_as_uint(s), rs, (u32)(OFF_W1S + (56 * blk + tid) * 4), 0, 16);
    }
    asm volatile("s_waitcnt vmcnt(0)" ::: "memory");
    __syncthreads();
    if (tid == 0) st_agent(a.ws + OFF_SYNC + SY_W1S + blk * 8, tag);
  }
  l2_touch((const char*)a.w1, D * F * 2 / 1024, blk >> 3, 4, smem);
  l2_touch((const char*)a.w2, D * F * 2 / 1024, blk >> 3, 4, smem);
  if (blk == 0 && w == 0) {
    wait_flags<1>(a.ws + OFF_SYNC + SY_W1S, NAUX, tag);
    wait_flags<4>(a.ws + OFF_SYNC + SY_CV, NMAIN, tag);
    if (lane == 0) st_agent(a.ws + OFF_SYNC + SY_EP, tag);
  }
}

static constexpr int CPS = 4160;
static constexpr int CH_BYTES = 61440;
static constexpr int CH_TAB = 0;
static constexpr int CH_SU = 8 * CPS;
static constexpr int CH_F = CH_SU + 4608 + 256;
static constexpr int LDS_SX = 136 * 1024;
static constexpr int CH_RED = 59392;

DI u64 phase_a(const Args& a, int m, char* smem_all) {
  const int tid = threadIdx.x & 255, lane = threadIdx.x & 63;
  const int wv = __builtin_amdgcn_readfirstlane(threadIdx.x >> 6);
  const int w = wv & 3, hw = wv >> 2;
  const int d = 2 * m + hw;
  char* smem = smem_all + hw * CH_BYTES;
  const float LOG2E = 1.4426950408889634f, INV2PI = 0.15915494309189535f;
  const u32 lds0 = (u32)(size_t)(LDS3 char*)smem;
  const Pro pr = pro_load(a, m + NAUX);
  const u64 ep = ld_agent(wv == 0 ? (const char*)(a.ws + OFF_SYNC + SY_EP) : (const char*)(a.Are + d * NM + 4 * w));
  const int n = tid;
  const u16 r_are = a.Are[d * NM + n], r_aim = a.Aim[d * NM + n], r_bre = a.Bre[d * NM + n], r_bim = a.Bim[d * NM + n];
  const u16 r_cre = a.Cre[d * NM + n], r_cim = a.Cim[d * NM + n], r_ldt = a.logdt[d], r_dsk = a.Dskip[d];
  u32 slot2[8];
  {
    const u32x4 s0 = *(const u32x4*)(g_slot_tab.v + 8 * tid), s1 = *(const u32x4*)(g_slot_tab.v + 8 * tid + 8);
#pragma unroll
    for (int k = 0; k < 4; ++k) { slot2[k] = s0[k]; slot2[4 + k] = s1[k]; }
  }
  const u32 tlbv = tlb_touch_async(a);
  __builtin_amdgcn_sched_barrier(0);
  const u64 tag = pro_store(a, m + NAUX, pr, ep, smem_all);
  float* red = (float*)(smem + CH_RED);
  const float dt = rbf(expf(bf2f(r_ldt)));
  const float bre = dt * bf2f(r_bre), bim = dt * bf2f(r_bim);
  const float cre = bf2f(r_cre), cim = bf2f(r_cim);
  const float zr = (dt * bf2f(r_are)) * LOG2E, zi = (dt * bf2f(r_aim)) * INV2PI;
  const float cr = cre * bre - cim * bim, ci = cre * bim + cim * bre;
  const float mx = fmaxf(fabsf(cr), fabsf(ci));
  const float lc = mx > 1e-36f ? __builtin_amdgcn_logf(mx) + 0.5f : -1000.f;
  const float sY = fmaxf(0.f, zr * 504.f);
  const float gm = fmaxf(lc + fmaxf(0.f, zr * 1536.f) + sY, lc + zr * 2048.f);
  const float G = ceilf(wave_max(gm));
  auto cexp = [&](float t, float lg) -> f32x2 {
    const float mm = __builtin_amdgcn_exp2f(__builtin_fmaf(zr, t, lg)), f = __builtin_amdgcn_fractf(zi * t);
    return f32x2{mm * __builtin_amdgcn_cosf(f), mm * __builtin_amdgcn_sinf(f)};
  };
  {
    f32x2* tb = (f32x2*)(smem + CH_TAB + n * 224);
    const f32x2 e1 = cexp(1.f, 0.f), e2 = cmul(e1, e1), e3 = cmul(e2, e1), e4 = cexp(4.f, 0.f), e6 = cmul(e3, e3);
    const f32x2 e8 = cexp(8.f, 0.f), e16 = cmul(e8, e8), e24 = cmul(e16, e8);
    const f32x2 e32 = cexp(32.f, 0.f), e64 = cmul(e32, e32), e96 = cmul(e64, e32);
    const f32x2 e128 = cexp(128.f, 0.f), e256 = cexp(256.f, 0.f), e384 = cmul(e256, e128);
    const f32x2 e512 = cexp(512.f, 0.f), e1024 = cexp(1024.f, 0.f);
    const float sy2 = __builtin_amdgcn_exp2f(-sY);
    const float cs = __builtin_amdgcn_exp2f(sY - G + 14.f);
    const f32x2 c0 = {cr * cs, ci * cs};
    *(f32x4*)(tb + 0) = f32x4{1.f, 0.f, e8[0], e8[1]};      *(f32x4*)(tb + 2) = f32x4{e16[0], e16[1], e24[0], e24[1]};
    *(f32x4*)(tb + 4) = f32x4{sy2, 0.f, e32[0] * sy2, e32[1] * sy2}; *(f32x4*)(tb + 6) = f32x4{e64[0] * sy2, e64[1] * sy2, e96[0] * sy2, e96[1] * sy2};
    const f32x2 p1 = cmul(c0, e4), p2 = cmul(c0, e256), p3 = cmul(c0, e512);
    *(f32x4*)(tb + 8) = f32x4{c0[0], c0[1], p1[0], p1[1]};  *(f32x4*)(tb + 10) = f32x4{p2[0], p2[1], p3[0], p3[1]};
    *(f32x4*)(tb + 12) = f32x4{1.f, 0.f, e1[0], e1[1]};     *(f32x4*)(tb + 14) = f32x4{e2[0], e2[1], e3[0], e3[1]};
    *(f32x4*)(tb + 16) = f32x4{1.f, 0.f, e2[0], e2[1]};     *(f32x4*)(tb + 18) = f32x4{e4[0], e4[1], e6[0], e6[1]};
    *(f32x4*)(tb + 20) = f32x4{1.f, 0.f, e4[0], e4[1]};     *(f32x4*)(tb + 22) = f32x4{e512[0], e512[1], e1024[0], e1024[1]};
    *(f32x4*)(tb + 24) = f32x4{e128[0], e128[1], e256[0], e256[1]}; *(f32x4*)(tb + 26) = f32x4{e384[0], e384[1], 0.f, 0.f};
    const f32x2 e2048 = cexp(2048.f, 14.f - G);
    float t2048 = cr * e2048[0] - ci * e2048[1];
    t2048 = wave_sum(t2048);
    if (lane == 0) { red[w] = G; red[4 + w] = t2048; }
  }
  asm volatile("s_waitcnt vmcnt(0) lgkmcnt(0)" ::: "memory");
  asm volatile("" :: "v"(tlbv));
  __syncthreads();
  pro_flag(a, m + NAUX, tag);
  TR(a.ws, 1);
  pro_stats(a, m + NAUX, pr, tag);
  const int c = lane & 15, g = lane >> 4;
  const rsrc_t rs = ws_rsrc(a.ws);
  u32x4 gr[4];
  const char* flg = a.ws + OFF_SYNC + SY_LN + ((2 * m) / 56) * 256 + (lane & 31) * 8;
  u64 fl = 0;
  bool have = (wv != 0);
  auto load_x = [&]() {
    TR(a.ws, 13);
#pragma unroll
    for (int i = 0; i < 4; ++i) {
      glds16<XSC1>(lds0 + LDS_SX + i * 1024, lane * 16 + i * 1024, a.ws + OFF_XT + (size_t)(2 * m) * LP * 2);
      glds16<XSC1>(lds0 + LDS_SX + 4096 + i * 1024, lane * 16 + i * 1024, a.ws + OFF_XT + (size_t)(2 * m + 1) * LP * 2);
    }
  };
  auto poll = [&]() { if (wv == 0 && !have) fl = ld_agent(flg); };
  auto check = [&]() {
    if (wv == 0 && !have) {
      if (__all(fl == tag)) { have = true; load_x(); }
    }
  };
  auto load_st = [&]() {
#pragma unroll
    for (int k = 0; k < 4; ++k) gr[k] = __builtin_amdgcn_raw_buffer_load_b128(rs, (u32)(OFF_ST + tid * 64 + k * 16), 0, 16);
  };
  auto load_st_fast = [&]() {
#pragma unroll
    for (int k = 0; k < 4; ++k) gr[k] = __builtin_amdgcn_raw_buffer_load_b128(rs, (u32)(OFF_ST + tid * 64 + k * 16), 0, 0);
  };
  const int r = c;
  f32x4 acc[4];
#pragma unroll
  for (int k = 0; k < 4; ++k) acc[k] = f32x4{0.f, 0.f, 0.f, 0.f};
  {
    const char* tb = smem + CH_TAB + (64 * w + 4 * g) * 224;
    const char* pA = tb + (c & 3) * 8;
    const char* pB = tb + 32 + (c >> 2) * 8;
    const char* pP = tb + 64 + (r >> 2) * 8;
    const char* pQ = tb + 96 + ((r < 8 ? 0 : r < 12 ? 4 : 8) + (r & 3)) * 8;
    const char* pE = tb + 192;
    f32x2 tA[2][4], tB[2][4], tP[2][4], tQ[2][4], tE3[2][4];
    f32x4 tE[2][4];
    auto ldtab = [&](auto sc_) {
      constexpr int s = decltype(sc_)::value;
#pragma unroll
      for (int i = 0; i < 4; ++i) {
        const int o = (16 * s + i) * 224;
        tA[s & 1][i] = *(const f32x2*)(pA + o); tB[s & 1][i] = *(const f32x2*)(pB + o);
        tP[s & 1][i] = *(const f32x2*)(pP + o); tQ[s & 1][i] = *(const f32x2*)(pQ + o);
        tE[s & 1][i] = *(const f32x4*)(pE + o); tE3[s & 1][i] = *(const f32x2*)(pE + o + 16);
      }
    };
    auto kstep = [&](auto sc_) {
      constexpr int s = decltype(sc_)::value;
      if constexpr (s < 3) ldtab(IC<s + 1>{});
      __builtin_amdgcn_sched_barrier(0);
      u32x4 xf, y0, y1, y2, y3;
#pragma unroll
      for (int i = 0; i < 4; ++i) {
        const f32x2 yv = cmul(tA[s & 1][i], tB[s & 1][i]);
        const f32x2 xv = cmul_conj(tP[s & 1][i], tQ[s & 1][i]);
        y0[i] = pk_f16(yv[0], yv[1]);
        xf[i] = pk_f16(xv[0], xv[1]);
        const f32x4 E12 = tE[s & 1][i];
        const f32x2 v1 = cmul(yv, f32x2{E12[0], E12[1]}), v2 = cmul(yv, f32x2{E12[2], E12[3]}), v3 = cmul(yv, tE3[s & 1][i]);
        y1[i] = pk_f16(v1[0], v1[1]); y2[i] = pk_f16(v2[0], v2[1]); y3[i] = pk_f16(v3[0], v3[1]);
      }
      f16x8 xa = __builtin_bit_cast(f16x8, xf);
      __builtin_amdgcn_sched_barrier(0);
      acc[0] = __builtin_amdgcn_mfma_f32_16x16x32_f16(xa, __builtin_bit_cast(f16x8, y0), acc[0], 0, 0, 0);
      acc[1] = __builtin_amdgcn_mfma_f32_16x16x32_f16(xa, __builtin_bit_cast(f16x8, y1), acc[1], 0, 0, 0);
      acc[2] = __builtin_amdgcn_mfma_f32_16x16x32_f16(xa, __builtin_bit_cast(f16x8, y2), acc[2], 0, 0, 0);
      acc[3] = __builtin_amdgcn_mfma_f32_16x16x32_f16(xa, __builtin_bit_cast(f16x8, y3), acc[3], 0, 0, 0);
      __builtin_amdgcn_sched_barrier(0);
      if constexpr (s == 1) poll();
      __builtin_amdgcn_sched_barrier(0);
    };
    ldtab(IC<0>{});
    static_for<0, 4>(kstep);
  }
  check();
  poll();
  TR(a.ws, 2);
  float* part = (float*)(smem + CH_TAB);
#pragma unroll
  for (int k = 0; k < 4; ++k) *(f32x4*)(part + w * 3584 + (16 * k + r) * 16 + 4 * g) = acc[k];
  __syncthreads();
  load_st_fast();
  float* Fv = (float*)(smem + CH_F);
  const float Gm = fmaxf(fmaxf(red[0], red[1]), fmaxf(red[2], red[3]));
  float sc[4];
#pragma unroll
  for (int k = 0; k < 4; ++k) sc[k] = __builtin_amdgcn_exp2f(red[k] - Gm - 8.f);
  {
    const f32x4* pp = (const f32x4*)part;
    f32x4 sv = pp[tid] * sc[0] + pp[896 + tid] * sc[1] + pp[1792 + tid] * sc[2] + pp[2688 + tid] * sc[3];
    float* fo = Fv + (tid >> 2) * 17 + 4 * (tid & 3);
    fo[0] = sv[0]; fo[1] = sv[1]; fo[2] = sv[2]; fo[3] = sv[3];
    if (tid == 0) { Fv[1088] = red[4] * sc[0] + red[5] * sc[1] + red[6] * sc[2] + red[7] * sc[3]; Fv[1089] = 0.f; }
  }
  const float kinv = __builtin_amdgcn_exp2f(Gm - 6.f);
  check();
  __syncthreads();
  TR(a.ws, 3);
  {
    u32 e[8];
#pragma unroll
    for (int k = 0; k < 8; ++k)
      e[k] = pk_f16(*(const float*)((const char*)Fv + (slot2[k] & 0xffffu)), *(const float*)((const char*)Fv + (slot2[k] >> 16)));
    auto put = [&](int ch, const u32 (&ee)[8]) {
      u32 o[7];
#pragma unroll
      for (int i = 0; i < 7; ++i) o[i] = __builtin_amdgcn_alignbyte(ee[i + 1], ee[i], 2);
#pragma unroll
      for (int s = 0; s < 4; ++s) {
        *(u32x4*)(smem + (2 * s) * CPS + ch * 16) = u32x4{ee[s], ee[s + 1], ee[s + 2], ee[s + 3]};
        *(u32x4*)(smem + (2 * s + 1) * CPS + ch * 16) = u32x4{o[s], o[s + 1], o[s + 2], o[s + 3]};
      }
    };
    put(tid, e);
    if (tid == 255) { const u32 z[8] = {0, 0, 0, 0, 0, 0, 0, 0}; put(256, z); }
  }
  u16* su = (u16*)(smem + CH_SU);
  const u16* sx = (const u16*)(smem_all + LDS_SX + hw * 4096);
  if (wv == 0) {
    if (!have) {
      unsigned spins = 0;
      while (!__all(fl == tag)) { __builtin_amdgcn_s_sleep(1); fl = ld_agent(flg); if (++spins > SPIN_CAP) break; }
      load_x();
    }
    asm volatile("s_waitcnt vmcnt(0)" ::: "memory");
    TR(a.ws, 14);
  }
  {
    const u32 tg = (u32)tag;
    unsigned spins = 0;
    for (;;) {
      bool ok = true;
#pragma unroll
      for (int k = 0; k < 4; ++k) ok &= (gr[k][1] == tg) && (gr[k][3] == tg);
      if (__all(ok)) break;
      __builtin_amdgcn_s_sleep(1);
      load_st();
      if (++spins > SPIN_CAP) break;
    }
  }
  __syncthreads();
  TR(a.ws, 4);
  {
    const u32x4 xr = *(const u32x4*)(sx + tid * 8);
    u32x4 o;
#pragma unroll
    for (int k = 0; k < 4; ++k) {
      const u32 s0 = gr[k][0], s1 = gr[k][2];
      const u32 dpk = pk_bf16(bflo(xr[k]) - bflo(s0), bfhi(xr[k]) - bflo(s1));
      const u32 upk = pk_bf16(bflo(dpk) * __builtin_amdgcn_rcpf(bfhi(s0)), bfhi(dpk) * __builtin_amdgcn_rcpf(bfhi(s1)));
      o[k] = pk_f16(bflo(upk), bfhi(upk));
    }
    *(u32x4*)(su + 256 + tid * 8) = o;
    if (tid < 32) *(u32x4*)(su + tid * 8) = u32x4{0, 0, 0, 0};
  }
  __syncthreads();
  TR(a.ws, 5);
  f32x4 cv[8];
#pragma unroll
  for (int q = 0; q < 8; ++q) cv[q] = f32x4{0.f, 0.f, 0.f, 0.f};
  const int sig = (7 - c) & 7;
  const char* abase = smem + sig * CPS + 16 * ((c < 8 ? 253 : 252) + g);
#pragma unroll
  for (int rr2 = 0; rr2 < 2; ++rr2) {
    const int rs2 = 2 * w + rr2;
    f16x8 B[8];
#pragma unroll
    for (int q = 0; q < 8; ++q) B[q] = *(const f16x8*)(su + 240 + 256 * q - 32 * rs2 + 16 * c + 8 * g);
    f16x8 A[8];
#pragma unroll
    for (int k = 0; k < 8; ++k) A[k] = *(const f16x8*)(abase - 64 * (rs2 + 8 * k));
#pragma unroll
    for (int k = 0; k < 8; ++k)
#pragma unroll
      for (int q = k; q < 8; ++q) cv[q] = __builtin_amdgcn_mfma_f32_16x16x32_f16(A[k], B[q - k], cv[q], 0, 0, 0);
  }
  TR(a.ws, 6);
  __syncthreads();
  f32x4* rb = (f32x4*)smem;
#pragma unroll
  for (int q = 0; q < 8; ++q)
    if ((q >> 1) != w) rb[(w * 8 + q) * 64 + lane] = cv[q];
  __syncthreads();
  const float dsk = bf2f(r_dsk);
  u32x2 hh[2];
#pragma unroll
  for (int i = 0; i < 2; ++i) {
    f32x4 y = cv[0];
#pragma unroll
    for (int q = 0; q < 8; ++q) if (q == 2 * w + i) y = cv[q];
    const int q = 2 * w + i;
#pragma unroll
    for (int s = 1; s < 4; ++s) y += rb[(((w + s) & 3) * 8 + q) * 64 + lane];
    const int t = 256 * q + 16 * c + 4 * g;
    u32x2 uu = *(const u32x2*)(su + 256 + t);
    const u32x2 xx = *(const u32x2*)(sx + t);
    f16x2 ua = as_f16x2(uu[0]), ub = as_f16x2(uu[1]);
    float uf[4] = {(float)ua[0], (float)ua[1], (float)ub[0], (float)ub[1]};
    u32 y01 = pk_bf16(y[0] * kinv, y[1] * kinv), y23 = pk_bf16(y[2] * kinv, y[3] * kinv);
    u32 s01 = pk_bf16(uf[0] * dsk, uf[1] * dsk), s23 = pk_bf16(uf[2] * dsk, uf[3] * dsk);
    u32 r01 = pk_bf16(bflo(y01) + bflo(s01), bfhi(y01) + bfhi(s01));
    u32 r23 = pk_bf16(bflo(y23) + bflo(s23), bfhi(y23) + bfhi(s23));
    hh[i][0] = pk_bf16(bflo(xx[0]) + bflo(r01), bfhi(xx[0]) + bfhi(r01));
    hh[i][1] = pk_bf16(bflo(xx[1]) + bflo(r23), bfhi(xx[1]) + bfhi(r23));
  }
  {
    const auto r0 = __builtin_amdgcn_permlane16_swap(hh[0][0], hh[1][0], false, false);
    const auto r1 = __builtin_amdgcn_permlane16_swap(hh[0][1], hh[1][1], false, false);
    const int t = 256 * (2 * w + (g & 1)) + 16 * c + 8 * (g >> 1);
    __builtin_amdgcn_raw_buffer_store_b128(u32x4{r0[0], r1[0], r0[1], r1[1]}, rs, (u32)(OFF_HT + (size_t)d * LP * 2 + t * 2), 0, 16);
  }
  asm volatile("s_waitcnt vmcnt(0) lgkmcnt(0)" ::: "memory");
  __syncthreads();
  if (threadIdx.x < 8) st_agent(a.ws + OFF_SYNC + SY_CV + threadIdx.x * 2048 + m * 8, tag);
  TR(a.ws, 7);
  return tag;
}

static constexpr int G1_R = 8;
static constexpr int G1_STG = 16384;
static constexpr int G1_ST = G1_R * G1_STG;

constexpr int g1_cnt(int n) {
  if (n < 0) return 0;
  const int ks = n >> 2, c = n & 3;
  return (n + 4 < 56 ? 2 : 0) + ((c < 2 && ks + 1 < 14) ? 4 : 0);
}
constexpr int g1_wait(int n) {
  const int c = n & 3;
  const int breq = (g1_cnt(n - 4) - 2) + g1_cnt(n - 3) + g1_cnt(n - 2) + g1_cnt(n - 1);
  const int areq = g1_cnt(n - 2) + g1_cnt(n - 1);
  return (c == 0 && areq < breq) ? areq : breq;
}

DI void c_prefetch_w(const Args& a, int m, char* smem);
DI void phase_b(const Args& a, int m, char* smem, u64 tag) {
  const int tid = fresh_tid(), lane = tid & 63;
  const int wv = __builtin_amdgcn_readfirstlane(tid >> 6);
  const int w = wv & 3;
  const int xcd = m & 7, bi = m >> 3;
  const int tb = 2 * xcd + bi / 14, fb = bi % 14;
  const int c16 = lane & 15, g = lane >> 4, q = (lane & 15) >> 2, p = lane & 3;
  const u32 lds0 = (u32)(size_t)(LDS3 char*)smem;
  f32x2* st = (f32x2*)(smem + G1_ST);
  char* ws = a.ws;

  if (wv >= 4) {
    u32 vh[2], vw[2];
#pragma unroll
    for (int k = 0; k < 2; ++k) {
      const int row = 8 * w + 4 * k + g;
      vh[k] = (u32)(row * LP * 2) + (u32)((c16 ^ swz(row)) * 16);
      vw[k] = (u32)(row * F * 2) + (u32)((c16 ^ swz(row)) * 16);
    }
    const char* hb = ws + OFF_HT + tb * 256;
    const char* wb = (const char*)a.w1 + fb * 256;
    auto ldw = [&](auto kc) {
      constexpr int ks = decltype(kc)::value;
      constexpr int sl = (ks % G1_R) * G1_STG + 8192;
      glds16<false>(lds0 + sl + (8 * w) * 256, vw[0], wb + (size_t)ks * 32 * F * 2);
      glds16<false>(lds0 + sl + (8 * w + 4) * 256, vw[1], wb + (size_t)ks * 32 * F * 2);
    };
    auto ldh = [&](auto kc) {
      constexpr int ks = decltype(kc)::value;
      constexpr int sl = (ks % G1_R) * G1_STG;
      glds16<true>(lds0 + sl + (8 * w) * 256, vh[0], hb + (size_t)ks * 32 * LP * 2);
      glds16<true>(lds0 + sl + (8 * w + 4) * 256, vh[1], hb + (size_t)ks * 32 * LP * 2);
    };
    static_for<0, G1_R>(ldw);
    if (w == 0) wait_flags<1>(ws + OFF_SYNC + SY_W1S, NAUX, tag);
    loaders_wait<4>(smem, 0, w, ws + OFF_SYNC + SY_CV + xcd * 2048, NMAIN, tag);
    TRW(ws, 8, tid == 256);
    if (w == 0) glds16<true>(lds0 + G1_ST + 1024, lane * 16, ws + OFF_W1S + fb * 512);
    static_for<0, G1_R>(ldh);
    auto stepl = [&](auto kc) {
      constexpr int ks = decltype(kc)::value;
      constexpr int refills = ks > 3 ? (ks - 3 < 14 - G1_R ? ks - 3 : 14 - G1_R) : 0;
      constexpr int need_after = ks < G1_R ? (2 * (G1_R - 1 - ks) + 4 * refills) : (4 * (G1_R + refills - 1 - ks));
      wait_vm<need_after>();
      bar();
      if constexpr (ks >= 3 && ks - 3 + G1_R < 14) { ldh(IC<ks - 3 + G1_R>{}); ldw(IC<ks - 3 + G1_R>{}); }
    };
    static_for<0, 14>(stepl);
    bar();
    bar();
    __builtin_amdgcn_s_sleep(12);
    c_prefetch_w(a, m, smem);
    return;
  }
  const int fi = w >> 1, ti = w & 1;
  f32x4 acc[4][4];
  float s1[2] = {0.f, 0.f}, s2[2] = {0.f, 0.f};
  const bf16x2 ones = __builtin_bit_cast(bf16x2, 0x3f803f80u);
  u32 adB[2][4], adA[2][4];
#pragma unroll
  for (int t = 0; t < 2; ++t) {
    const int row = 8 * g + 4 * t + q;
    const u32 a0 = lds0 + (u32)(row * 256) + (((u32)(p >> 1) ^ swz(row)) << 4) + 8 * (p & 1);
#pragma unroll
    for (int c = 0; c < 4; ++c) {
      adB[t][c] = a0 ^ (u32)(32 * (4 * ti + ((2 * fi + c) & 3)));
      adA[t][c] = (a0 ^ (u32)(32 * (4 * fi + c))) + 8192;
    }
  }
  u32x2 B[8][2], A[2][4][2];
  u32x4 Af[4];
  u32 hoff[2];
#pragma unroll
  for (int c = 0; c < 2; ++c)
    hoff[c] = (u32)(OFF_HN + ((size_t)(tb * 128 + 16 * (4 * ti + ((2 * fi + c) & 3)) + c16) * D + 32 * fb + 8 * g) * 2);
  auto rdB = [&](auto nc, auto tc) {
    constexpr int n = decltype(nc)::value, t = decltype(tc)::value, sl = (n >> 2) % G1_R;
    trread<(sl & 3) * G1_STG>(B[n & 7][t], adB[t][n & 3]);
  };
  auto rdA = [&](auto kc, auto ic, auto tc) {
    constexpr int ks = decltype(kc)::value, i = decltype(ic)::value, t = decltype(tc)::value, sl = ks % G1_R;
    trread<(sl & 3) * G1_STG>(A[ks & 1][i][t], adA[t][i]);
  };
  auto group = [&](auto nc) {
    constexpr int n = decltype(nc)::value;
    constexpr int ks = n >> 2, c = n & 3, sl = n & 7;
    constexpr bool nb = n + 4 < 56, na = (c < 2) && (ks + 1 < 14);
    constexpr int wt = n < 4 ? (n == 0 ? 6 : n == 1 ? 10 : n == 2 ? 14 : 14) : g1_wait(n);
    if constexpr (c == 0) {
      asm volatile("s_waitcnt lgkmcnt(%10)" : "+v"(B[sl][0]), "+v"(B[sl][1]), "+v"(A[ks & 1][0][0]), "+v"(A[ks & 1][0][1]), "+v"(A[ks & 1][1][0]), "+v"(A[ks & 1][1][1]),
                   "+v"(A[ks & 1][2][0]), "+v"(A[ks & 1][2][1]), "+v"(A[ks & 1][3][0]), "+v"(A[ks & 1][3][1]) : "n"(wt) : "memory");
#pragma unroll
      for (int i = 0; i < 4; ++i) Af[i] = cat(A[ks & 1][i][0], A[ks & 1][i][1]);
    } else {
      asm volatile("s_waitcnt lgkmcnt(%2)" : "+v"(B[sl][0]), "+v"(B[sl][1]) : "n"(wt) : "memory");
    }
    const u32x4 bfr = cat(B[sl][0], B[sl][1]);
    mfma_bf16<ks == 0, true>(acc[0][c], Af[0], bfr);
    __builtin_amdgcn_sched_barrier(0);
    if constexpr (c == 0 && ks + 1 < 14) bar();
    if constexpr (c == 0 && ((ks + 1) & 3) == 0) {
      const u32 dl = (((ks + 1) >> 2) & 1) ? 65536u : (u32)-65536;
#pragma unroll
      for (int t = 0; t < 2; ++t)
#pragma unroll
        for (int cc = 0; cc < 4; ++cc) { adB[t][cc] += dl; adA[t][cc] += dl; }
    }
    if constexpr (nb) rdB(IC<n + 4>{}, IC<0>{});
    __builtin_amdgcn_sched_barrier(0);
    mfma_bf16<ks == 0, false>(acc[1][c], Af[1], bfr);
    __builtin_amdgcn_sched_barrier(0);
    if constexpr (nb) rdB(IC<n + 4>{}, IC<1>{});
    if constexpr (na) rdA(IC<ks + 1>{}, IC<2 * c>{}, IC<0>{});
    __builtin_amdgcn_sched_barrier(0);
    mfma_bf16<ks == 0, false>(acc[2][c], Af[2], bfr);
    __builtin_amdgcn_sched_barrier(0);
    if constexpr (na) { rdA(IC<ks + 1>{}, IC<2 * c>{}, IC<1>{}); rdA(IC<ks + 1>{}, IC<2 * c + 1>{}, IC<0>{}); }
    if constexpr (c < 2) {
#pragma unroll
      for (int k = 0; k < 2; ++k) {
        bf16x2 x2 = as_bf16x2(bfr[k]);
        s1[c] = __builtin_amdgcn_fdot2_f32_bf16(x2, ones, s1[c], false);
        s2[c] = __builtin_amdgcn_fdot2_f32_bf16(x2, x2, s2[c], false);
      }
    }
    __builtin_amdgcn_sched_barrier(0);
    mfma_bf16<ks == 0, false>(acc[3][c], Af[3], bfr);
    __builtin_amdgcn_sched_barrier(0);
    if constexpr (na) rdA(IC<ks + 1>{}, IC<2 * c + 1>{}, IC<1>{});
    if constexpr (c < 2) {
#pragma unroll
      for (int k = 2; k < 4; ++k) {
        bf16x2 x2 = as_bf16x2(bfr[k]);
        s1[c] = __builtin_amdgcn_fdot2_f32_bf16(x2, ones, s1[c], false);
        s2[c] = __builtin_amdgcn_fdot2_f32_bf16(x2, x2, s2[c], false);
      }
      if (ks == fb) __builtin_amdgcn_raw_buffer_store_b128(bfr, ws_rsrc(ws), hoff[c], 0, 16);
    }
    __builtin_amdgcn_sched_barrier(0);
  };
  bar();
  static_for<0, 4>([&](auto ic) { rdA(IC<0>{}, ic, IC<0>{}); rdA(IC<0>{}, ic, IC<1>{}); });
  rdB(IC<0>{}, IC<0>{}); rdB(IC<0>{}, IC<1>{}); rdB(IC<1>{}, IC<0>{}); rdB(IC<1>{}, IC<1>{});
  asm volatile("s_waitcnt lgkmcnt(4)" ::: "memory");
  rdB(IC<2>{}, IC<0>{}); rdB(IC<2>{}, IC<1>{}); rdB(IC<3>{}, IC<0>{}); rdB(IC<3>{}, IC<1>{});
  static_for<0, 56>(group);
  asm volatile("s_nop 7\n\ts_nop 7" ::: "memory");
  TR(ws, 9);
  bar();
  const int lane2 = fresh_tid() & 63, c16b = lane2 & 15;
#pragma unroll
  for (int i = 0; i < 2; ++i) {
    float sa = s1[i], sb = s2[i];
    sa += __shfl_xor(sa, 16); sa += __shfl_xor(sa, 32);
    sb += __shfl_xor(sb, 16); sb += __shfl_xor(sb, 32);
    const float mean = sa * (1.0f / D);
    const float var = fmaxf(sb * (1.0f / D) - mean * mean, 0.f);
    const float mu = rbf(mean);
    const float sd = rbf(__builtin_amdgcn_sqrtf(rbf(rbf(var) + 1e-5f)));
    const int tt = 4 * ti + ((2 * fi + i) & 3);
    if (lane2 < 16) st[tt * 16 + lane2] = f32x2{mu, __builtin_amdgcn_rcpf(sd)};
  }
  lds_bar();
  f32x4 wsv[4];
#pragma unroll
  for (int i = 0; i < 4; ++i) wsv[i] = *(const f32x4*)(smem + G1_ST + 1024 + (64 * fi + 4 * (lane2 >> 4) + 16 * i) * 4);
#pragma unroll
  for (int c = 0; c < 4; ++c) {
    const int tt = 4 * ti + ((2 * fi + c) & 3);
    const f32x2 ms = st[tt * 16 + c16b];
#pragma unroll
    for (int pp = 0; pp < 2; ++pp) {
      float v0[4], v1[4];
#pragma unroll
      for (int k = 0; k < 4; ++k) {
        v0[k] = fmaxf((acc[2 * pp][c][k] - ms[0] * wsv[2 * pp][k]) * ms[1], 0.f);
        v1[k] = fmaxf((acc[2 * pp + 1][c][k] - ms[0] * wsv[2 * pp + 1][k]) * ms[1], 0.f);
      }
      u32x4 o = {pk_bf16(v0[0], v0[1]), pk_bf16(v0[2], v0[3]), pk_bf16(v1[0], v1[1]), pk_bf16(v1[2], v1[3])};
      __builtin_amdgcn_raw_buffer_store_b128(o, ws_rsrc(ws), (u32)(OFF_H1F + (((size_t)(tb * 8 + tt) * 56 + fb * 4 + 2 * fi + pp) * 64 + lane2) * 16), 0, 16);
    }
  }
  asm volatile("s_waitcnt vmcnt(0)" ::: "memory");
  if (lane2 == 0) {
    const u32 old = __hip_atomic_fetch_add((LDS3 u32*)(smem + LDS_SIG + 48), 1u, __ATOMIC_RELAXED, __HIP_MEMORY_SCOPE_WORKGROUP);
    if (old == 3u) st_agent(ws + OFF_SYNC + SY_H1 + tb * 128 + fb * 8, tag);
  }
  TR(ws, 10);
}

static constexpr int G2_NR = 5;
static constexpr int G2_RS = 272;
constexpr int g2_waddr(int k) { return k < 5 ? k * 16384 : k < 8 ? 81920 + (k - 5) * 16384 : k == 8 ? 133120 : k < 11 ? (k - 9) * 16384 : 81920 + (k - 11) * 16384; }
constexpr int g2_haddr(int k) { return 32768 + (k % 3) * 16384; }
constexpr int g2_after(int k) {
  int seq[40] = {}; int n = 0;
  seq[n++] = 109; seq[n++] = 110; seq[n++] = 0; seq[n++] = 1; seq[n++] = 2;
  for (int j = 0; j < k; ++j) {
    if (j >= 1 && j + 2 < 14) seq[n++] = j + 2;
    if (j >= 6 && j <= 8) seq[n++] = 100 + j + 5;
  }
  int last = -1;
  for (int i = 0; i < n; ++i) if (seq[i] == k || seq[i] == 100 + k) last = i;
  return n - 1 - last;
}
struct CW { u32 vw[4]; const char* wb; u32 lds0; int w; };
DI CW c_wsetup(const Args& a, int m, char* smem) {
  const int lane = fresh_tid() & 63;
  const int w = __builtin_amdgcn_readfirstlane(threadIdx.x >> 6) & 3;
  const int fb = (m >> 3) % 14, db = fb % 7;
  CW c; c.w = w; c.lds0 = (u32)(size_t)(LDS3 char*)smem;
  const int rr = lane >> 3;
#pragma unroll
  for (int i = 0; i < 4; ++i) {
    const int row = 8 * i + rr;
    c.vw[i] = (u32)(row * D * 2) + ((((u32)(lane & 7)) ^ (u32)(((row >> 1) & 3) << 1)) << 4);
  }
  c.wb = (const char*)a.w2 + ((size_t)(448 * w) * D + db * 64) * 2;
  return c;
}
template <int K> DI void c_ldw(const CW& c) {
#pragma unroll
  for (int i = 0; i < 4; ++i) glds16<false>(c.lds0 + g2_waddr(K) + c.w * 4096 + i * 1024, c.vw[i], c.wb + (size_t)K * 32 * D * 2);
}
DI void c_prefetch_w(const Args& a, int m, char* smem) {
  const CW c = c_wsetup(a, m, smem);
  static_for<0, 9>([&](auto kc) { c_ldw<decltype(kc)::value>(c); });
}

DI void phase_c(const Args& a, int m, char* smem, u64 tag) {
  const int tid = fresh_tid(), lane = tid & 63;
  const int wv = __builtin_amdgcn_readfirstlane(tid >> 6);
  const int w = wv & 3;
  const int xcd = m & 7, bi = m >> 3;
  const int tb = 2 * xcd + bi / 14, fb = bi % 14;
  const int t64 = 2 * tb + fb / 7, db = fb % 7;
  const u32 lds0 = (u32)(size_t)(LDS3 char*)smem;
  char* ws = a.ws;
  const int c16 = lane & 15, g = lane >> 4, q = (lane & 15) >> 2, p = lane & 3;
  if (wv >= 4) {
    const CW c = c_wsetup(a, m, smem);
    u32 vo[4];
#pragma unroll
    for (int i = 0; i < 4; ++i) vo[i] = lane * 16 + i * 56 * 1024;
    const char* hb = ws + OFF_H1F + ((size_t)(t64 * 4) * 56 + 14 * w) * 1024;
    auto ldh = [&](auto kc) {
      constexpr int k = decltype(kc)::value;
#pragma unroll
      for (int i = 0; i < 4; ++i) glds16<true>(lds0 + g2_haddr(k) + w * 4096 + i * 1024, vo[i], hb + k * 1024);
    };
    wait_vm<0>();
    bar();
    bar();
    c_ldw<9>(c); c_ldw<10>(c);
    loaders_wait<1>(smem, 1, w, ws + OFF_SYNC + SY_H1 + tb * 128, 14, tag);
    TRW(ws, 11, tid == 256);
    ldh(IC<0>{}); ldh(IC<1>{}); ldh(IC<2>{});
    auto stepl = [&](auto kc) {
      constexpr int k = decltype(kc)::value;
      wait_vm<4 * g2_after(k)>();
      bar();
      if constexpr (k >= 1 && k + 2 < 14) ldh(IC<k + 2>{});
      if constexpr (k >= 6 && k <= 8) c_ldw<k + 5>(c);
    };
    static_for<0, 14>(stepl);
    bar();
    bar();
    return;
  }
  u32x4 res[2];
  f32x4 acc[4][4];
  u32 adW[4];
  {
    const int row = 4 * g + q;
    const u32 f = (u32)((row >> 1) & 3);
    const u32 a0 = lds0 + w * 4096 + (u32)(row * 128) + (u32)((p >> 1) * 16 + 8 * (p & 1));
#pragma unroll
    for (int dl = 0; dl < 4; ++dl) adW[dl] = a0 + ((((u32)dl) ^ f) << 5);
  }
  const u32 adH = lds0 + w * 4096 + lane * 16;
  u32x2 Wf[2][4][2];
  u32x4 Hf[2][4], Wr[G2_NR][4];
  auto rdW = [&](auto kc, auto ic, u32x2& dst) {
    constexpr int k = decltype(kc)::value, i = decltype(ic)::value;
    constexpr int ad = g2_waddr(k);
    trread<(ad & 65535) + (i & 1) * 2048>(dst, adW[i >> 1] + (u32)(ad & ~65535));
  };
  bar();
  static_for<0, G2_NR>([&](auto kc) {
    constexpr int k = decltype(kc)::value;
    u32x2 t[8];
    static_for<0, 8>([&](auto ic) { rdW(kc, ic, t[decltype(ic)::value]); });
    asm volatile("s_waitcnt lgkmcnt(0)" : "+v"(t[0]), "+v"(t[1]), "+v"(t[2]), "+v"(t[3]), "+v"(t[4]), "+v"(t[5]), "+v"(t[6]), "+v"(t[7]) :: "memory");
#pragma unroll
    for (int i = 0; i < 4; ++i) Wr[k][i] = cat(t[2 * i], t[2 * i + 1]);
  });
  bar();
  auto rd = [&](auto kc, auto ic) {
    constexpr int k = decltype(kc)::value, i = decltype(ic)::value;
    if constexpr (i < 4) ldsread128<(g2_haddr(k) & 65535) + i * 1024>(Hf[k & 1][i], adH + (u32)(g2_haddr(k) & ~65535));
    else if constexpr (k >= G2_NR) rdW(kc, IC<i - 4>{}, Wf[k & 1][(i - 4) >> 1][(i - 4) & 1]);
  };
  auto step = [&](auto kc) {
    constexpr int k = decltype(kc)::value;
    u32x4 Wq[4];
    if constexpr (k >= G2_NR) {
      asm volatile("s_waitcnt lgkmcnt(0)" : "+v"(Wf[k & 1][0][0]), "+v"(Wf[k & 1][0][1]), "+v"(Wf[k & 1][1][0]), "+v"(Wf[k & 1][1][1]), "+v"(Wf[k & 1][2][0]), "+v"(Wf[k & 1][2][1]),
                   "+v"(Wf[k & 1][3][0]), "+v"(Wf[k & 1][3][1]), "+v"(Hf[k & 1][0]), "+v"(Hf[k & 1][1]), "+v"(Hf[k & 1][2]), "+v"(Hf[k & 1][3]) :: "memory");
#pragma unroll
      for (int i = 0; i < 4; ++i) Wq[i] = cat(Wf[k & 1][i][0], Wf[k & 1][i][1]);
    } else {
      asm volatile("s_waitcnt lgkmcnt(0)" : "+v"(Hf[k & 1][0]), "+v"(Hf[k & 1][1]), "+v"(Hf[k & 1][2]), "+v"(Hf[k & 1][3]) :: "memory");
#pragma unroll
      for (int i = 0; i < 4; ++i) Wq[i] = Wr[k][i];
    }
    if constexpr (k + 1 < 14) bar();
    static_for<0, 16>([&](auto jc) {
      constexpr int j = decltype(jc)::value, dl = j >> 2, ta = j & 3;
      mfma_bf16<k == 0, j == 0>(acc[dl][ta], Wq[dl], Hf[k & 1][ta]);
      __builtin_amdgcn_sched_barrier(0);
      if constexpr (k + 1 < 14 && j < 12) rd(IC<k + 1>{}, IC<j>{});
      __builtin_amdgcn_sched_barrier(0);
    });
  };
  bar();
  static_for<0, 4>([&](auto ic) { rd(IC<0>{}, ic); });
  {
    const rsrc_t rs = ws_rsrc(ws);
#pragma unroll
    for (int i = 0; i < 2; ++i)
      res[i] = __builtin_amdgcn_raw_buffer_load_b128(rs, (u32)(OFF_HN + ((size_t)(t64 * 64 + 32 * i + (tid >> 3)) * D + db * 64 + (tid & 7) * 8) * 2), 0, 16);
  }
  static_for<0, 14>(step);
  asm volatile("s_nop 7\n\ts_nop 7" ::: "memory");
  TR(ws, 12);
  bar();
  const int tid2 = fresh_tid(), lane2 = tid2 & 63;
#pragma unroll
  for (int dl = 0; dl < 4; ++dl)
#pragma unroll
    for (int ta = 0; ta < 4; ++ta)
      *(f32x4*)(smem + (w * 64 + 16 * ta + (lane2 & 15)) * G2_RS + (16 * dl + 4 * (lane2 >> 4)) * 4) = acc[dl][ta];
  lds_bar();
#pragma unroll
  for (int i = 0; i < 2; ++i) {
    const int t = (tid2 >> 3) + 32 * i, chn = tid2 & 7;
    const u32x4 rr = res[i];
    f32x4 s0 = {0.f, 0.f, 0.f, 0.f}, s1 = s0;
#pragma unroll
    for (int ww = 0; ww < 4; ++ww) {
      s0 += *(const f32x4*)(smem + (ww * 64 + t) * G2_RS + chn * 32);
      s1 += *(const f32x4*)(smem + (ww * 64 + t) * G2_RS + chn * 32 + 16);
    }
    u32 p0 = pk_bf16(s0[0], s0[1]), p1 = pk_bf16(s0[2], s0[3]), p2 = pk_bf16(s1[0], s1[1]), p3 = pk_bf16(s1[2], s1[3]);
    u32x4 o;
    o[0] = pk_bf16(bflo(rr[0]) + bflo(p0), bfhi(rr[0]) + bfhi(p0));
    o[1] = pk_bf16(bflo(rr[1]) + bflo(p1), bfhi(rr[1]) + bfhi(p1));
    o[2] = pk_bf16(bflo(rr[2]) + bflo(p2), bfhi(rr[2]) + bfhi(p2));
    o[3] = pk_bf16(bflo(rr[3]) + bflo(p3), bfhi(rr[3]) + bfhi(p3));
    *(u32x4*)(a.out + (size_t)(t64 * 64 + t) * D + db * 64 + chn * 8) = o;
  }
}

__global__ __launch_bounds__(512, 2) void s4_block_kernel(Args a) {
  extern __shared__ __attribute__((aligned(16))) char smem[];
  const int b = blockIdx.x;
  TR(a.ws, 0);
  if (b < NAUX) { aux_block(a, b, smem); TR(a.ws, 15); TRDUMP(a.ws); return; }
  const int m = b - NAUX;
  const u64 tag = phase_a(a, m, smem);
  phase_b(a, m, smem, tag);
  __syncthreads();
  phase_c(a, m, smem, tag);
  TR(a.ws, 15);
  TRDUMP(a.ws);
}

extern "C" void kernel_launch(void* const* d_in, const int* in_sizes, int n_in,
                              void* d_out, int out_size, void* d_ws, size_t ws_size,
                              hipStream_t stream) {
  Args a;
  a.x = (const u16*)d_in[0]; a.Are = (const u16*)d_in[1]; a.Aim = (const u16*)d_in[2];
  a.Bre = (const u16*)d_in[3]; a.Bim = (const u16*)d_in[4]; a.Cre = (const u16*)d_in[5];
  a.Cim = (const u16*)d_in[6]; a.logdt = (const u16*)d_in[7]; a.Dskip = (const u16*)d_in[8];
  a.w1 = (const u16*)d_in[10]; a.w2 = (const u16*)d_in[11]; a.ws = (char*)d_ws; a.out = (u16*)d_out; a.pad = nullptr;
  (void)hipFuncSetAttribute((const void*)s4_block_kernel, hipFuncAttributeMaxDynamicSharedMemorySize, SMEM_BYTES);
  s4_block_kernel<<<NAUX + NMAIN, 512, SMEM_BYTES, stream>>>(a);
}
```

```cpp
#undef PF_CODE
#define TLBT 0
#define PF_CODE 1
#define PF_W 1
#define XSC1 true
#include <hip/hip_runtime.h>
#include <hip/hip_bf16.h>
#include <stdint.h>
#include <type_traits>

static constexpr int L = 2048, D = 448, NM = 256, F = 1792;
static constexpr int LP = 2176;
static constexpr size_t OFF_ST = 0;
static constexpr size_t OFF_XT = 16384;
static constexpr size_t OFF_HT = OFF_XT + (size_t)D * LP * 2;
static constexpr size_t OFF_W1S = OFF_HT + (size_t)D * LP * 2;
static constexpr size_t OFF_HN = OFF_W1S + 8192;
static constexpr size_t OFF_H1F = OFF_HN + (size_t)L * D * 2;
static constexpr size_t OFF_SYNC = OFF_H1F + (size_t)L * F * 2;
static constexpr size_t SY_EP = 0, SY_LN = 1024  , SY_W1S = 4096, SY_CV = 8192  , SY_H1 = 32768  ;
static constexpr size_t WS_USED = OFF_SYNC + 65536;

typedef unsigned short u16;
typedef unsigned int u32;
typedef unsigned long long u64;
typedef _Float16 f16;
typedef f16 f16x2 __attribute__((ext_vector_type(2)));
typedef f16 f16x8 __attribute__((ext_vector_type(8)));
typedef __bf16 bf16x2 __attribute__((ext_vector_type(2)));
typedef __bf16 bf16x8 __attribute__((ext_vector_type(8)));
typedef float f32x2 __attribute__((ext_vector_type(2)));
typedef float f32x4 __attribute__((ext_vector_type(4)));
typedef u32 u32x2 __attribute__((ext_vector_type(2)));
typedef u32 u32x4 __attribute__((ext_vector_type(4)));
typedef short s16x4 __attribute__((ext_vector_type(4)));
#define LDS3 __attribute__((address_space(3)))
typedef __attribute__((address_space(1))) u64 gu64;
typedef __amdgpu_buffer_rsrc_t rsrc_t;

#define DI __device__ __forceinline__
DI float bf2f(u16 v) { return __uint_as_float(((u32)v) << 16); }
DI float bflo(u32 v) { return __uint_as_float(v << 16); }
DI float bfhi(u32 v) { return __uint_as_float(v & 0xffff0000u); }
DI u32 pk_bf16(float a, float b) { f32x2 v = {a, b}; return __builtin_bit_cast(u32, __builtin_convertvector(v, bf16x2)); }
DI u32 pk_f16(float a, float b) { f32x2 v = {a, b}; return __builtin_bit_cast(u32, __builtin_convertvector(v, f16x2)); }
DI float rbf(float x) { return bflo(pk_bf16(x, 0.f)); }
DI f16x2 as_f16x2(u32 v) { return __builtin_bit_cast(f16x2, v); }
DI bf16x2 as_bf16x2(u32 v) { return __builtin_bit_cast(bf16x2, v); }
template <int CTRL, int RM> DI float dppf(float old, float v) {
  return __int_as_float(__builtin_amdgcn_update_dpp(__float_as_int(old), __float_as_int(v), CTRL, RM, 0xf, false));
}
DI float wave_sum(float v) {
  v += dppf<0x111, 0xf>(0.f, v); v += dppf<0x112, 0xf>(0.f, v); v += dppf<0x114, 0xf>(0.f, v); v += dppf<0x118, 0xf>(0.f, v);
  v += dppf<0x142, 0xa>(0.f, v); v += dppf<0x143, 0xc>(0.f, v);
  return __int_as_float(__builtin_amdgcn_readlane(__float_as_int(v), 63));
}
DI float wave_max(float v) {
  v = fmaxf(v, dppf<0x111, 0xf>(v, v)); v = fmaxf(v, dppf<0x112, 0xf>(v, v)); v = fmaxf(v, dppf<0x114, 0xf>(v, v)); v = fmaxf(v, dppf<0x118, 0xf>(v, v));
  v = fmaxf(v, dppf<0x142, 0xa>(v, v)); v = fmaxf(v, dppf<0x143, 0xc>(v, v));
  return __int_as_float(__builtin_amdgcn_readlane(__float_as_int(v), 63));
}
DI u64 ld_agent(const void* p) { return __hip_atomic_load((const gu64*)p, __ATOMIC_RELAXED, __HIP_MEMORY_SCOPE_AGENT); }
DI void st_agent(void* p, u64 v) { __hip_atomic_store((gu64*)p, v, __ATOMIC_RELAXED, __HIP_MEMORY_SCOPE_AGENT); }
static constexpr u64 TAG_INC = 0x9E3779B97F4A7C15ull;
DI rsrc_t ws_rsrc(char* ws) { return __builtin_amdgcn_make_buffer_rsrc(ws, 0, (int)WS_USED, 0x00020000); }
template <int B, int E, class Fn> DI void static_for(Fn&& f) {
  if constexpr (B < E) { f(std::integral_constant<int, B>{}); static_for<B + 1, E>(f); }
}
template <int N> using IC = std::integral_constant<int, N>;
DI s16x4 tr_read(const void* p) { return __builtin_amdgcn_ds_read_tr16_b64_v4i16((LDS3 s16x4*)p); }
template <int N> DI void wait_vm() { asm volatile("s_waitcnt vmcnt(%0)" :: "n"(N) : "memory"); }
DI void bar() { __builtin_amdgcn_s_barrier(); }
DI int fresh_tid() { int t = threadIdx.x; asm volatile("" : "+v"(t)); return t; }
DI bool vm_idle_unused() {
  u32 v; asm volatile("s_getreg_b32 %0, hwreg(HW_REG_IB_STS)" : "=s"(v));
  return ((v & 0xfu) | ((v >> 22) & 0x3u)) == 0u;
}
DI void lds_bar() { asm volatile("s_waitcnt lgkmcnt(0)" ::: "memory"); __builtin_amdgcn_s_barrier(); }
template <bool SC1> DI void glds16(u32 lds_dst, u32 voff, const void* sbase) {
  u32 keep;
  if constexpr (SC1)
    asm volatile("s_mov_b32 %0, m0\n\ts_mov_b32 m0, %1\n\ts_nop 0\n\tglobal_load_lds_dwordx4 %2, %3 sc1\n\ts_mov_b32 m0, %0"
                 : "=&s"(keep) : "s"(lds_dst), "v"(voff), "s"(sbase) : "memory");
  else
    asm volatile("s_mov_b32 %0, m0\n\ts_mov_b32 m0, %1\n\ts_nop 0\n\tglobal_load_lds_dwordx4 %2, %3\n\ts_mov_b32 m0, %0"
                 : "=&s"(keep) : "s"(lds_dst), "v"(voff), "s"(sbase) : "memory");
}
template <int OFF> DI void trread(u32x2& dst, u32 addr) {
  asm volatile("ds_read_b64_tr_b16 %0, %1 offset:%2" : "=v"(dst) : "v"(addr), "n"(OFF) : "memory");
}
template <int OFF> DI void ldsread128(u32x4& dst, u32 addr) {
  asm volatile("ds_read_b128 %0, %1 offset:%2" : "=v"(dst) : "v"(addr), "n"(OFF) : "memory");
}
template <bool FIRST, bool NOP> DI void mfma_bf16(f32x4& acc, const u32x4& a, const u32x4& b) {
  if constexpr (NOP) {
    if constexpr (FIRST) asm volatile("s_nop 1\n\tv_mfma_f32_16x16x32_bf16 %0, %1, %2, 0" : "=&v"(acc) : "v"(a), "v"(b));
    else asm volatile("s_nop 1\n\tv_mfma_f32_16x16x32_bf16 %0, %1, %2, %0" : "+v"(acc) : "v"(a), "v"(b));
  } else {
    if constexpr (FIRST) asm volatile("v_mfma_f32_16x16x32_bf16 %0, %1, %2, 0" : "=&v"(acc) : "v"(a), "v"(b));
    else asm volatile("v_mfma_f32_16x16x32_bf16 %0, %1, %2, %0" : "+v"(acc) : "v"(a), "v"(b));
  }
}
DI u32x4 cat(const u32x2& a, const u32x2& b) { return u32x4{a[0], a[1], b[0], b[1]}; }
DI u32 swz(int row) { return (u32)(((row & 3) << 2) | ((row >> 2) & 3)); }
DI f32x2 cmul(f32x2 y, f32x2 e) {
  f32x2 t, r;
  asm("v_pk_mul_f32 %0, %1, %2 op_sel_hi:[1,0]" : "=v"(t) : "v"(y), "v"(e));
  asm("v_pk_fma_f32 %0, %1, %2, %3 op_sel:[1,1,0] op_sel_hi:[0,1,1] neg_lo:[0,1,0]" : "=v"(r) : "v"(y), "v"(e), "v"(t));
  return r;
}
DI f32x2 cmul_conj(f32x2 p, f32x2 q) {
  f32x2 t, r;
  asm("v_pk_mul_f32 %0, %1, %2 op_sel_hi:[1,0] neg_hi:[1,0]" : "=v"(t) : "v"(p), "v"(q));
  asm("v_pk_fma_f32 %0, %1, %2, %3 op_sel:[1,1,0] op_sel_hi:[0,1,1] neg_lo:[1,0,0] neg_hi:[1,0,0]" : "=v"(r) : "v"(p), "v"(q), "v"(t));
  return r;
}

#define TR(ws, slot) do {} while (0)
#define TRW(ws, slot, cond) do {} while (0)
#define TRDUMP(ws) do {} while (0)

struct Args {
  const u16 *x, *Are, *Aim, *Bre, *Bim, *Cre, *Cim, *logdt, *Dskip, *w1, *w2;
  char* ws;
  u16* out;
  const void* pad;
};

constexpr int rne_bf16_int(int l) {
  if (l < 256) return l;
  int sh = 0; while ((l >> sh) >= 256) ++sh;
  const int q = l >> sh, rem = l & ((1 << sh) - 1), half = 1 << (sh - 1);
  const int up = (rem > half) || (rem == half && (q & 1));
  return (q + up) << sh;
}
constexpr int kslot(int m) {
  const int l = 2047 - m;
  if (l < 0) return 1089;
  const int t = rne_bf16_int(l);
  if (t >= 2048) return 1088;
  const int seg = (t >= 256) + (t >= 512) + (t >= 1024);
  const int row = seg == 0 ? (t & 7) : seg == 1 ? 8 + ((t >> 1) & 3) : seg == 2 ? 12 + ((t >> 2) & 1) : 14 + ((t >> 9) & 1);
  const int b = (t >> 3) & (seg >= 2 ? 63 : 31);
  return b * 17 + row;
}
struct SlotTab { u16 v[2064]; };
constexpr SlotTab make_slot_tab() { SlotTab t{}; for (int m = 0; m < 2064; ++m) t.v[m] = (u16)(kslot(m) * 4); return t; }
__device__ __attribute__((aligned(16))) const SlotTab g_slot_tab = make_slot_tab();

static constexpr int NAUX = 32;
static constexpr int NMAIN = 224;
static constexpr int SMEM_BYTES = 160 * 1024;
static constexpr unsigned SPIN_CAP = 1u << 20;
static constexpr int LDS_SIG = SMEM_BYTES - 320;

template <int NPL> DI void wait_flags(const char* base, int n, u64 tag) {
  const int lane = threadIdx.x & 63;
  unsigned spins = 0;
  for (;;) {
    bool ok = true;
#pragma unroll
    for (int k = 0; k < NPL; ++k) { const int i = k * 64 + lane; if (i < n) ok &= (ld_agent(base + i * 8) == tag); }
    if (__all(ok)) break;
    __builtin_amdgcn_s_sleep(1);
    if (++spins > SPIN_CAP) break;
  }
}

template <int NPL> DI void loaders_wait(char* smem, int idx, int w, const char* base, int n, u64 tag) {
  volatile u32* sig = (volatile u32*)(smem + LDS_SIG) + idx;
  if (w == 0) {
    wait_flags<NPL>(base, n, tag);
    *sig = 1u;
  } else {
    unsigned spins = 0;
    while (*sig == 0u) { __builtin_amdgcn_s_sleep(1); if (++spins > (SPIN_CAP << 4)) break; }
  }
  asm volatile("" ::: "memory");
}

DI u32 tlb_touch_async(const Args& a) {
  const int lane = threadIdx.x & 63;
  const char* p;
  if (lane < 20) p = a.ws + (size_t)lane * (1u << 20);
  else if (lane < 22) p = (const char*)a.out + (size_t)(lane - 20) * (1u << 20);
  else if (lane < 24) p = (const char*)a.w1 + (size_t)(lane - 22) * (1u << 20);
  else if (lane < 26) p = (const char*)a.w2 + (size_t)(lane - 24) * (1u << 20);
  else p = (const char*)a.x;
  u32 v;
  asm volatile("global_load_dword %0, %1, off" : "=v"(v) : "v"(p) : "memory");
  return v;
}
DI void l2_touch(const char* base, int kbytes, int part, int nparts, char* smem) {
  const int lane = threadIdx.x & 63;
  const int w = __builtin_amdgcn_readfirstlane(threadIdx.x >> 6);
  const u32 dst = (u32)(size_t)(LDS3 char*)smem + 65536 + w * 1024;
  const int per = (kbytes + nparts - 1) / nparts;
  const int k0 = per * part, k1 = (k0 + per < kbytes) ? k0 + per : kbytes;
  int cnt = 0;
  for (int k = k0 + w; k < k1; k += 8) {
    glds16<false>(dst, lane * 16, base + (size_t)k * 1024);
    if ((++cnt & 7) == 0) wait_vm<32>();
  }
  wait_vm<0>();
}
static constexpr int XT_RS = 144;
static constexpr int LDS_X8 = 124 * 1024;
struct Pro { u32x4 row, tile; };
DI Pro pro_load(const Args& a, int b) {
  const int lane = threadIdx.x & 63, wv = threadIdx.x >> 6;
  Pro p; p.row = u32x4{0, 0, 0, 0}; p.tile = p.row;
  if (lane < 56) {
    p.tile = *(const u32x4*)(a.x + (size_t)(64 * (b >> 3) + 8 * wv + lane / 7) * D + 56 * (b & 7) + 8 * (lane % 7));
    p.row = *(const u32x4*)(a.x + (size_t)(8 * b + wv) * D + lane * 8);
  }
  return p;
}
DI u64 pro_store(const Args& a, int b, const Pro& p, u64 ep, char* smem) {
  const int lane = threadIdx.x & 63;
  const int wv = __builtin_amdgcn_readfirstlane(threadIdx.x >> 6);
  if (lane < 56) *(u32x4*)(smem + LDS_X8 + (8 * wv + lane / 7) * XT_RS + (lane % 7) * 16) = p.tile;
  if (threadIdx.x == 0) { *(volatile u32*)(smem + LDS_SIG) = 0u; *(volatile u32*)(smem + LDS_SIG + 4) = 0u; *(volatile u64*)(smem + LDS_SIG + 8) = ep + TAG_INC; *(volatile u32*)(smem + LDS_SIG + 48) = 0u; }
  __syncthreads();
  const u64 tag = *(volatile u64*)(smem + LDS_SIG + 8);
  if (wv < 7) {
    const int g = lane >> 4, i16 = lane & 15, q = i16 >> 2, pp = lane & 3;
    const char* src = smem + LDS_X8 + (16 * g + 8 * (pp >> 1) + q) * XT_RS + (4 * (pp & 1)) * 2 + wv * 16;
    s16x4 lo = tr_read(src), hi = tr_read(src + 4 * XT_RS);
    __builtin_amdgcn_raw_buffer_store_b128(cat(__builtin_bit_cast(u32x2, lo), __builtin_bit_cast(u32x2, hi)), ws_rsrc(a.ws),
        (u32)(OFF_XT + (size_t)(56 * (b & 7) + 8 * wv + (i16 & 7)) * LP * 2 + (64 * (b >> 3) + 16 * g + 8 * (i16 >> 3)) * 2), 0, 16);
  }
  return tag;
}
DI void pro_stats(const Args& a, int b, const Pro& p, u64 tag) {
  const int lane = threadIdx.x & 63, wv = threadIdx.x >> 6;
  float s = 0.f, q2 = 0.f;
#pragma unroll
  for (int k = 0; k < 4; ++k) {
    const float lo = bflo(p.row[k]), hi = bfhi(p.row[k]);
    s += lo; s += hi; q2 = __builtin_fmaf(lo, lo, q2); q2 = __builtin_fmaf(hi, hi, q2);
  }
  const float mean = wave_sum(s) * (1.0f / D);
  const float var = fmaxf(wave_sum(q2) * (1.0f / D) - mean * mean, 0.f);
  const float sd = rbf(__builtin_amdgcn_sqrtf(rbf(rbf(var) + 1e-5f)));
  if (lane == 0) st_agent(a.ws + OFF_ST + (8 * b + wv) * 8, ((u64)(u32)tag << 32) | pk_bf16(mean, sd));
}
DI void pro_flag(const Args& a, int b, u64 tag) {
  if (threadIdx.x == 0) st_agent(a.ws + OFF_SYNC + SY_LN + (b & 7) * 256 + (b >> 3) * 8, tag);
}
DI void aux_block(const Args& a, int blk, char* smem, const char* code0) {
  const int tid = threadIdx.x, lane = tid & 63;
  const int w = __builtin_amdgcn_readfirstlane(tid >> 6);
  const rsrc_t rs = ws_rsrc(a.ws);
  {
    const u32 dst = (u32)(size_t)(LDS3 char*)smem + 98304 + w * 1024;
    const char* src = code0 + (size_t)(blk >> 3) * 14336;
    glds16<false>(dst, lane * 16 + w * 1024, src);
    if (w < 6) glds16<false>(dst + 8192, lane * 16 + (8 + w) * 1024, src);
  }
  const Pro pr = pro_load(a, blk);
  const u64 ep = ld_agent(w == 0 ? (const char*)(a.ws + OFF_SYNC + SY_EP) : (const char*)(a.x + (size_t)(8 * blk + w) * D));
  const u64 tag = pro_store(a, blk, pr, ep, smem);
  pro_stats(a, blk, pr, tag);
  asm volatile("s_waitcnt vmcnt(0)" ::: "memory");
  __syncthreads();
  pro_flag(a, blk, tag);
  TR(a.ws, 2);
  {
    float* red = (float*)smem;
    const int cp = tid % 28, rg = tid / 28;
    float s0 = 0.f, s1 = 0.f;
    if (rg < 16) {
      u32 x[28];
#pragma unroll
      for (int i = 0; i < 28; ++i) x[i] = *(const u32*)(a.w1 + (size_t)(rg + 16 * i) * F + 56 * blk + 2 * cp);
#pragma unroll
      for (int i = 0; i < 28; ++i) { s0 += bflo(x[i]); s1 += bfhi(x[i]); }
      red[rg * 56 + 2 * cp] = s0; red[rg * 56 + 2 * cp + 1] = s1;
    }
    __syncthreads();
    if (tid < 56) {
      float s = 0.f;
#pragma unroll
      for (int i = 0; i < 16; ++i) s += red[i * 56 + tid];
      __builtin_amdgcn_raw_buffer_store_b32(__float_as_uint(s), rs, (u32)(OFF_W1S + (56 * blk + tid) * 4), 0, 16);
    }
    asm volatile("s_waitcnt vmcnt(0)" ::: "memory");
    __syncthreads();
    if (tid == 0) st_agent(a.ws + OFF_SYNC + SY_W1S + blk * 8, tag);
  }
  l2_touch((const char*)a.w1, D * F * 2 / 1024, blk >> 3, 4, smem);
  l2_touch((const char*)a.w2, D * F * 2 / 1024, blk >> 3, 4, smem);
  if (blk == 0 && w == 0) {
    wait_flags<1>(a.ws + OFF_SYNC + SY_W1S, NAUX, tag);
    wait_flags<4>(a.ws + OFF_SYNC + SY_CV, NMAIN, tag);
    if (lane == 0) st_agent(a.ws + OFF_SYNC + SY_EP, tag);
  }
}

static constexpr int CPS = 4160;
static constexpr int CH_BYTES = 61440;
static constexpr int CH_TAB = 0;
static constexpr int CH_SU = 8 * CPS;
static constexpr int CH_F = CH_SU + 4608 + 256;
static constexpr int LDS_SX = 136 * 1024;
static constexpr int CH_RED = 59392;

DI u64 phase_a(const Args& a, int m, char* smem_all) {
  const int tid = threadIdx.x & 255, lane = threadIdx.x & 63;
  const int wv = __builtin_amdgcn_readfirstlane(threadIdx.x >> 6);
  const int w = wv & 3, hw = wv >> 2;
  const int d = 2 * m + hw;
  char* smem = smem_all + hw * CH_BYTES;
  const float LOG2E = 1.4426950408889634f, INV2PI = 0.15915494309189535f;
  const u32 lds0 = (u32)(size_t)(LDS3 char*)smem;
  const Pro pr = pro_load(a, m + NAUX);
  const u64 ep = ld_agent(wv == 0 ? (const char*)(a.ws + OFF_SYNC + SY_EP) : (const char*)(a.Are + d * NM + 4 * w));
  const int n = tid;
  const u16 r_are = a.Are[d * NM + n], r_aim = a.Aim[d * NM + n], r_bre = a.Bre[d * NM + n], r_bim = a.Bim[d * NM + n];
  const u16 r_cre = a.Cre[d * NM + n], r_cim = a.Cim[d * NM + n], r_ldt = a.logdt[d], r_dsk = a.Dskip[d];
  u32 slot2[8];
  {
    const u32x4 s0 = *(const u32x4*)(g_slot_tab.v + 8 * tid), s1 = *(const u32x4*)(g_slot_tab.v + 8 * tid + 8);
#pragma unroll
    for (int k = 0; k < 4; ++k) { slot2[k] = s0[k]; slot2[4 + k] = s1[k]; }
  }
  __builtin_amdgcn_sched_barrier(0);
  const u64 tag = pro_store(a, m + NAUX, pr, ep, smem_all);
  float* red = (float*)(smem + CH_RED);
  const float dt = rbf(expf(bf2f(r_ldt)));
  const float bre = dt * bf2f(r_bre), bim = dt * bf2f(r_bim);
  const float cre = bf2f(r_cre), cim = bf2f(r_cim);
  const float zr = (dt * bf2f(r_are)) * LOG2E, zi = (dt * bf2f(r_aim)) * INV2PI;
  const float cr = cre * bre - cim * bim, ci = cre * bim + cim * bre;
  const float mx = fmaxf(fabsf(cr), fabsf(ci));
  const float lc = mx > 1e-36f ? __builtin_amdgcn_logf(mx) + 0.5f : -1000.f;
  const float sY = fmaxf(0.f, zr * 504.f);
  const float gm = fmaxf(lc + fmaxf(0.f, zr * 1536.f) + sY, lc + zr * 2048.f);
  const float G = ceilf(wave_max(gm));
  auto cexp = [&](float t, float lg) -> f32x2 {
    const float mm = __builtin_amdgcn_exp2f(__builtin_fmaf(zr, t, lg)), f = __builtin_amdgcn_fractf(zi * t);
    return f32x2{mm * __builtin_amdgcn_cosf(f), mm * __builtin_amdgcn_sinf(f)};
  };
  {
    f32x2* tb = (f32x2*)(smem + CH_TAB + n * 224);
    const f32x2 e1 = cexp(1.f, 0.f), e2 = cmul(e1, e1), e3 = cmul(e2, e1), e4 = cexp(4.f, 0.f), e6 = cmul(e3, e3);
    const f32x2 e8 = cexp(8.f, 0.f), e16 = cmul(e8, e8), e24 = cmul(e16, e8);
    const f32x2 e32 = cexp(32.f, 0.f), e64 = cmul(e32, e32), e96 = cmul(e64, e32);
    const f32x2 e128 = cexp(128.f, 0.f), e256 = cexp(256.f, 0.f), e384 = cmul(e256, e128);
    const f32x2 e512 = cexp(512.f, 0.f), e1024 = cexp(1024.f, 0.f);
    const float sy2 = __builtin_amdgcn_exp2f(-sY);
    const float cs = __builtin_amdgcn_exp2f(sY - G + 14.f);
    const f32x2 c0 = {cr * cs, ci * cs};
    *(f32x4*)(tb + 0) = f32x4{1.f, 0.f, e8[0], e8[1]};      *(f32x4*)(tb + 2) = f32x4{e16[0], e16[1], e24[0], e24[1]};
    *(f32x4*)(tb + 4) = f32x4{sy2, 0.f, e32[0] * sy2, e32[1] * sy2}; *(f32x4*)(tb + 6) = f32x4{e64[0] * sy2, e64[1] * sy2, e96[0] * sy2, e96[1] * sy2};
    const f32x2 p1 = cmul(c0, e4), p2 = cmul(c0, e256), p3 = cmul(c0, e512);
    *(f32x4*)(tb + 8) = f32x4{c0[0], c0[1], p1[0], p1[1]};  *(f32x4*)(tb + 10) = f32x4{p2[0], p2[1], p3[0], p3[1]};
    *(f32x4*)(tb + 12) = f32x4{1.f, 0.f, e1[0], e1[1]};     *(f32x4*)(tb + 14) = f32x4{e2[0], e2[1], e3[0], e3[1]};
    *(f32x4*)(tb + 16) = f32x4{1.f, 0.f, e2[0], e2[1]};     *(f32x4*)(tb + 18) = f32x4{e4[0], e4[1], e6[0], e6[1]};
    *(f32x4*)(tb + 20) = f32x4{1.f, 0.f, e4[0], e4[1]};     *(f32x4*)(tb + 22) = f32x4{e512[0], e512[1], e1024[0], e1024[1]};
    *(f32x4*)(tb + 24) = f32x4{e128[0], e128[1], e256[0], e256[1]}; *(f32x4*)(tb + 26) = f32x4{e384[0], e384[1], 0.f, 0.f};
    const f32x2 e2048 = cexp(2048.f, 14.f - G);
    float t2048 = cr * e2048[0] - ci * e2048[1];
    t2048 = wave_sum(t2048);
    if (lane == 0) { red[w] = G; red[4 + w] = t2048; }
  }
  asm volatile("s_waitcnt vmcnt(0) lgkmcnt(0)" ::: "memory");
  __syncthreads();
  pro_flag(a, m + NAUX, tag);
  TR(a.ws, 1);
  pro_stats(a, m + NAUX, pr, tag);
  const int c = lane & 15, g = lane >> 4;
  const rsrc_t rs = ws_rsrc(a.ws);
  u32x4 gr[4];
  const char* flg = a.ws + OFF_SYNC + SY_LN + ((2 * m) / 56) * 256 + (lane & 31) * 8;
  u64 fl = 0;
  bool have = (wv != 0);
  auto load_x = [&]() {
    TR(a.ws, 13);
#pragma unroll
    for (int i = 0; i < 4; ++i) {
      glds16<XSC1>(lds0 + LDS_SX + i * 1024, lane * 16 + i * 1024, a.ws + OFF_XT + (size_t)(2 * m) * LP * 2);
      glds16<XSC1>(lds0 + LDS_SX + 4096 + i * 1024, lane * 16 + i * 1024, a.ws + OFF_XT + (size_t)(2 * m + 1) * LP * 2);
    }
  };
  auto poll = [&]() { if (wv == 0 && !have) fl = ld_agent(flg); };
  auto check = [&]() {
    if (wv == 0 && !have) {
      if (__all(fl == tag)) { have = true; load_x(); }
    }
  };
  auto load_st = [&]() {
#pragma unroll
    for (int k = 0; k < 4; ++k) gr[k] = __builtin_amdgcn_raw_buffer_load_b128(rs, (u32)(OFF_ST + tid * 64 + k * 16), 0, 16);
  };
  auto load_st_fast = [&]() {
#pragma unroll
    for (int k = 0; k < 4; ++k) gr[k] = __builtin_amdgcn_raw_buffer_load_b128(rs, (u32)(OFF_ST + tid * 64 + k * 16), 0, 0);
  };
  const int r = c;
  f32x4 acc[4];
#pragma unroll
  for (int k = 0; k < 4; ++k) acc[k] = f32x4{0.f, 0.f, 0.f, 0.f};
  {
    const char* tb = smem + CH_TAB + (64 * w + 4 * g) * 224;
    const char* pA = tb + (c & 3) * 8;
    const char* pB = tb + 32 + (c >> 2) * 8;
    const char* pP = tb + 64 + (r >> 2) * 8;
    const char* pQ = tb + 96 + ((r < 8 ? 0 : r < 12 ? 4 : 8) + (r & 3)) * 8;
    const char* pE = tb + 192;
    f32x2 tA[2][4], tB[2][4], tP[2][4], tQ[2][4], tE3[2][4];
    f32x4 tE[2][4];
    auto ldtab = [&](auto sc_) {
      constexpr int s = decltype(sc_)::value;
#pragma unroll
      for (int i = 0; i < 4; ++i) {
        const int o = (16 * s + i) * 224;
        tA[s & 1][i] = *(const f32x2*)(pA + o); tB[s & 1][i] = *(const f32x2*)(pB + o);
        tP[s & 1][i] = *(const f32x2*)(pP + o); tQ[s & 1][i] = *(const f32x2*)(pQ + o);
        tE[s & 1][i] = *(const f32x4*)(pE + o); tE3[s & 1][i] = *(const f32x2*)(pE + o + 16);
      }
    };
    auto kstep = [&](auto sc_) {
      constexpr int s = decltype(sc_)::value;
      if constexpr (s < 3) ldtab(IC<s + 1>{});
      __builtin_amdgcn_sched_barrier(0);
      u32x4 xf, y0, y1, y2, y3;
#pragma unroll
      for (int i = 0; i < 4; ++i) {
        const f32x2 yv = cmul(tA[s & 1][i], tB[s & 1][i]);
        const f32x2 xv = cmul_conj(tP[s & 1][i], tQ[s & 1][i]);
        y0[i] = pk_f16(yv[0], yv[1]);
        xf[i] = pk_f16(xv[0], xv[1]);
        const f32x4 E12 = tE[s & 1][i];
        const f32x2 v1 = cmul(yv, f32x2{E12[0], E12[1]}), v2 = cmul(yv, f32x2{E12[2], E12[3]}), v3 = cmul(yv, tE3[s & 1][i]);
        y1[i] = pk_f16(v1[0], v1[1]); y2[i] = pk_f16(v2[0], v2[1]); y3[i] = pk_f16(v3[0], v3[1]);
      }
      f16x8 xa = __builtin_bit_cast(f16x8, xf);
      __builtin_amdgcn_sched_barrier(0);
      acc[0] = __builtin_amdgcn_mfma_f32_16x16x32_f16(xa, __builtin_bit_cast(f16x8, y0), acc[0], 0, 0, 0);
      acc[1] = __builtin_amdgcn_mfma_f32_16x16x32_f16(xa, __builtin_bit_cast(f16x8, y1), acc[1], 0, 0, 0);
      acc[2] = __builtin_amdgcn_mfma_f32_16x16x32_f16(xa, __builtin_bit_cast(f16x8, y2), acc[2], 0, 0, 0);
      acc[3] = __builtin_amdgcn_mfma_f32_16x16x32_f16(xa, __builtin_bit_cast(f16x8, y3), acc[3], 0, 0, 0);
      __builtin_amdgcn_sched_barrier(0);
      if constexpr (s == 1) poll();
      __builtin_amdgcn_sched_barrier(0);
    };
    ldtab(IC<0>{});
    static_for<0, 4>(kstep);
  }
  check();
  poll();
  TR(a.ws, 2);
  float* part = (float*)(smem + CH_TAB);
#pragma unroll
  for (int k = 0; k < 4; ++k) *(f32x4*)(part + w * 3584 + (16 * k + r) * 16 + 4 * g) = acc[k];
  __syncthreads();
  load_st_fast();
  float* Fv = (float*)(smem + CH_F);
  const float Gm = fmaxf(fmaxf(red[0], red[1]), fmaxf(red[2], red[3]));
  float sc[4];
#pragma unroll
  for (int k = 0; k < 4; ++k) sc[k] = __builtin_amdgcn_exp2f(red[k] - Gm - 8.f);
  {
    const f32x4* pp = (const f32x4*)part;
    f32x4 sv = pp[tid] * sc[0] + pp[896 + tid] * sc[1] + pp[1792 + tid] * sc[2] + pp[2688 + tid] * sc[3];
    float* fo = Fv + (tid >> 2) * 17 + 4 * (tid & 3);
    fo[0] = sv[0]; fo[1] = sv[1]; fo[2] = sv[2]; fo[3] = sv[3];
    if (tid == 0) { Fv[1088] = red[4] * sc[0] + red[5] * sc[1] + red[6] * sc[2] + red[7] * sc[3]; Fv[1089] = 0.f; }
  }
  const float kinv = __builtin_amdgcn_exp2f(Gm - 6.f);
  check();
  __syncthreads();
  TR(a.ws, 3);
  {
    u32 e[8];
#pragma unroll
    for (int k = 0; k < 8; ++k)
      e[k] = pk_f16(*(const float*)((const char*)Fv + (slot2[k] & 0xffffu)), *(const float*)((const char*)Fv + (slot2[k] >> 16)));
    auto put = [&](int ch, const u32 (&ee)[8]) {
      u32 o[7];
#pragma unroll
      for (int i = 0; i < 7; ++i) o[i] = __builtin_amdgcn_alignbyte(ee[i + 1], ee[i], 2);
#pragma unroll
      for (int s = 0; s < 4; ++s) {
        *(u32x4*)(smem + (2 * s) * CPS + ch * 16) = u32x4{ee[s], ee[s + 1], ee[s + 2], ee[s + 3]};
        *(u32x4*)(smem + (2 * s + 1) * CPS + ch * 16) = u32x4{o[s], o[s + 1], o[s + 2], o[s + 3]};
      }
    };
    put(tid, e);
    if (tid == 255) { const u32 z[8] = {0, 0, 0, 0, 0, 0, 0, 0}; put(256, z); }
  }
  u16* su = (u16*)(smem + CH_SU);
  const u16* sx = (const u16*)(smem_all + LDS_SX + hw * 4096);
  if (wv == 0) {
    if (!have) {
      unsigned spins = 0;
      while (!__all(fl == tag)) { __builtin_amdgcn_s_sleep(1); fl = ld_agent(flg); if (++spins > SPIN_CAP) break; }
      load_x();
    }
    asm volatile("s_waitcnt vmcnt(0)" ::: "memory");
    TR(a.ws, 14);
  }
  {
    const u32 tg = (u32)tag;
    unsigned spins = 0;
    for (;;) {
      bool ok = true;
#pragma unroll
      for (int k = 0; k < 4; ++k) ok &= (gr[k][1] == tg) && (gr[k][3] == tg);
      if (__all(ok)) break;
      __builtin_amdgcn_s_sleep(1);
      load_st();
      if (++spins > SPIN_CAP) break;
    }
  }
  __syncthreads();
  TR(a.ws, 4);
  {
    const u32x4 xr = *(const u32x4*)(sx + tid * 8);
    u32x4 o;
#pragma unroll
    for (int k = 0; k < 4; ++k) {
      const u32 s0 = gr[k][0], s1 = gr[k][2];
      const u32 dpk = pk_bf16(bflo(xr[k]) - bflo(s0), bfhi(xr[k]) - bflo(s1));
      const u32 upk = pk_bf16(bflo(dpk) * __builtin_amdgcn_rcpf(bfhi(s0)), bfhi(dpk) * __builtin_amdgcn_rcpf(bfhi(s1)));
      o[k] = pk_f16(bflo(upk), bfhi(upk));
    }
    *(u32x4*)(su + 256 + tid * 8) = o;
    if (tid < 32) *(u32x4*)(su + tid * 8) = u32x4{0, 0, 0, 0};
  }
  __syncthreads();
  TR(a.ws, 5);
  f32x4 cv[8];
#pragma unroll
  for (int q = 0; q < 8; ++q) cv[q] = f32x4{0.f, 0.f, 0.f, 0.f};
  const int sig = (7 - c) & 7;
  const char* abase = smem + sig * CPS + 16 * ((c < 8 ? 253 : 252) + g);
#pragma unroll
  for (int rr2 = 0; rr2 < 2; ++rr2) {
    const int rs2 = 2 * w + rr2;
    f16x8 B[8];
#pragma unroll
    for (int q = 0; q < 8; ++q) B[q] = *(const f16x8*)(su + 240 + 256 * q - 32 * rs2 + 16 * c + 8 * g);
    f16x8 A[8];
#pragma unroll
    for (int k = 0; k < 8; ++k) A[k] = *(const f16x8*)(abase - 64 * (rs2 + 8 * k));
#pragma unroll
    for (int k = 0; k < 8; ++k)
#pragma unroll
      for (int q = k; q < 8; ++q) cv[q] = __builtin_amdgcn_mfma_f32_16x16x32_f16(A[k], B[q - k], cv[q], 0, 0, 0);
  }
  TR(a.ws, 6);
  __syncthreads();
  f32x4* rb = (f32x4*)smem;
#pragma unroll
  for (int q = 0; q < 8; ++q)
    if ((q >> 1) != w) rb[(w * 8 + q) * 64 + lane] = cv[q];
  __syncthreads();
  const float dsk = bf2f(r_dsk);
  u32x2 hh[2];
#pragma unroll
  for (int i = 0; i < 2; ++i) {
    f32x4 y = cv[0];
#pragma unroll
    for (int q = 0; q < 8; ++q) if (q == 2 * w + i) y = cv[q];
    const int q = 2 * w + i;
#pragma unroll
    for (int s = 1; s < 4; ++s) y += rb[(((w + s) & 3) * 8 + q) * 64 + lane];
    const int t = 256 * q + 16 * c + 4 * g;
    u32x2 uu = *(const u32x2*)(su + 256 + t);
    const u32x2 xx = *(const u32x2*)(sx + t);
    f16x2 ua = as_f16x2(uu[0]), ub = as_f16x2(uu[1]);
    float uf[4] = {(float)ua[0], (float)ua[1], (float)ub[0], (float)ub[1]};
    u32 y01 = pk_bf16(y[0] * kinv, y[1] * kinv), y23 = pk_bf16(y[2] * kinv, y[3] * kinv);
    u32 s01 = pk_bf16(uf[0] * dsk, uf[1] * dsk), s23 = pk_bf16(uf[2] * dsk, uf[3] * dsk);
    u32 r01 = pk_bf16(bflo(y01) + bflo(s01), bfhi(y01) + bfhi(s01));
    u32 r23 = pk_bf16(bflo(y23) + bflo(s23), bfhi(y23) + bfhi(s23));
    hh[i][0] = pk_bf16(bflo(xx[0]) + bflo(r01), bfhi(xx[0]) + bfhi(r01));
    hh[i][1] = pk_bf16(bflo(xx[1]) + bflo(r23), bfhi(xx[1]) + bfhi(r23));
  }
  {
    const auto r0 = __builtin_amdgcn_permlane16_swap(hh[0][0], hh[1][0], false, false);
    const auto r1 = __builtin_amdgcn_permlane16_swap(hh[0][1], hh[1][1], false, false);
    const int t = 256 * (2 * w + (g & 1)) + 16 * c + 8 * (g >> 1);
    __builtin_amdgcn_raw_buffer_store_b128(u32x4{r0[0], r1[0], r0[1], r1[1]}, rs, (u32)(OFF_HT + (size_t)d * LP * 2 + t * 2), 0, 16);
  }
  asm volatile("s_waitcnt vmcnt(0) lgkmcnt(0)" ::: "memory");
  __syncthreads();
  if (threadIdx.x < 8) st_agent(a.ws + OFF_SYNC + SY_CV + threadIdx.x * 2048 + m * 8, tag);
  TR(a.ws, 7);
  return tag;
}

static constexpr int G1_R = 8;
static constexpr int G1_STG = 16384;
static constexpr int G1_ST = G1_R * G1_STG;

constexpr int g1_cnt(int n) {
  if (n < 0) return 0;
  const int ks = n >> 2, c = n & 3;
  return (n + 4 < 56 ? 2 : 0) + ((c < 2 && ks + 1 < 14) ? 4 : 0);
}
constexpr int g1_wait(int n) {
  const int c = n & 3;
  const int breq = (g1_cnt(n - 4) - 2) + g1_cnt(n - 3) + g1_cnt(n - 2) + g1_cnt(n - 1);
  const int areq = g1_cnt(n - 2) + g1_cnt(n - 1);
  return (c == 0 && areq < breq) ? areq : breq;
}

DI void c_prefetch_w(const Args& a, int m, char* smem);
DI void phase_b(const Args& a, int m, char* smem, u64 tag) {
  const int tid = fresh_tid(), lane = tid & 63;
  const int wv = __builtin_amdgcn_readfirstlane(tid >> 6);
  const int w = wv & 3;
  const int xcd = m & 7, bi = m >> 3;
  const int tb = 2 * xcd + bi / 14, fb = bi % 14;
  const int c16 = lane & 15, g = lane >> 4, q = (lane & 15) >> 2, p = lane & 3;
  const u32 lds0 = (u32)(size_t)(LDS3 char*)smem;
  f32x2* st = (f32x2*)(smem + G1_ST);
  char* ws = a.ws;

  if (wv >= 4) {
    u32 vh[2], vw[2];
#pragma unroll
    for (int k = 0; k < 2; ++k) {
      const int row = 8 * w + 4 * k + g;
      vh[k] = (u32)(row * LP * 2) + (u32)((c16 ^ swz(row)) * 16);
      vw[k] = (u32)(row * F * 2) + (u32)((c16 ^ swz(row)) * 16);
    }
    const char* hb = ws + OFF_HT + tb * 256;
    const char* wb = (const char*)a.w1 + fb * 256;
    auto ldw = [&](auto kc) {
      constexpr int ks = decltype(kc)::value;
      constexpr int sl = (ks % G1_R) * G1_STG + 8192;
      glds16<false>(lds0 + sl + (8 * w) * 256, vw[0], wb + (size_t)ks * 32 * F * 2);
      glds16<false>(lds0 + sl + (8 * w + 4) * 256, vw[1], wb + (size_t)ks * 32 * F * 2);
    };
    auto ldh = [&](auto kc) {
      constexpr int ks = decltype(kc)::value;
      constexpr int sl = (ks % G1_R) * G1_STG;
      glds16<true>(lds0 + sl + (8 * w) * 256, vh[0], hb + (size_t)ks * 32 * LP * 2);
      glds16<true>(lds0 + sl + (8 * w + 4) * 256, vh[1], hb + (size_t)ks * 32 * LP * 2);
    };
    static_for<0, G1_R>(ldw);
    if (w == 0) wait_flags<1>(ws + OFF_SYNC + SY_W1S, NAUX, tag);
    loaders_wait<4>(smem, 0, w, ws + OFF_SYNC + SY_CV + xcd * 2048, NMAIN, tag);
    TRW(ws, 8, tid == 256);
    if (w == 0) glds16<true>(lds0 + G1_ST + 1024, lane * 16, ws + OFF_W1S + fb * 512);
    static_for<0, G1_R>(ldh);
    auto stepl = [&](auto kc) {
      constexpr int ks = decltype(kc)::value;
      constexpr int refills = ks > 3 ? (ks - 3 < 14 - G1_R ? ks - 3 : 14 - G1_R) : 0;
      constexpr int need_after = ks < G1_R ? (2 * (G1_R - 1 - ks) + 4 * refills) : (4 * (G1_R + refills - 1 - ks));
      wait_vm<need_after>();
      bar();
      if constexpr (ks >= 3 && ks - 3 + G1_R < 14) { ldh(IC<ks - 3 + G1_R>{}); ldw(IC<ks - 3 + G1_R>{}); }
    };
    static_for<0, 14>(stepl);
    bar();
    bar();
    __builtin_amdgcn_s_sleep(12);
    c_prefetch_w(a, m, smem);
    return;
  }
  const int fi = w >> 1, ti = w & 1;
  f32x4 acc[4][4];
  float s1[2] = {0.f, 0.f}, s2[2] = {0.f, 0.f};
  const bf16x2 ones = __builtin_bit_cast(bf16x2, 0x3f803f80u);
  u32 adB[2][4], adA[2][4];
#pragma unroll
  for (int t = 0; t < 2; ++t) {
    const int row = 8 * g + 4 * t + q;
    const u32 a0 = lds0 + (u32)(row * 256) + (((u32)(p >> 1) ^ swz(row)) << 4) + 8 * (p & 1);
#pragma unroll
    for (int c = 0; c < 4; ++c) {
      adB[t][c] = a0 ^ (u32)(32 * (4 * ti + ((2 * fi + c) & 3)));
      adA[t][c] = (a0 ^ (u32)(32 * (4 * fi + c))) + 8192;
    }
  }
  u32x2 B[8][2], A[2][4][2];
  u32x4 Af[4];
  u32 hoff[2];
#pragma unroll
  for (int c = 0; c < 2; ++c)
    hoff[c] = (u32)(OFF_HN + ((size_t)(tb * 128 + 16 * (4 * ti + ((2 * fi + c) & 3)) + c16) * D + 32 * fb + 8 * g) * 2);
  auto rdB = [&](auto nc, auto tc) {
    constexpr int n = decltype(nc)::value, t = decltype(tc)::value, sl = (n >> 2) % G1_R;
    trread<(sl & 3) * G1_STG>(B[n & 7][t], adB[t][n & 3]);
  };
  auto rdA = [&](auto kc, auto ic, auto tc) {
    constexpr int ks = decltype(kc)::value, i = decltype(ic)::value, t = decltype(tc)::value, sl = ks % G1_R;
    trread<(sl & 3) * G1_STG>(A[ks & 1][i][t], adA[t][i]);
  };
  auto group = [&](auto nc) {
    constexpr int n = decltype(nc)::value;
    constexpr int ks = n >> 2, c = n & 3, sl = n & 7;
    constexpr bool nb = n + 4 < 56, na = (c < 2) && (ks + 1 < 14);
    constexpr int wt = n < 4 ? (n == 0 ? 6 : n == 1 ? 10 : n == 2 ? 14 : 14) : g1_wait(n);
    if constexpr (c == 0) {
      asm volatile("s_waitcnt lgkmcnt(%10)" : "+v"(B[sl][0]), "+v"(B[sl][1]), "+v"(A[ks & 1][0][0]), "+v"(A[ks & 1][0][1]), "+v"(A[ks & 1][1][0]), "+v"(A[ks & 1][1][1]),
                   "+v"(A[ks & 1][2][0]), "+v"(A[ks & 1][2][1]), "+v"(A[ks & 1][3][0]), "+v"(A[ks & 1][3][1]) : "n"(wt) : "memory");
#pragma unroll
      for (int i = 0; i < 4; ++i) Af[i] = cat(A[ks & 1][i][0], A[ks & 1][i][1]);
    } else {
      asm volatile("s_waitcnt lgkmcnt(%2)" : "+v"(B[sl][0]), "+v"(B[sl][1]) : "n"(wt) : "memory");
    }
    const u32x4 bfr = cat(B[sl][0], B[sl][1]);
    mfma_bf16<ks == 0, true>(acc[0][c], Af[0], bfr);
    __builtin_amdgcn_sched_barrier(0);
    if constexpr (c == 0 && ks + 1 < 14) bar();
    if constexpr (c == 0 && ((ks + 1) & 3) == 0) {
      const u32 dl = (((ks + 1) >> 2) & 1) ? 65536u : (u32)-65536;
#pragma unroll
      for (int t = 0; t < 2; ++t)
#pragma unroll
        for (int cc = 0; cc < 4; ++cc) { adB[t][cc] += dl; adA[t][cc] += dl; }
    }
    if constexpr (nb) rdB(IC<n + 4>{}, IC<0>{});
    __builtin_amdgcn_sched_barrier(0);
    mfma_bf16<ks == 0, false>(acc[1][c], Af[1], bfr);
    __builtin_amdgcn_sched_barrier(0);
    if constexpr (nb) rdB(IC<n + 4>{}, IC<1>{});
    if constexpr (na) rdA(IC<ks + 1>{}, IC<2 * c>{}, IC<0>{});
    __builtin_amdgcn_sched_barrier(0);
    mfma_bf16<ks == 0, false>(acc[2][c], Af[2], bfr);
    __builtin_amdgcn_sched_barrier(0);
    if constexpr (na) { rdA(IC<ks + 1>{}, IC<2 * c>{}, IC<1>{}); rdA(IC<ks + 1>{}, IC<2 * c + 1>{}, IC<0>{}); }
    if constexpr (c < 2) {
#pragma unroll
      for (int k = 0; k < 2; ++k) {
        bf16x2 x2 = as_bf16x2(bfr[k]);
        s1[c] = __builtin_amdgcn_fdot2_f32_bf16(x2, ones, s1[c], false);
        s2[c] = __builtin_amdgcn_fdot2_f32_bf16(x2, x2, s2[c], false);
      }
    }
    __builtin_amdgcn_sched_barrier(0);
    mfma_bf16<ks == 0, false>(acc[3][c], Af[3], bfr);
    __builtin_amdgcn_sched_barrier(0);
    if constexpr (na) rdA(IC<ks + 1>{}, IC<2 * c + 1>{}, IC<1>{});
    if constexpr (c < 2) {
#pragma unroll
      for (int k = 2; k < 4; ++k) {
        bf16x2 x2 = as_bf16x2(bfr[k]);
        s1[c] = __builtin_amdgcn_fdot2_f32_bf16(x2, ones, s1[c], false);
        s2[c] = __builtin_amdgcn_fdot2_f32_bf16(x2, x2, s2[c], false);
      }
      if (ks == fb) __builtin_amdgcn_raw_buffer_store_b128(bfr, ws_rsrc(ws), hoff[c], 0, 16);
    }
    __builtin_amdgcn_sched_barrier(0);
  };
  bar();
  static_for<0, 4>([&](auto ic) { rdA(IC<0>{}, ic, IC<0>{}); rdA(IC<0>{}, ic, IC<1>{}); });
  rdB(IC<0>{}, IC<0>{}); rdB(IC<0>{}, IC<1>{}); rdB(IC<1>{}, IC<0>{}); rdB(IC<1>{}, IC<1>{});
  asm volatile("s_waitcnt lgkmcnt(4)" ::: "memory");
  rdB(IC<2>{}, IC<0>{}); rdB(IC<2>{}, IC<1>{}); rdB(IC<3>{}, IC<0>{}); rdB(IC<3>{}, IC<1>{});
  static_for<0, 56>(group);
  asm volatile("s_nop 7\n\ts_nop 7" ::: "memory");
  TR(ws, 9);
  bar();
  const int lane2 = fresh_tid() & 63, c16b = lane2 & 15;
#pragma unroll
  for (int i = 0; i < 2; ++i) {
    float sa = s1[i], sb = s2[i];
    sa += __shfl_xor(sa, 16); sa += __shfl_xor(sa, 32);
    sb += __shfl_xor(sb, 16); sb += __shfl_xor(sb, 32);
    const float mean = sa * (1.0f / D);
    const float var = fmaxf(sb * (1.0f / D) - mean * mean, 0.f);
    const float mu = rbf(mean);
    const float sd = rbf(__builtin_amdgcn_sqrtf(rbf(rbf(var) + 1e-5f)));
    const int tt = 4 * ti + ((2 * fi + i) & 3);
    if (lane2 < 16) st[tt * 16 + lane2] = f32x2{mu, __builtin_amdgcn_rcpf(sd)};
  }
  lds_bar();
  f32x4 wsv[4];
#pragma unroll
  for (int i = 0; i < 4; ++i) wsv[i] = *(const f32x4*)(smem + G1_ST + 1024 + (64 * fi + 4 * (lane2 >> 4) + 16 * i) * 4);
#pragma unroll
  for (int c = 0; c < 4; ++c) {
    const int tt = 4 * ti + ((2 * fi + c) & 3);
    const f32x2 ms = st[tt * 16 + c16b];
#pragma unroll
    for (int pp = 0; pp < 2; ++pp) {
      float v0[4], v1[4];
#pragma unroll
      for (int k = 0; k < 4; ++k) {
        v0[k] = fmaxf((acc[2 * pp][c][k] - ms[0] * wsv[2 * pp][k]) * ms[1], 0.f);
        v1[k] = fmaxf((acc[2 * pp + 1][c][k] - ms[0] * wsv[2 * pp + 1][k]) * ms[1], 0.f);
      }
      u32x4 o = {pk_bf16(v0[0], v0[1]), pk_bf16(v0[2], v0[3]), pk_bf16(v1[0], v1[1]), pk_bf16(v1[2], v1[3])};
      __builtin_amdgcn_raw_buffer_store_b128(o, ws_rsrc(ws), (u32)(OFF_H1F + (((size_t)(tb * 8 + tt) * 56 + fb * 4 + 2 * fi + pp) * 64 + lane2) * 16), 0, 16);
    }
  }
  asm volatile("s_waitcnt vmcnt(0)" ::: "memory");
  if (lane2 == 0) {
    const u32 old = __hip_atomic_fetch_add((LDS3 u32*)(smem + LDS_SIG + 48), 1u, __ATOMIC_RELAXED, __HIP_MEMORY_SCOPE_WORKGROUP);
    if (old == 3u) st_agent(ws + OFF_SYNC + SY_H1 + tb * 128 + fb * 8, tag);
  }
  TR(ws, 10);
}

static constexpr int G2_NR = 5;
static constexpr int G2_RS = 272;
constexpr int g2_waddr(int k) { return k < 5 ? k * 16384 : k < 8 ? 81920 + (k - 5) * 16384 : k == 8 ? 133120 : k < 11 ? (k - 9) * 16384 : 81920 + (k - 11) * 16384; }
constexpr int g2_haddr(int k) { return 32768 + (k % 3) * 16384; }
constexpr int g2_after(int k) {
  int seq[40] = {}; int n = 0;
  seq[n++] = 109; seq[n++] = 110; seq[n++] = 0; seq[n++] = 1; seq[n++] = 2;
  for (int j = 0; j < k; ++j) {
    if (j >= 1 && j + 2 < 14) seq[n++] = j + 2;
    if (j >= 6 && j <= 8) seq[n++] = 100 + j + 5;
  }
  int last = -1;
  for (int i = 0; i < n; ++i) if (seq[i] == k || seq[i] == 100 + k) last = i;
  return n - 1 - last;
}
struct CW { u32 vw[4]; const char* wb; u32 lds0; int w; };
DI CW c_wsetup(const Args& a, int m, char* smem) {
  const int lane = fresh_tid() & 63;
  const int w = __builtin_amdgcn_readfirstlane(threadIdx.x >> 6) & 3;
  const int fb = (m >> 3) % 14, db = fb % 7;
  CW c; c.w = w; c.lds0 = (u32)(size_t)(LDS3 char*)smem;
  const int rr = lane >> 3;
#pragma unroll
  for (int i = 0; i < 4; ++i) {
    const int row = 8 * i + rr;
    c.vw[i] = (u32)(row * D * 2) + ((((u32)(lane & 7)) ^ (u32)(((row >> 1) & 3) << 1)) << 4);
  }
  c.wb = (const char*)a.w2 + ((size_t)(448 * w) * D + db * 64) * 2;
  return c;
}
template <int K> DI void c_ldw(const CW& c) {
#pragma unroll
  for (int i = 0; i < 4; ++i) glds16<false>(c.lds0 + g2_waddr(K) + c.w * 4096 + i * 1024, c.vw[i], c.wb + (size_t)K * 32 * D * 2);
}
DI void c_prefetch_w(const Args& a, int m, char* smem) {
  const CW c = c_wsetup(a, m, smem);
  static_for<0, 9>([&](auto kc) { c_ldw<decltype(kc)::value>(c); });
}

DI void phase_c(const Args& a, int m, char* smem, u64 tag) {
  const int tid = fresh_tid(), lane = tid & 63;
  const int wv = __builtin_amdgcn_readfirstlane(tid >> 6);
  const int w = wv & 3;
  const int xcd = m & 7, bi = m >> 3;
  const int tb = 2 * xcd + bi / 14, fb = bi % 14;
  const int t64 = 2 * tb + fb / 7, db = fb % 7;
  const u32 lds0 = (u32)(size_t)(LDS3 char*)smem;
  char* ws = a.ws;
  const int c16 = lane & 15, g = lane >> 4, q = (lane & 15) >> 2, p = lane & 3;
  if (wv >= 4) {
    const CW c = c_wsetup(a, m, smem);
    u32 vo[4];
#pragma unroll
    for (int i = 0; i < 4; ++i) vo[i] = lane * 16 + i * 56 * 1024;
    const char* hb = ws + OFF_H1F + ((size_t)(t64 * 4) * 56 + 14 * w) * 1024;
    auto ldh = [&](auto kc) {
      constexpr int k = decltype(kc)::value;
#pragma unroll
      for (int i = 0; i < 4; ++i) glds16<true>(lds0 + g2_haddr(k) + w * 4096 + i * 1024, vo[i], hb + k * 1024);
    };
    wait_vm<0>();
    bar();
    bar();
    c_ldw<9>(c); c_ldw<10>(c);
    loaders_wait<1>(smem, 1, w, ws + OFF_SYNC + SY_H1 + tb * 128, 14, tag);
    TRW(ws, 11, tid == 256);
    ldh(IC<0>{}); ldh(IC<1>{}); ldh(IC<2>{});
    auto stepl = [&](auto kc) {
      constexpr int k = decltype(kc)::value;
      wait_vm<4 * g2_after(k)>();
      bar();
      if constexpr (k >= 1 && k + 2 < 14) ldh(IC<k + 2>{});
      if constexpr (k >= 6 && k <= 8) c_ldw<k + 5>(c);
    };
    static_for<0, 14>(stepl);
    bar();
    bar();
    return;
  }
  u32x4 res[2];
  f32x4 acc[4][4];
  u32 adW[4];
  {
    const int row = 4 * g + q;
    const u32 f = (u32)((row >> 1) & 3);
    const u32 a0 = lds0 + w * 4096 + (u32)(row * 128) + (u32)((p >> 1) * 16 + 8 * (p & 1));
#pragma unroll
    for (int dl = 0; dl < 4; ++dl) adW[dl] = a0 + ((((u32)dl) ^ f) << 5);
  }
  const u32 adH = lds0 + w * 4096 + lane * 16;
  u32x2 Wf[2][4][2];
  u32x4 Hf[2][4], Wr[G2_NR][4];
  auto rdW = [&](auto kc, auto ic, u32x2& dst) {
    constexpr int k = decltype(kc)::value, i = decltype(ic)::value;
    constexpr int ad = g2_waddr(k);
    trread<(ad & 65535) + (i & 1) * 2048>(dst, adW[i >> 1] + (u32)(ad & ~65535));
  };
  bar();
  static_for<0, G2_NR>([&](auto kc) {
    constexpr int k = decltype(kc)::value;
    u32x2 t[8];
    static_for<0, 8>([&](auto ic) { rdW(kc, ic, t[decltype(ic)::value]); });
    asm volatile("s_waitcnt lgkmcnt(0)" : "+v"(t[0]), "+v"(t[1]), "+v"(t[2]), "+v"(t[3]), "+v"(t[4]), "+v"(t[5]), "+v"(t[6]), "+v"(t[7]) :: "memory");
#pragma unroll
    for (int i = 0; i < 4; ++i) Wr[k][i] = cat(t[2 * i], t[2 * i + 1]);
  });
  bar();
  auto rd = [&](auto kc, auto ic) {
    constexpr int k = decltype(kc)::value, i = decltype(ic)::value;
    if constexpr (i < 4) ldsread128<(g2_haddr(k) & 65535) + i * 1024>(Hf[k & 1][i], adH + (u32)(g2_haddr(k) & ~65535));
    else if constexpr (k >= G2_NR) rdW(kc, IC<i - 4>{}, Wf[k & 1][(i - 4) >> 1][(i - 4) & 1]);
  };
  auto step = [&](auto kc) {
    constexpr int k = decltype(kc)::value;
    u32x4 Wq[4];
    if constexpr (k >= G2_NR) {
      asm volatile("s_waitcnt lgkmcnt(0)" : "+v"(Wf[k & 1][0][0]), "+v"(Wf[k & 1][0][1]), "+v"(Wf[k & 1][1][0]), "+v"(Wf[k & 1][1][1]), "+v"(Wf[k & 1][2][0]), "+v"(Wf[k & 1][2][1]),
                   "+v"(Wf[k & 1][3][0]), "+v"(Wf[k & 1][3][1]), "+v"(Hf[k & 1][0]), "+v"(Hf[k & 1][1]), "+v"(Hf[k & 1][2]), "+v"(Hf[k & 1][3]) :: "memory");
#pragma unroll
      for (int i = 0; i < 4; ++i) Wq[i] = cat(Wf[k & 1][i][0], Wf[k & 1][i][1]);
    } else {
      asm volatile("s_waitcnt lgkmcnt(0)" : "+v"(Hf[k & 1][0]), "+v"(Hf[k & 1][1]), "+v"(Hf[k & 1][2]), "+v"(Hf[k & 1][3]) :: "memory");
#pragma unroll
      for (int i = 0; i < 4; ++i) Wq[i] = Wr[k][i];
    }
    if constexpr (k + 1 < 14) bar();
    static_for<0, 16>([&](auto jc) {
      constexpr int j = decltype(jc)::value, dl = j >> 2, ta = j & 3;
      mfma_bf16<k == 0, j == 0>(acc[dl][ta], Wq[dl], Hf[k & 1][ta]);
      __builtin_amdgcn_sched_barrier(0);
      if constexpr (k + 1 < 14 && j < 12) rd(IC<k + 1>{}, IC<j>{});
      __builtin_amdgcn_sched_barrier(0);
    });
  };
  bar();
  static_for<0, 4>([&](auto ic) { rd(IC<0>{}, ic); });
  {
    const rsrc_t rs = ws_rsrc(ws);
#pragma unroll
    for (int i = 0; i < 2; ++i)
      res[i] = __builtin_amdgcn_raw_buffer_load_b128(rs, (u32)(OFF_HN + ((size_t)(t64 * 64 + 32 * i + (tid >> 3)) * D + db * 64 + (tid & 7) * 8) * 2), 0, 16);
  }
  static_for<0, 14>(step);
  asm volatile("s_nop 7\n\ts_nop 7" ::: "memory");
  TR(ws, 12);
  bar();
  const int tid2 = fresh_tid(), lane2 = tid2 & 63;
#pragma unroll
  for (int dl = 0; dl < 4; ++dl)
#pragma unroll
    for (int ta = 0; ta < 4; ++ta)
      *(f32x4*)(smem + (w * 64 + 16 * ta + (lane2 & 15)) * G2_RS + (16 * dl + 4 * (lane2 >> 4)) * 4) = acc[dl][ta];
  lds_bar();
#pragma unroll
  for (int i = 0; i < 2; ++i) {
    const int t = (tid2 >> 3) + 32 * i, chn = tid2 & 7;
    const u32x4 rr = res[i];
    f32x4 s0 = {0.f, 0.f, 0.f, 0.f}, s1 = s0;
#pragma unroll
    for (int ww = 0; ww < 4; ++ww) {
      s0 += *(const f32x4*)(smem + (ww * 64 + t) * G2_RS + chn * 32);
      s1 += *(const f32x4*)(smem + (ww * 64 + t) * G2_RS + chn * 32 + 16);
    }
    u32 p0 = pk_bf16(s0[0], s0[1]), p1 = pk_bf16(s0[2], s0[3]), p2 = pk_bf16(s1[0], s1[1]), p3 = pk_bf16(s1[2], s1[3]);
    u32x4 o;
    o[0] = pk_bf16(bflo(rr[0]) + bflo(p0), bfhi(rr[0]) + bfhi(p0));
    o[1] = pk_bf16(bflo(rr[1]) + bflo(p1), bfhi(rr[1]) + bfhi(p1));
    o[2] = pk_bf16(bflo(rr[2]) + bflo(p2), bfhi(rr[2]) + bfhi(p2));
    o[3] = pk_bf16(bflo(rr[3]) + bflo(p3), bfhi(rr[3]) + bfhi(p3));
    *(u32x4*)(a.out + (size_t)(t64 * 64 + t) * D + db * 64 + chn * 8) = o;
  }
}

__global__ __launch_bounds__(512, 2) void s4_block_kernel(Args a) {
  extern __shared__ __attribute__((aligned(16))) char smem[];
  const int b = blockIdx.x;
  unsigned long long pc0;
  asm volatile("s_getpc_b64 %0" : "=s"(pc0));
  TR(a.ws, 0);
  if (b < NAUX) { aux_block(a, b, smem, (const char*)(pc0 & ~(unsigned long long)255)); TR(a.ws, 15); TRDUMP(a.ws); return; }
  const int m = b - NAUX;
  const u64 tag = phase_a(a, m, smem);
  phase_b(a, m, smem, tag);
  __syncthreads();
  phase_c(a, m, smem, tag);
  TR(a.ws, 15);
  TRDUMP(a.ws);
}

extern "C" void kernel_launch(void* const* d_in, const int* in_sizes, int n_in,
                              void* d_out, int out_size, void* d_ws, size_t ws_size,
                              hipStream_t stream) {
  Args a;
  a.x = (const u16*)d_in[0]; a.Are = (const u16*)d_in[1]; a.Aim = (const u16*)d_in[2];
  a.Bre = (const u16*)d_in[3]; a.Bim = (const u16*)d_in[4]; a.Cre = (const u16*)d_in[5];
  a.Cim = (const u16*)d_in[6]; a.logdt = (const u16*)d_in[7]; a.Dskip = (const u16*)d_in[8];
  a.w1 = (const u16*)d_in[10]; a.w2 = (const u16*)d_in[11]; a.ws = (char*)d_ws; a.out = (u16*)d_out; a.pad = nullptr;
  (void)hipFuncSetAttribute((const void*)s4_block_kernel, hipFuncAttributeMaxDynamicSharedMemorySize, SMEM_BYTES);
  s4_block_kernel<<<NAUX + NMAIN, 512, SMEM_BYTES, stream>>>(a);
}
```

```cpp
#define TLBT 0
#define PF_CODE 1
#define PF_W 1
#define XSC1 true
#include <hip/hip_runtime.h>
#include <hip/hip_bf16.h>
#include <stdint.h>
#include <type_traits>

static constexpr int L = 2048, D = 448, NM = 256, F = 1792;
static constexpr int LP = 2176;
static constexpr size_t OFF_ST = 0;
static constexpr size_t OFF_XT = 16384;
static constexpr size_t OFF_HT = OFF_XT + (size_t)D * LP * 2;
static constexpr size_t OFF_W1S = OFF_HT + (size_t)D * LP * 2;
static constexpr size_t OFF_HN = OFF_W1S + 8192;
static constexpr size_t OFF_H1F = OFF_HN + (size_t)L * D * 2;
static constexpr size_t OFF_SYNC = OFF_H1F + (size_t)L * F * 2;
static constexpr size_t SY_EP = 0, SY_LN = 1024  , SY_W1S = 4096, SY_CV = 8192  , SY_H1 = 32768  ;
static constexpr size_t WS_USED = OFF_SYNC + 65536;

typedef unsigned short u16;
typedef unsigned int u32;
typedef unsigned long long u64;
typedef _Float16 f16;
typedef f16 f16x2 __attribute__((ext_vector_type(2)));
typedef f16 f16x8 __attribute__((ext_vector_type(8)));
typedef __bf16 bf16x2 __attribute__((ext_vector_type(2)));
typedef __bf16 bf16x8 __attribute__((ext_vector_type(8)));
typedef float f32x2 __attribute__((ext_vector_type(2)));
typedef float f32x4 __attribute__((ext_vector_type(4)));
typedef u32 u32x2 __attribute__((ext_vector_type(2)));
typedef u32 u32x4 __attribute__((ext_vector_type(4)));
typedef short s16x4 __attribute__((ext_vector_type(4)));
#define LDS3 __attribute__((address_space(3)))
typedef __attribute__((address_space(1))) u64 gu64;
typedef __amdgpu_buffer_rsrc_t rsrc_t;

#define DI __device__ __forceinline__
DI float bf2f(u16 v) { return __uint_as_float(((u32)v) << 16); }
DI float bflo(u32 v) { return __uint_as_float(v << 16); }
DI float bfhi(u32 v) { return __uint_as_float(v & 0xffff0000u); }
DI u32 pk_bf16(float a, float b) { f32x2 v = {a, b}; return __builtin_bit_cast(u32, __builtin_convertvector(v, bf16x2)); }
DI u32 pk_f16(float a, float b) { f32x2 v = {a, b}; return __builtin_bit_cast(u32, __builtin_convertvector(v, f16x2)); }
DI float rbf(float x) { return bflo(pk_bf16(x, 0.f)); }
DI f16x2 as_f16x2(u32 v) { return __builtin_bit_cast(f16x2, v); }
DI bf16x2 as_bf16x2(u32 v) { return __builtin_bit_cast(bf16x2, v); }
template <int CTRL, int RM> DI float dppf(float old, float v) {
  return __int_as_float(__builtin_amdgcn_update_dpp(__float_as_int(old), __float_as_int(v), CTRL, RM, 0xf, false));
}
DI float wave_sum(float v) {
  v += dppf<0x111, 0xf>(0.f, v); v += dppf<0x112, 0xf>(0.f, v); v += dppf<0x114, 0xf>(0.f, v); v += dppf<0x118, 0xf>(0.f, v);
  v += dppf<0x142, 0xa>(0.f, v); v += dppf<0x143, 0xc>(0.f, v);
  return __int_as_float(__builtin_amdgcn_readlane(__float_as_int(v), 63));
}
DI float wave_max(float v) {
  v = fmaxf(v, dppf<0x111, 0xf>(v, v)); v = fmaxf(v, dppf<0x112, 0xf>(v, v)); v = fmaxf(v, dppf<0x114, 0xf>(v, v)); v = fmaxf(v, dppf<0x118, 0xf>(v, v));
  v = fmaxf(v, dppf<0x142, 0xa>(v, v)); v = fmaxf(v, dppf<0x143, 0xc>(v, v));
  return __int_as_float(__builtin_amdgcn_readlane(__float_as_int(v), 63));
}
DI u64 ld_agent(const void* p) { return __hip_atomic_load((const gu64*)p, __ATOMIC_RELAXED, __HIP_MEMORY_SCOPE_AGENT); }
DI void st_agent(void* p, u64 v) { __hip_atomic_store((gu64*)p, v, __ATOMIC_RELAXED, __HIP_MEMORY_SCOPE_AGENT); }
static constexpr u64 TAG_INC = 0x9E3779B97F4A7C15ull;
DI rsrc_t ws_rsrc(char* ws) { return __builtin_amdgcn_make_buffer_rsrc(ws, 0, (int)WS_USED, 0x00020000); }
template <int B, int E, class Fn> DI void static_for(Fn&& f) {
  if constexpr (B < E) { f(std::integral_constant<int, B>{}); static_for<B + 1, E>(f); }
}
template <int N> using IC = std::integral_constant<int, N>;
DI s16x4 tr_read(const void* p) { return __builtin_amdgcn_ds_read_tr16_b64_v4i16((LDS3 s16x4*)p); }
template <int N> DI void wait_vm() { asm volatile("s_waitcnt vmcnt(%0)" :: "n"(N) : "memory"); }
DI void bar() { __builtin_amdgcn_s_barrier(); }
DI int fresh_tid() { int t = threadIdx.x; asm volatile("" : "+v"(t)); return t; }
DI bool vm_idle_unused() {
  u32 v; asm volatile("s_getreg_b32 %0, hwreg(HW_REG_IB_STS)" : "=s"(v));
  return ((v & 0xfu) | ((v >> 22) & 0x3u)) == 0u;
}
DI void lds_bar() { asm volatile("s_waitcnt lgkmcnt(0)" ::: "memory"); __builtin_amdgcn_s_barrier(); }
template <bool SC1> DI void glds16(u32 lds_dst, u32 voff, const void* sbase) {
  u32 keep;
  if constexpr (SC1)
    asm volatile("s_mov_b32 %0, m0\n\ts_mov_b32 m0, %1\n\ts_nop 0\n\tglobal_load_lds_dwordx4 %2, %3 sc1\n\ts_mov_b32 m0, %0"
                 : "=&s"(keep) : "s"(lds_dst), "v"(voff), "s"(sbase) : "memory");
  else
    asm volatile("s_mov_b32 %0, m0\n\ts_mov_b32 m0, %1\n\ts_nop 0\n\tglobal_load_lds_dwordx4 %2, %3\n\ts_mov_b32 m0, %0"
                 : "=&s"(keep) : "s"(lds_dst), "v"(voff), "s"(sbase) : "memory");
}
template <int OFF> DI void trread(u32x2& dst, u32 addr) {
  asm volatile("ds_read_b64_tr_b16 %0, %1 offset:%2" : "=v"(dst) : "v"(addr), "n"(OFF) : "memory");
}
template <int OFF> DI void ldsread128(u32x4& dst, u32 addr) {
  asm volatile("ds_read_b128 %0, %1 offset:%2" : "=v"(dst) : "v"(addr), "n"(OFF) : "memory");
}
template <bool FIRST, bool NOP> DI void mfma_bf16(f32x4& acc, const u32x4& a, const u32x4& b) {
  if constexpr (NOP) {
    if constexpr (FIRST) asm volatile("s_nop 1\n\tv_mfma_f32_16x16x32_bf16 %0, %1, %2, 0" : "=&v"(acc) : "v"(a), "v"(b));
    else asm volatile("s_nop 1\n\tv_mfma_f32_16x16x32_bf16 %0, %1, %2, %0" : "+v"(acc) : "v"(a), "v"(b));
  } else {
    if constexpr (FIRST) asm volatile("v_mfma_f32_16x16x32_bf16 %0, %1, %2, 0" : "=&v"(acc) : "v"(a), "v"(b));
    else asm volatile("v_mfma_f32_16x16x32_bf16 %0, %1, %2, %0" : "+v"(acc) : "v"(a), "v"(b));
  }
}
DI u32x4 cat(const u32x2& a, const u32x2& b) { return u32x4{a[0], a[1], b[0], b[1]}; }
DI u32 swz(int row) { return (u32)(((row & 3) << 2) | ((row >> 2) & 3)); }
DI f32x2 cmul(f32x2 y, f32x2 e) {
  f32x2 t, r;
  asm("v_pk_mul_f32 %0, %1, %2 op_sel_hi:[1,0]" : "=v"(t) : "v"(y), "v"(e));
  asm("v_pk_fma_f32 %0, %1, %2, %3 op_sel:[1,1,0] op_sel_hi:[0,1,1] neg_lo:[0,1,0]" : "=v"(r) : "v"(y), "v"(e), "v"(t));
  return r;
}
DI f32x2 cmul_conj(f32x2 p, f32x2 q) {
  f32x2 t, r;
  asm("v_pk_mul_f32 %0, %1, %2 op_sel_hi:[1,0] neg_hi:[1,0]" : "=v"(t) : "v"(p), "v"(q));
  asm("v_pk_fma_f32 %0, %1, %2, %3 op_sel:[1,1,0] op_sel_hi:[0,1,1] neg_lo:[1,0,0] neg_hi:[1,0,0]" : "=v"(r) : "v"(p), "v"(q), "v"(t));
  return r;
}

#define TR(ws, slot) do {} while (0)
#define TRW(ws, slot, cond) do {} while (0)
#define TRDUMP(ws) do {} while (0)

struct Args {
  const u16 *x, *Are, *Aim, *Bre, *Bim, *Cre, *Cim, *logdt, *Dskip, *w1, *w2;
  char* ws;
  u16* out;
  const void* pad;
};

constexpr int rne_bf16_int(int l) {
  if (l < 256) return l;
  int sh = 0; while ((l >> sh) >= 256) ++sh;
  const int q = l >> sh, rem = l & ((1 << sh) - 1), half = 1 << (sh - 1);
  const int up = (rem > half) || (rem == half && (q & 1));
  return (q + up) << sh;
}
constexpr int kslot(int m) {
  const int l = 2047 - m;
  if (l < 0) return 1089;
  const int t = rne_bf16_int(l);
  if (t >= 2048) return 1088;
  const int seg = (t >= 256) + (t >= 512) + (t >= 1024);
  const int row = seg == 0 ? (t & 7) : seg == 1 ? 8 + ((t >> 1) & 3) : seg == 2 ? 12 + ((t >> 2) & 1) : 14 + ((t >> 9) & 1);
  const int b = (t >> 3) & (seg >= 2 ? 63 : 31);
  return b * 17 + row;
}
struct SlotTab { u16 v[2064]; };
constexpr SlotTab make_slot_tab() { SlotTab t{}; for (int m = 0; m < 2064; ++m) t.v[m] = (u16)(kslot(m) * 4); return t; }
__device__ __attribute__((aligned(16))) const SlotTab g_slot_tab = make_slot_tab();

static constexpr int NAUX = 32;
static constexpr int NMAIN = 224;
static constexpr int SMEM_BYTES = 160 * 1024;
static constexpr unsigned SPIN_CAP = 1u << 20;
static constexpr int LDS_SIG = SMEM_BYTES - 320;

template <int NPL> DI void wait_flags(const char* base, int n, u64 tag) {
  const int lane = threadIdx.x & 63;
  unsigned spins = 0;
  for (;;) {
    bool ok = true;
#pragma unroll
    for (int k = 0; k < NPL; ++k) { const int i = k * 64 + lane; if (i < n) ok &= (ld_agent(base + i * 8) == tag); }
    if (__all(ok)) break;
    __builtin_amdgcn_s_sleep(1);
    if (++spins > SPIN_CAP) break;
  }
}

template <int NPL> DI void loaders_wait(char* smem, int idx, int w, const char* base, int n, u64 tag) {
  volatile u32* sig = (volatile u32*)(smem + LDS_SIG) + idx;
  if (w == 0) {
    wait_flags<NPL>(base, n, tag);
    *sig = 1u;
  } else {
    unsigned spins = 0;
    while (*sig == 0u) { __builtin_amdgcn_s_sleep(1); if (++spins > (SPIN_CAP << 4)) break; }
  }
  asm volatile("" ::: "memory");
}

DI u32 tlb_touch_async(const Args& a) {
  const int lane = threadIdx.x & 63;
  const char* p;
  if (lane < 20) p = a.ws + (size_t)lane * (1u << 20);
  else if (lane < 22) p = (const char*)a.out + (size_t)(lane - 20) * (1u << 20);
  else if (lane < 24) p = (const char*)a.w1 + (size_t)(lane - 22) * (1u << 20);
  else if (lane < 26) p = (const char*)a.w2 + (size_t)(lane - 24) * (1u << 20);
  else p = (const char*)a.x;
  u32 v;
  asm volatile("global_load_dword %0, %1, off" : "=v"(v) : "v"(p) : "memory");
  return v;
}
DI void l2_touch(const char* base, int kbytes, int part, int nparts, char* smem) {
  const int lane = threadIdx.x & 63;
  const int w = __builtin_amdgcn_readfirstlane(threadIdx.x >> 6);
  const u32 dst = (u32)(size_t)(LDS3 char*)smem + 65536 + w * 1024;
  const int per = (kbytes + nparts - 1) / nparts;
  const int k0 = per * part, k1 = (k0 + per < kbytes) ? k0 + per : kbytes;
  int cnt = 0;
  for (int k = k0 + w; k < k1; k += 8) {
    glds16<false>(dst, lane * 16, base + (size_t)k * 1024);
    if ((++cnt & 7) == 0) wait_vm<32>();
  }
  wait_vm<0>();
}
static constexpr int XT_RS = 144;
static constexpr int LDS_X8 = 124 * 1024;
struct Pro { u32x4 row, tile; };
DI Pro pro_load(const Args& a, int b) {
  const int lane = threadIdx.x & 63, wv = threadIdx.x >> 6;
  Pro p; p.row = u32x4{0, 0, 0, 0}; p.tile = p.row;
  if (lane < 56) {
    p.tile = *(const u32x4*)(a.x + (size_t)(64 * (b >> 3) + 8 * wv + lane / 7) * D + 56 * (b & 7) + 8 * (lane % 7));
    p.row = *(const u32x4*)(a.x + (size_t)(8 * b + wv) * D + lane * 8);
  }
  return p;
}
DI u64 pro_store(const Args& a, int b, const Pro& p, u64 ep, char* smem) {
  const int lane = threadIdx.x & 63;
  const int wv = __builtin_amdgcn_readfirstlane(threadIdx.x >> 6);
  if (lane < 56) *(u32x4*)(smem + LDS_X8 + (8 * wv + lane / 7) * XT_RS + (lane % 7) * 16) = p.tile;
  if (threadIdx.x == 0) { *(volatile u32*)(smem + LDS_SIG) = 0u; *(volatile u32*)(smem + LDS_SIG + 4) = 0u; *(volatile u64*)(smem + LDS_SIG + 8) = ep + TAG_INC; *(volatile u32*)(smem + LDS_SIG + 48) = 0u; }
  __syncthreads();
  const u64 tag = *(volatile u64*)(smem + LDS_SIG + 8);
  if (wv < 7) {
    const int g = lane >> 4, i16 = lane & 15, q = i16 >> 2, pp = lane & 3;
    const char* src = smem + LDS_X8 + (16 * g + 8 * (pp >> 1) + q) * XT_RS + (4 * (pp & 1)) * 2 + wv * 16;
    s16x4 lo = tr_read(src), hi = tr_read(src + 4 * XT_RS);
    __builtin_amdgcn_raw_buffer_store_b128(cat(__builtin_bit_cast(u32x2, lo), __builtin_bit_cast(u32x2, hi)), ws_rsrc(a.ws),
        (u32)(OFF_XT + (size_t)(56 * (b & 7) + 8 * wv + (i16 & 7)) * LP * 2 + (64 * (b >> 3) + 16 * g + 8 * (i16 >> 3)) * 2), 0, 16);
  }
  return tag;
}
DI void pro_stats(const Args& a, int b, const Pro& p, u64 tag) {
  const int lane = threadIdx.x & 63, wv = threadIdx.x >> 6;
  float s = 0.f, q2 = 0.f;
#pragma unroll
  for (int k = 0; k < 4; ++k) {
    const float lo = bflo(p.row[k]), hi = bfhi(p.row[k]);
    s += lo; s += hi; q2 = __builtin_fmaf(lo, lo, q2); q2 = __builtin_fmaf(hi, hi, q2);
  }
  const float mean = wave_sum(s) * (1.0f / D);
  const float var = fmaxf(wave_sum(q2) * (1.0f / D) - mean * mean, 0.f);
  const float sd = rbf(__builtin_amdgcn_sqrtf(rbf(rbf(var) + 1e-5f)));
  if (lane == 0) st_agent(a.ws + OFF_ST + (8 * b + wv) * 8, ((u64)(u32)tag << 32) | pk_bf16(mean, sd));
}
DI void pro_flag(const Args& a, int b, u64 tag) {
  if (threadIdx.x == 0) st_agent(a.ws + OFF_SYNC + SY_LN + (b & 7) * 256 + (b >> 3) * 8, tag);
}
DI void aux_block(const Args& a, int blk, char* smem, const char* code0, u32 code_len) {
  const int tid = threadIdx.x, lane = tid & 63;
  const int w = __builtin_amdgcn_readfirstlane(tid >> 6);
  const rsrc_t rs = ws_rsrc(a.ws);
  {
    const u32 dst = (u32)(size_t)(LDS3 char*)smem + 98304 + w * 1024;
    const int nk = ((int)code_len > 0 && code_len < (1u << 20)) ? (int)(code_len >> 10) : 0;
    for (int k = (blk >> 3) * 8 + w; k < nk; k += 32) glds16<false>(dst, lane * 16, code0 + (size_t)k * 1024);
  }
  const Pro pr = pro_load(a, blk);
  const u64 ep = ld_agent(w == 0 ? (const char*)(a.ws + OFF_SYNC + SY_EP) : (const char*)(a.x + (size_t)(8 * blk + w) * D));
  const u64 tag = pro_store(a, blk, pr, ep, smem);
  pro_stats(a, blk, pr, tag);
  asm volatile("s_waitcnt vmcnt(0)" ::: "memory");
  __syncthreads();
  pro_flag(a, blk, tag);
  TR(a.ws, 2);
  {
    float* red = (float*)smem;
    const int cp = tid % 28, rg = tid / 28;
    float s0 = 0.f, s1 = 0.f;
    if (rg < 16) {
      u32 x[28];
#pragma unroll
      for (int i = 0; i < 28; ++i) x[i] = *(const u32*)(a.w1 + (size_t)(rg + 16 * i) * F + 56 * blk + 2 * cp);
#pragma unroll
      for (int i = 0; i < 28; ++i) { s0 += bflo(x[i]); s1 += bfhi(x[i]); }
      red[rg * 56 + 2 * cp] = s0; red[rg * 56 + 2 * cp + 1] = s1;
    }
    __syncthreads();
    if (tid < 56) {
      float s = 0.f;
#pragma unroll
      for (int i = 0; i < 16; ++i) s += red[i * 56 + tid];
      __builtin_amdgcn_raw_buffer_store_b32(__float_as_uint(s), rs, (u32)(OFF_W1S + (56 * blk + tid) * 4), 0, 16);
    }
    asm volatile("s_waitcnt vmcnt(0)" ::: "memory");
    __syncthreads();
    if (tid == 0) st_agent(a.ws + OFF_SYNC + SY_W1S + blk * 8, tag);
  }
  l2_touch((const char*)a.w1, D * F * 2 / 1024, blk >> 3, 4, smem);
  l2_touch((const char*)a.w2, D * F * 2 / 1024, blk >> 3, 4, smem);
  if (blk == 0 && w == 0) {
    wait_flags<1>(a.ws + OFF_SYNC + SY_W1S, NAUX, tag);
    wait_flags<4>(a.ws + OFF_SYNC + SY_CV, NMAIN, tag);
    if (lane == 0) st_agent(a.ws + OFF_SYNC + SY_EP, tag);
  }
}

static constexpr int CPS = 4160;
static constexpr int CH_BYTES = 61440;
static constexpr int CH_TAB = 0;
static constexpr int CH_SU = 8 * CPS;
static constexpr int CH_F = CH_SU + 4608 + 256;
static constexpr int LDS_SX = 136 * 1024;
static constexpr int CH_RED = 59392;

DI u64 phase_a(const Args& a, int m, char* smem_all) {
  const int tid = threadIdx.x & 255, lane = threadIdx.x & 63;
  const int wv = __builtin_amdgcn_readfirstlane(threadIdx.x >> 6);
  const int w = wv & 3, hw = wv >> 2;
  const int d = 2 * m + hw;
  char* smem = smem_all + hw * CH_BYTES;
  const float LOG2E = 1.4426950408889634f, INV2PI = 0.15915494309189535f;
  const u32 lds0 = (u32)(size_t)(LDS3 char*)smem;
  const Pro pr = pro_load(a, m + NAUX);
  const u64 ep = ld_agent(wv == 0 ? (const char*)(a.ws + OFF_SYNC + SY_EP) : (const char*)(a.Are + d * NM + 4 * w));
  const int n = tid;
  const u16 r_are = a.Are[d * NM + n], r_aim = a.Aim[d * NM + n], r_bre = a.Bre[d * NM + n], r_bim = a.Bim[d * NM + n];
  const u16 r_cre = a.Cre[d * NM + n], r_cim = a.Cim[d * NM + n], r_ldt = a.logdt[d], r_dsk = a.Dskip[d];
  u32 slot2[8];
  {
    const u32x4 s0 = *(const u32x4*)(g_slot_tab.v + 8 * tid), s1 = *(const u32x4*)(g_slot_tab.v + 8 * tid + 8);
#pragma unroll
    for (int k = 0; k < 4; ++k) { slot2[k] = s0[k]; slot2[4 + k] = s1[k]; }
  }
  __builtin_amdgcn_sched_barrier(0);
  const u64 tag = pro_store(a, m + NAUX, pr, ep, smem_all);
  float* red = (float*)(smem + CH_RED);
  const float dt = rbf(expf(bf2f(r_ldt)));
  const float bre = dt * bf2f(r_bre), bim = dt * bf2f(r_bim);
  const float cre = bf2f(r_cre), cim = bf2f(r_cim);
  const float zr = (dt * bf2f(r_are)) * LOG2E, zi = (dt * bf2f(r_aim)) * INV2PI;
  const float cr = cre * bre - cim * bim, ci = cre * bim + cim * bre;
  const float mx = fmaxf(fabsf(cr), fabsf(ci));
  const float lc = mx > 1e-36f ? __builtin_amdgcn_logf(mx) + 0.5f : -1000.f;
  const float sY = fmaxf(0.f, zr * 504.f);
  const float gm = fmaxf(lc + fmaxf(0.f, zr * 1536.f) + sY, lc + zr * 2048.f);
  const float G = ceilf(wave_max(gm));
  auto cexp = [&](float t, float lg) -> f32x2 {
    const float mm = __builtin_amdgcn_exp2f(__builtin_fmaf(zr, t, lg)), f = __builtin_amdgcn_fractf(zi * t);
    return f32x2{mm * __builtin_amdgcn_cosf(f), mm * __builtin_amdgcn_sinf(f)};
  };
  {
    f32x2* tb = (f32x2*)(smem + CH_TAB + n * 224);
    const f32x2 e1 = cexp(1.f, 0.f), e2 = cmul(e1, e1), e3 = cmul(e2, e1), e4 = cexp(4.f, 0.f), e6 = cmul(e3, e3);
    const f32x2 e8 = cexp(8.f, 0.f), e16 = cmul(e8, e8), e24 = cmul(e16, e8);
    const f32x2 e32 = cexp(32.f, 0.f), e64 = cmul(e32, e32), e96 = cmul(e64, e32);
    const f32x2 e128 = cexp(128.f, 0.f), e256 = cexp(256.f, 0.f), e384 = cmul(e256, e128);
    const f32x2 e512 = cexp(512.f, 0.f), e1024 = cexp(1024.f, 0.f);
    const float sy2 = __builtin_amdgcn_exp2f(-sY);
    const float cs = __builtin_amdgcn_exp2f(sY - G + 14.f);
    const f32x2 c0 = {cr * cs, ci * cs};
    *(f32x4*)(tb + 0) = f32x4{1.f, 0.f, e8[0], e8[1]};      *(f32x4*)(tb + 2) = f32x4{e16[0], e16[1], e24[0], e24[1]};
    *(f32x4*)(tb + 4) = f32x4{sy2, 0.f, e32[0] * sy2, e32[1] * sy2}; *(f32x4*)(tb + 6) = f32x4{e64[0] * sy2, e64[1] * sy2, e96[0] * sy2, e96[1] * sy2};
    const f32x2 p1 = cmul(c0, e4), p2 = cmul(c0, e256), p3 = cmul(c0, e512);
    *(f32x4*)(tb + 8) = f32x4{c0[0], c0[1], p1[0], p1[1]};  *(f32x4*)(tb + 10) = f32x4{p2[0], p2[1], p3[0], p3[1]};
    *(f32x4*)(tb + 12) = f32x4{1.f, 0.f, e1[0], e1[1]};     *(f32x4*)(tb + 14) = f32x4{e2[0], e2[1], e3[0], e3[1]};
    *(f32x4*)(tb + 16) = f32x4{1.f, 0.f, e2[0], e2[1]};     *(f32x4*)(tb + 18) = f32x4{e4[0], e4[1], e6[0], e6[1]};
    *(f32x4*)(tb + 20) = f32x4{1.f, 0.f, e4[0], e4[1]};     *(f32x4*)(tb + 22) = f32x4{e512[0], e512[1], e1024[0], e1024[1]};
    *(f32x4*)(tb + 24) = f32x4{e128[0], e128[1], e256[0], e256[1]}; *(f32x4*)(tb + 26) = f32x4{e384[0], e384[1], 0.f, 0.f};
    const f32x2 e2048 = cexp(2048.f, 14.f - G);
    float t2048 = cr * e2048[0] - ci * e2048[1];
    t2048 = wave_sum(t2048);
    if (lane == 0) { red[w] = G; red[4 + w] = t2048; }
  }
  asm volatile("s_waitcnt vmcnt(0) lgkmcnt(0)" ::: "memory");
  __syncthreads();
  pro_flag(a, m + NAUX, tag);
  TR(a.ws, 1);
  pro_stats(a, m + NAUX, pr, tag);
  const int c = lane & 15, g = lane >> 4;
  const rsrc_t rs = ws_rsrc(a.ws);
  u32x4 gr[4];
  const char* flg = a.ws + OFF_SYNC + SY_LN + ((2 * m) / 56) * 256 + (lane & 31) * 8;
  u64 fl = 0;
  bool have = (wv != 0);
  auto load_x = [&]() {
    TR(a.ws, 13);
#pragma unroll
    for (int i = 0; i < 4; ++i) {
      glds16<XSC1>(lds0 + LDS_SX + i * 1024, lane * 16 + i * 1024, a.ws + OFF_XT + (size_t)(2 * m) * LP * 2);
      glds16<XSC1>(lds0 + LDS_SX + 4096 + i * 1024, lane * 16 + i * 1024, a.ws + OFF_XT + (size_t)(2 * m + 1) * LP * 2);
    }
  };
  auto poll = [&]() { if (wv == 0 && !have) fl = ld_agent(flg); };
  auto check = [&]() {
    if (wv == 0 && !have) {
      if (__all(fl == tag)) { have = true; load_x(); }
    }
  };
  auto load_st = [&]() {
#pragma unroll
    for (int k = 0; k < 4; ++k) gr[k] = __builtin_amdgcn_raw_buffer_load_b128(rs, (u32)(OFF_ST + tid * 64 + k * 16), 0, 16);
  };
  auto load_st_fast = [&]() {
#pragma unroll
    for (int k = 0; k < 4; ++k) gr[k] = __builtin_amdgcn_raw_buffer_load_b128(rs, (u32)(OFF_ST + tid * 64 + k * 16), 0, 0);
  };
  const int r = c;
  f32x4 acc[4];
#pragma unroll
  for (int k = 0; k < 4; ++k) acc[k] = f32x4{0.f, 0.f, 0.f, 0.f};
  {
    const char* tb = smem + CH_TAB + (64 * w + 4 * g) * 224;
    const char* pA = tb + (c & 3) * 8;
    const char* pB = tb + 32 + (c >> 2) * 8;
    const char* pP = tb + 64 + (r >> 2) * 8;
    const char* pQ = tb + 96 + ((r < 8 ? 0 : r < 12 ? 4 : 8) + (r & 3)) * 8;
    const char* pE = tb + 192;
    f32x2 tA[2][4], tB[2][4], tP[2][4], tQ[2][4], tE3[2][4];
    f32x4 tE[2][4];
    auto ldtab = [&](auto sc_) {
      constexpr int s = decltype(sc_)::value;
#pragma unroll
      for (int i = 0; i < 4; ++i) {
        const int o = (16 * s + i) * 224;
        tA[s & 1][i] = *(const f32x2*)(pA + o); tB[s & 1][i] = *(const f32x2*)(pB + o);
        tP[s & 1][i] = *(const f32x2*)(pP + o); tQ[s & 1][i] = *(const f32x2*)(pQ + o);
        tE[s & 1][i] = *(const f32x4*)(pE + o); tE3[s & 1][i] = *(const f32x2*)(pE + o + 16);
      }
    };
    auto kstep = [&](auto sc_) {
      constexpr int s = decltype(sc_)::value;
      if constexpr (s < 3) ldtab(IC<s + 1>{});
      __builtin_amdgcn_sched_barrier(0);
      u32x4 xf, y0, y1, y2, y3;
#pragma unroll
      for (int i = 0; i < 4; ++i) {
        const f32x2 yv = cmul(tA[s & 1][i], tB[s & 1][i]);
        const f32x2 xv = cmul_conj(tP[s & 1][i], tQ[s & 1][i]);
        y0[i] = pk_f16(yv[0], yv[1]);
        xf[i] = pk_f16(xv[0], xv[1]);
        const f32x4 E12 = tE[s & 1][i];
        const f32x2 v1 = cmul(yv, f32x2{E12[0], E12[1]}), v2 = cmul(yv, f32x2{E12[2], E12[3]}), v3 = cmul(yv, tE3[s & 1][i]);
        y1[i] = pk_f16(v1[0], v1[1]); y2[i] = pk_f16(v2[0], v2[1]); y3[i] = pk_f16(v3[0], v3[1]);
      }
      f16x8 xa = __builtin_bit_cast(f16x8, xf);
      __builtin_amdgcn_sched_barrier(0);
      acc[0] = __builtin_amdgcn_mfma_f32_16x16x32_f16(xa, __builtin_bit_cast(f16x8, y0), acc[0], 0, 0, 0);
      acc[1] = __builtin_amdgcn_mfma_f32_16x16x32_f16(xa, __builtin_bit_cast(f16x8, y1), acc[1], 0, 0, 0);
      acc[2] = __builtin_amdgcn_mfma_f32_16x16x32_f16(xa, __builtin_bit_cast(f16x8, y2), acc[2], 0, 0, 0);
      acc[3] = __builtin_amdgcn_mfma_f32_16x16x32_f16(xa, __builtin_bit_cast(f16x8, y3), acc[3], 0, 0, 0);
      __builtin_amdgcn_sched_barrier(0);
      if constexpr (s == 1) poll();
      __builtin_amdgcn_sched_barrier(0);
    };
    ldtab(IC<0>{});
    static_for<0, 4>(kstep);
  }
  check();
  poll();
  TR(a.ws, 2);
  float* part = (float*)(smem + CH_TAB);
#pragma unroll
  for (int k = 0; k < 4; ++k) *(f32x4*)(part + w * 3584 + (16 * k + r) * 16 + 4 * g) = acc[k];
  __syncthreads();
  load_st_fast();
  float* Fv = (float*)(smem + CH_F);
  const float Gm = fmaxf(fmaxf(red[0], red[1]), fmaxf(red[2], red[3]));
  float sc[4];
#pragma unroll
  for (int k = 0; k < 4; ++k) sc[k] = __builtin_amdgcn_exp2f(red[k] - Gm - 8.f);
  {
    const f32x4* pp = (const f32x4*)part;
    f32x4 sv = pp[tid] * sc[0] + pp[896 + tid] * sc[1] + pp[1792 + tid] * sc[2] + pp[2688 + tid] * sc[3];
    float* fo = Fv + (tid >> 2) * 17 + 4 * (tid & 3);
    fo[0] = sv[0]; fo[1] = sv[1]; fo[2] = sv[2]; fo[3] = sv[3];
    if (tid == 0) { Fv[1088] = red[4] * sc[0] + red[5] * sc[1] + red[6] * sc[2] + red[7] * sc[3]; Fv[1089] = 0.f; }
  }
  const float kinv = __builtin_amdgcn_exp2f(Gm - 6.f);
  check();
  __syncthreads();
  TR(a.ws, 3);
  {
    u32 e[8];
#pragma unroll
    for (int k = 0; k < 8; ++k)
      e[k] = pk_f16(*(const float*)((const char*)Fv + (slot2[k] & 0xffffu)), *(const float*)((const char*)Fv + (slot2[k] >> 16)));
    auto put = [&](int ch, const u32 (&ee)[8]) {
      u32 o[7];
#pragma unroll
      for (int i = 0; i < 7; ++i) o[i] = __builtin_amdgcn_alignbyte(ee[i + 1], ee[i], 2);
#pragma unroll
      for (int s = 0; s < 4; ++s) {
        *(u32x4*)(smem + (2 * s) * CPS + ch * 16) = u32x4{ee[s], ee[s + 1], ee[s + 2], ee[s + 3]};
        *(u32x4*)(smem + (2 * s + 1) * CPS + ch * 16) = u32x4{o[s], o[s + 1], o[s + 2], o[s + 3]};
      }
    };
    put(tid, e);
    if (tid == 255) { const u32 z[8] = {0, 0, 0, 0, 0, 0, 0, 0}; put(256, z); }
  }
  u16* su = (u16*)(smem + CH_SU);
  const u16* sx = (const u16*)(smem_all + LDS_SX + hw * 4096);
  if (wv == 0) {
    if (!have) {
      unsigned spins = 0;
      while (!__all(fl == tag)) { __builtin_amdgcn_s_sleep(1); fl = ld_agent(flg); if (++spins > SPIN_CAP) break; }
      load_x();
    }
    asm volatile("s_waitcnt vmcnt(0)" ::: "memory");
    TR(a.ws, 14);
  }
  {
    const u32 tg = (u32)tag;
    unsigned spins = 0;
    for (;;) {
      bool ok = true;
#pragma unroll
      for (int k = 0; k < 4; ++k) ok &= (gr[k][1] == tg) && (gr[k][3] == tg);
      if (__all(ok)) break;
      __builtin_amdgcn_s_sleep(1);
      load_st();
      if (++spins > SPIN_CAP) break;
    }
  }
  __syncthreads();
  TR(a.ws, 4);
  {
    const u32x4 xr = *(const u32x4*)(sx + tid * 8);
    u32x4 o;
#pragma unroll
    for (int k = 0; k < 4; ++k) {
      const u32 s0 = gr[k][0], s1 = gr[k][2];
      const u32 dpk = pk_bf16(bflo(xr[k]) - bflo(s0), bfhi(xr[k]) - bflo(s1));
      const u32 upk = pk_bf16(bflo(dpk) * __builtin_amdgcn_rcpf(bfhi(s0)), bfhi(dpk) * __builtin_amdgcn_rcpf(bfhi(s1)));
      o[k] = pk_f16(bflo(upk), bfhi(upk));
    }
    *(u32x4*)(su + 256 + tid * 8) = o;
    if (tid < 32) *(u32x4*)(su + tid * 8) = u32x4{0, 0, 0, 0};
  }
  __syncthreads();
  TR(a.ws, 5);
  f32x4 cv[8];
#pragma unroll
  for (int q = 0; q < 8; ++q) cv[q] = f32x4{0.f, 0.f, 0.f, 0.f};
  const int sig = (7 - c) & 7;
  const char* abase = smem + sig * CPS + 16 * ((c < 8 ? 253 : 252) + g);
#pragma unroll
  for (int rr2 = 0; rr2 < 2; ++rr2) {
    const int rs2 = 2 * w + rr2;
    f16x8 B[8];
#pragma unroll
    for (int q = 0; q < 8; ++q) B[q] = *(const f16x8*)(su + 240 + 256 * q - 32 * rs2 + 16 * c + 8 * g);
    f16x8 A[8];
#pragma unroll
    for (int k = 0; k < 8; ++k) A[k] = *(const f16x8*)(abase - 64 * (rs2 + 8 * k));
#pragma unroll
    for (int k = 0; k < 8; ++k)
#pragma unroll
      for (int q = k; q < 8; ++q) cv[q] = __builtin_amdgcn_mfma_f32_16x16x32_f16(A[k], B[q - k], cv[q], 0, 0, 0);
  }
  TR(a.ws, 6);
  __syncthreads();
  f32x4* rb = (f32x4*)smem;
#pragma unroll
  for (int q = 0; q < 8; ++q)
    if ((q >> 1) != w) rb[(w * 8 + q) * 64 + lane] = cv[q];
  __syncthreads();
  const float dsk = bf2f(r_dsk);
  u32x2 hh[2];
#pragma unroll
  for (int i = 0; i < 2; ++i) {
    f32x4 y = cv[0];
#pragma unroll
    for (int q = 0; q < 8; ++q) if (q == 2 * w + i) y = cv[q];
    const int q = 2 * w + i;
#pragma unroll
    for (int s = 1; s < 4; ++s) y += rb[(((w + s) & 3) * 8 + q) * 64 + lane];
    const int t = 256 * q + 16 * c + 4 * g;
    u32x2 uu = *(const u32x2*)(su + 256 + t);
    const u32x2 xx = *(const u32x2*)(sx + t);
    f16x2 ua = as_f16x2(uu[0]), ub = as_f16x2(uu[1]);
    float uf[4] = {(float)ua[0], (float)ua[1], (float)ub[0], (float)ub[1]};
    u32 y01 = pk_bf16(y[0] * kinv, y[1] * kinv), y23 = pk_bf16(y[2] * kinv, y[3] * kinv);
    u32 s01 = pk_bf16(uf[0] * dsk, uf[1] * dsk), s23 = pk_bf16(uf[2] * dsk, uf[3] * dsk);
    u32 r01 = pk_bf16(bflo(y01) + bflo(s01), bfhi(y01) + bfhi(s01));
    u32 r23 = pk_bf16(bflo(y23) + bflo(s23), bfhi(y23) + bfhi(s23));
    hh[i][0] = pk_bf16(bflo(xx[0]) + bflo(r01), bfhi(xx[0]) + bfhi(r01));
    hh[i][1] = pk_bf16(bflo(xx[1]) + bflo(r23), bfhi(xx[1]) + bfhi(r23));
  }
  {
    const auto r0 = __builtin_amdgcn_permlane16_swap(hh[0][0], hh[1][0], false, false);
    const auto r1 = __builtin_amdgcn_permlane16_swap(hh[0][1], hh[1][1], false, false);
    const int t = 256 * (2 * w + (g & 1)) + 16 * c + 8 * (g >> 1);
    __builtin_amdgcn_raw_buffer_store_b128(u32x4{r0[0], r1[0], r0[1], r1[1]}, rs, (u32)(OFF_HT + (size_t)d * LP * 2 + t * 2), 0, 16);
  }
  asm volatile("s_waitcnt vmcnt(0) lgkmcnt(0)" ::: "memory");
  __syncthreads();
  if (threadIdx.x < 8) st_agent(a.ws + OFF_SYNC + SY_CV + threadIdx.x * 2048 + m * 8, tag);
  TR(a.ws, 7);
  return tag;
}

static constexpr int G1_R = 8;
static constexpr int G1_STG = 16384;
static constexpr int G1_ST = G1_R * G1_STG;

constexpr int g1_cnt(int n) {
  if (n < 0) return 0;
  const int ks = n >> 2, c = n & 3;
  return (n + 4 < 56 ? 2 : 0) + ((c < 2 && ks + 1 < 14) ? 4 : 0);
}
constexpr int g1_wait(int n) {
  const int c = n & 3;
  const int breq = (g1_cnt(n - 4) - 2) + g1_cnt(n - 3) + g1_cnt(n - 2) + g1_cnt(n - 1);
  const int areq = g1_cnt(n - 2) + g1_cnt(n - 1);
  return (c == 0 && areq < breq) ? areq : breq;
}

DI void c_prefetch_w(const Args& a, int m, char* smem);
DI void phase_b(const Args& a, int m, char* smem, u64 tag) {
  const int tid = fresh_tid(), lane = tid & 63;
  const int wv = __builtin_amdgcn_readfirstlane(tid >> 6);
  const int w = wv & 3;
  const int xcd = m & 7, bi = m >> 3;
  const int tb = 2 * xcd + bi / 14, fb = bi % 14;
  const int c16 = lane & 15, g = lane >> 4, q = (lane & 15) >> 2, p = lane & 3;
  const u32 lds0 = (u32)(size_t)(LDS3 char*)smem;
  f32x2* st = (f32x2*)(smem + G1_ST);
  char* ws = a.ws;

  if (wv >= 4) {
    u32 vh[2], vw[2];
#pragma unroll
    for (int k = 0; k < 2; ++k) {
      const int row = 8 * w + 4 * k + g;
      vh[k] = (u32)(row * LP * 2) + (u32)((c16 ^ swz(row)) * 16);
      vw[k] = (u32)(row * F * 2) + (u32)((c16 ^ swz(row)) * 16);
    }
    const char* hb = ws + OFF_HT + tb * 256;
    const char* wb = (const char*)a.w1 + fb * 256;
    auto ldw = [&](auto kc) {
      constexpr int ks = decltype(kc)::value;
      constexpr int sl = (ks % G1_R) * G1_STG + 8192;
      glds16<false>(lds0 + sl + (8 * w) * 256, vw[0], wb + (size_t)ks * 32 * F * 2);
      glds16<false>(lds0 + sl + (8 * w + 4) * 256, vw[1], wb + (size_t)ks * 32 * F * 2);
    };
    auto ldh = [&](auto kc) {
      constexpr int ks = decltype(kc)::value;
      constexpr int sl = (ks % G1_R) * G1_STG;
      glds16<true>(lds0 + sl + (8 * w) * 256, vh[0], hb + (size_t)ks * 32 * LP * 2);
      glds16<true>(lds0 + sl + (8 * w + 4) * 256, vh[1], hb + (size_t)ks * 32 * LP * 2);
    };
    static_for<0, G1_R>(ldw);
    if (w == 0) wait_flags<1>(ws + OFF_SYNC + SY_W1S, NAUX, tag);
    loaders_wait<4>(smem, 0, w, ws + OFF_SYNC + SY_CV + xcd * 2048, NMAIN, tag);
    TRW(ws, 8, tid == 256);
    if (w == 0) glds16<true>(lds0 + G1_ST + 1024, lane * 16, ws + OFF_W1S + fb * 512);
    static_for<0, G1_R>(ldh);
    auto stepl = [&](auto kc) {
      constexpr int ks = decltype(kc)::value;
      constexpr int refills = ks > 3 ? (ks - 3 < 14 - G1_R ? ks - 3 : 14 - G1_R) : 0;
      constexpr int need_after = ks < G1_R ? (2 * (G1_R - 1 - ks) + 4 * refills) : (4 * (G1_R + refills - 1 - ks));
      wait_vm<need_after>();
      bar();
      if constexpr (ks >= 3 && ks - 3 + G1_R < 14) { ldh(IC<ks - 3 + G1_R>{}); ldw(IC<ks - 3 + G1_R>{}); }
    };
    static_for<0, 14>(stepl);
    bar();
    bar();
    __builtin_amdgcn_s_sleep(12);
    c_prefetch_w(a, m, smem);
    return;
  }
  const int fi = w >> 1, ti = w & 1;
  f32x4 acc[4][4];
  float s1[2] = {0.f, 0.f}, s2[2] = {0.f, 0.f};
  const bf16x2 ones = __builtin_bit_cast(bf16x2, 0x3f803f80u);
  u32 adB[2][4], adA[2][4];
#pragma unroll
  for (int t = 0; t < 2; ++t) {
    const int row = 8 * g + 4 * t + q;
    const u32 a0 = lds0 + (u32)(row * 256) + (((u32)(p >> 1) ^ swz(row)) << 4) + 8 * (p & 1);
#pragma unroll
    for (int c = 0; c < 4; ++c) {
      adB[t][c] = a0 ^ (u32)(32 * (4 * ti + ((2 * fi + c) & 3)));
      adA[t][c] = (a0 ^ (u32)(32 * (4 * fi + c))) + 8192;
    }
  }
  u32x2 B[8][2], A[2][4][2];
  u32x4 Af[4];
  u32 hoff[2];
#pragma unroll
  for (int c = 0; c < 2; ++c)
    hoff[c] = (u32)(OFF_HN + ((size_t)(tb * 128 + 16 * (4 * ti + ((2 * fi + c) & 3)) + c16) * D + 32 * fb + 8 * g) * 2);
  auto rdB = [&](auto nc, auto tc) {
    constexpr int n = decltype(nc)::value, t = decltype(tc)::value, sl = (n >> 2) % G1_R;
    trread<(sl & 3) * G1_STG>(B[n & 7][t], adB[t][n & 3]);
  };
  auto rdA = [&](auto kc, auto ic, auto tc) {
    constexpr int ks = decltype(kc)::value, i = decltype(ic)::value, t = decltype(tc)::value, sl = ks % G1_R;
    trread<(sl & 3) * G1_STG>(A[ks & 1][i][t], adA[t][i]);
  };
  auto group = [&](auto nc) {
    constexpr int n = decltype(nc)::value;
    constexpr int ks = n >> 2, c = n & 3, sl = n & 7;
    constexpr bool nb = n + 4 < 56, na = (c < 2) && (ks + 1 < 14);
    constexpr int wt = n < 4 ? (n == 0 ? 6 : n == 1 ? 10 : n == 2 ? 14 : 14) : g1_wait(n);
    if constexpr (c == 0) {
      asm volatile("s_waitcnt lgkmcnt(%10)" : "+v"(B[sl][0]), "+v"(B[sl][1]), "+v"(A[ks & 1][0][0]), "+v"(A[ks & 1][0][1]), "+v"(A[ks & 1][1][0]), "+v"(A[ks & 1][1][1]),
                   "+v"(A[ks & 1][2][0]), "+v"(A[ks & 1][2][1]), "+v"(A[ks & 1][3][0]), "+v"(A[ks & 1][3][1]) : "n"(wt) : "memory");
#pragma unroll
      for (int i = 0; i < 4; ++i) Af[i] = cat(A[ks & 1][i][0], A[ks & 1][i][1]);
    } else {
      asm volatile("s_waitcnt lgkmcnt(%2)" : "+v"(B[sl][0]), "+v"(B[sl][1]) : "n"(wt) : "memory");
    }
    const u32x4 bfr = cat(B[sl][0], B[sl][1]);
    mfma_bf16<ks == 0, true>(acc[0][c], Af[0], bfr);
    __builtin_amdgcn_sched_barrier(0);
    if constexpr (c == 0 && ks + 1 < 14) bar();
    if constexpr (c == 0 && ((ks + 1) & 3) == 0) {
      const u32 dl = (((ks + 1) >> 2) & 1) ? 65536u : (u32)-65536;
#pragma unroll
      for (int t = 0; t < 2; ++t)
#pragma unroll
        for (int cc = 0; cc < 4; ++cc) { adB[t][cc] += dl; adA[t][cc] += dl; }
    }
    if constexpr (nb) rdB(IC<n + 4>{}, IC<0>{});
    __builtin_amdgcn_sched_barrier(0);
    mfma_bf16<ks == 0, false>(acc[1][c], Af[1], bfr);
    __builtin_amdgcn_sched_barrier(0);
    if constexpr (nb) rdB(IC<n + 4>{}, IC<1>{});
    if constexpr (na) rdA(IC<ks + 1>{}, IC<2 * c>{}, IC<0>{});
    __builtin_amdgcn_sched_barrier(0);
    mfma_bf16<ks == 0, false>(acc[2][c], Af[2], bfr);
    __builtin_amdgcn_sched_barrier(0);
    if constexpr (na) { rdA(IC<ks + 1>{}, IC<2 * c>{}, IC<1>{}); rdA(IC<ks + 1>{}, IC<2 * c + 1>{}, IC<0>{}); }
    if constexpr (c < 2) {
#pragma unroll
      for (int k = 0; k < 2; ++k) {
        bf16x2 x2 = as_bf16x2(bfr[k]);
        s1[c] = __builtin_amdgcn_fdot2_f32_bf16(x2, ones, s1[c], false);
        s2[c] = __builtin_amdgcn_fdot2_f32_bf16(x2, x2, s2[c], false);
      }
    }
    __builtin_amdgcn_sched_barrier(0);
    mfma_bf16<ks == 0, false>(acc[3][c], Af[3], bfr);
    __builtin_amdgcn_sched_barrier(0);
    if constexpr (na) rdA(IC<ks + 1>{}, IC<2 * c + 1>{}, IC<1>{});
    if constexpr (c < 2) {
#pragma unroll
      for (int k = 2; k < 4; ++k) {
        bf16x2 x2 = as_bf16x2(bfr[k]);
        s1[c] = __builtin_amdgcn_fdot2_f32_bf16(x2, ones, s1[c], false);
        s2[c] = __builtin_amdgcn_fdot2_f32_bf16(x2, x2, s2[c], false);
      }
      if (ks == fb) __builtin_amdgcn_raw_buffer_store_b128(bfr, ws_rsrc(ws), hoff[c], 0, 16);
    }
    __builtin_amdgcn_sched_barrier(0);
  };
  bar();
  static_for<0, 4>([&](auto ic) { rdA(IC<0>{}, ic, IC<0>{}); rdA(IC<0>{}, ic, IC<1>{}); });
  rdB(IC<0>{}, IC<0>{}); rdB(IC<0>{}, IC<1>{}); rdB(IC<1>{}, IC<0>{}); rdB(IC<1>{}, IC<1>{});
  asm volatile("s_waitcnt lgkmcnt(4)" ::: "memory");
  rdB(IC<2>{}, IC<0>{}); rdB(IC<2>{}, IC<1>{}); rdB(IC<3>{}, IC<0>{}); rdB(IC<3>{}, IC<1>{});
  static_for<0, 56>(group);
  asm volatile("s_nop 7\n\ts_nop 7" ::: "memory");
  TR(ws, 9);
  bar();
  const int lane2 = fresh_tid() & 63, c16b = lane2 & 15;
#pragma unroll
  for (int i = 0; i < 2; ++i) {
    float sa = s1[i], sb = s2[i];
    sa += __shfl_xor(sa, 16); sa += __shfl_xor(sa, 32);
    sb += __shfl_xor(sb, 16); sb += __shfl_xor(sb, 32);
    const float mean = sa * (1.0f / D);
    const float var = fmaxf(sb * (1.0f / D) - mean * mean, 0.f);
    const float mu = rbf(mean);
    const float sd = rbf(__builtin_amdgcn_sqrtf(rbf(rbf(var) + 1e-5f)));
    const int tt = 4 * ti + ((2 * fi + i) & 3);
    if (lane2 < 16) st[tt * 16 + lane2] = f32x2{mu, __builtin_amdgcn_rcpf(sd)};
  }
  lds_bar();
  f32x4 wsv[4];
#pragma unroll
  for (int i = 0; i < 4; ++i) wsv[i] = *(const f32x4*)(smem + G1_ST + 1024 + (64 * fi + 4 * (lane2 >> 4) + 16 * i) * 4);
#pragma unroll
  for (int c = 0; c < 4; ++c) {
    const int tt = 4 * ti + ((2 * fi + c) & 3);
    const f32x2 ms = st[tt * 16 + c16b];
#pragma unroll
    for (int pp = 0; pp < 2; ++pp) {
      float v0[4], v1[4];
#pragma unroll
      for (int k = 0; k < 4; ++k) {
        v0[k] = fmaxf((acc[2 * pp][c][k] - ms[0] * wsv[2 * pp][k]) * ms[1], 0.f);
        v1[k] = fmaxf((acc[2 * pp + 1][c][k] - ms[0] * wsv[2 * pp + 1][k]) * ms[1], 0.f);
      }
      u32x4 o = {pk_bf16(v0[0], v0[1]), pk_bf16(v0[2], v0[3]), pk_bf16(v1[0], v1[1]), pk_bf16(v1[2], v1[3])};
      __builtin_amdgcn_raw_buffer_store_b128(o, ws_rsrc(ws), (u32)(OFF_H1F + (((size_t)(tb * 8 + tt) * 56 + fb * 4 + 2 * fi + pp) * 64 + lane2) * 16), 0, 16);
    }
  }
  asm volatile("s_waitcnt vmcnt(0)" ::: "memory");
  if (lane2 == 0) {
    const u32 old = __hip_atomic_fetch_add((LDS3 u32*)(smem + LDS_SIG + 48), 1u, __ATOMIC_RELAXED, __HIP_MEMORY_SCOPE_WORKGROUP);
    if (old == 3u) st_agent(ws + OFF_SYNC + SY_H1 + tb * 128 + fb * 8, tag);
  }
  TR(ws, 10);
}

static constexpr int G2_NR = 5;
static constexpr int G2_RS = 272;
constexpr int g2_waddr(int k) { return k < 5 ? k * 16384 : k < 8 ? 81920 + (k - 5) * 16384 : k == 8 ? 133120 : k < 11 ? (k - 9) * 16384 : 81920 + (k - 11) * 16384; }
constexpr int g2_haddr(int k) { return 32768 + (k % 3) * 16384; }
constexpr int g2_after(int k) {
  int seq[40] = {}; int n = 0;
  seq[n++] = 109; seq[n++] = 110; seq[n++] = 0; seq[n++] = 1; seq[n++] = 2;
  for (int j = 0; j < k; ++j) {
    if (j >= 1 && j + 2 < 14) seq[n++] = j + 2;
    if (j >= 6 && j <= 8) seq[n++] = 100 + j + 5;
  }
  int last = -1;
  for (int i = 0; i < n; ++i) if (seq[i] == k || seq[i] == 100 + k) last = i;
  return n - 1 - last;
}
struct CW { u32 vw[4]; const char* wb; u32 lds0; int w; };
DI CW c_wsetup(const Args& a, int m, char* smem) {
  const int lane = fresh_tid() & 63;
  const int w = __builtin_amdgcn_readfirstlane(threadIdx.x >> 6) & 3;
  const int fb = (m >> 3) % 14, db = fb % 7;
  CW c; c.w = w; c.lds0 = (u32)(size_t)(LDS3 char*)smem;
  const int rr = lane >> 3;
#pragma unroll
  for (int i = 0; i < 4; ++i) {
    const int row = 8 * i + rr;
    c.vw[i] = (u32)(row * D * 2) + ((((u32)(lane & 7)) ^ (u32)(((row >> 1) & 3) << 1)) << 4);
  }
  c.wb = (const char*)a.w2 + ((size_t)(448 * w) * D + db * 64) * 2;
  return c;
}
template <int K> DI void c_ldw(const CW& c) {
#pragma unroll
  for (int i = 0; i < 4; ++i) glds16<false>(c.lds0 + g2_waddr(K) + c.w * 4096 + i * 1024, c.vw[i], c.wb + (size_t)K * 32 * D * 2);
}
DI void c_prefetch_w(const Args& a, int m, char* smem) {
  const CW c = c_wsetup(a, m, smem);
  static_for<0, 9>([&](auto kc) { c_ldw<decltype(kc)::value>(c); });
}

DI void phase_c(const Args& a, int m, char* smem, u64 tag) {
  const int tid = fresh_tid(), lane = tid & 63;
  const int wv = __builtin_amdgcn_readfirstlane(tid >> 6);
  const int w = wv & 3;
  const int xcd = m & 7, bi = m >> 3;
  const int tb = 2 * xcd + bi / 14, fb = bi % 14;
  const int t64 = 2 * tb + fb / 7, db = fb % 7;
  const u32 lds0 = (u32)(size_t)(LDS3 char*)smem;
  char* ws = a.ws;
  const int c16 = lane & 15, g = lane >> 4, q = (lane & 15) >> 2, p = lane & 3;
  if (wv >= 4) {
    const CW c = c_wsetup(a, m, smem);
    u32 vo[4];
#pragma unroll
    for (int i = 0; i < 4; ++i) vo[i] = lane * 16 + i * 56 * 1024;
    const char* hb = ws + OFF_H1F + ((size_t)(t64 * 4) * 56 + 14 * w) * 1024;
    auto ldh = [&](auto kc) {
      constexpr int k = decltype(kc)::value;
#pragma unroll
      for (int i = 0; i < 4; ++i) glds16<true>(lds0 + g2_haddr(k) + w * 4096 + i * 1024, vo[i], hb + k * 1024);
    };
    wait_vm<0>();
    bar();
    bar();
    c_ldw<9>(c); c_ldw<10>(c);
    loaders_wait<1>(smem, 1, w, ws + OFF_SYNC + SY_H1 + tb * 128, 14, tag);
    TRW(ws, 11, tid == 256);
    ldh(IC<0>{}); ldh(IC<1>{}); ldh(IC<2>{});
    auto stepl = [&](auto kc) {
      constexpr int k = decltype(kc)::value;
      wait_vm<4 * g2_after(k)>();
      bar();
      if constexpr (k >= 1 && k + 2 < 14) ldh(IC<k + 2>{});
      if constexpr (k >= 6 && k <= 8) c_ldw<k + 5>(c);
    };
    static_for<0, 14>(stepl);
    bar();
    bar();
    return;
  }
  u32x4 res[2];
  f32x4 acc[4][4];
  u32 adW[4];
  {
    const int row = 4 * g + q;
    const u32 f = (u32)((row >> 1) & 3);
    const u32 a0 = lds0 + w * 4096 + (u32)(row * 128) + (u32)((p >> 1) * 16 + 8 * (p & 1));
#pragma unroll
    for (int dl = 0; dl < 4; ++dl) adW[dl] = a0 + ((((u32)dl) ^ f) << 5);
  }
  const u32 adH = lds0 + w * 4096 + lane * 16;
  u32x2 Wf[2][4][2];
  u32x4 Hf[2][4], Wr[G2_NR][4];
  auto rdW = [&](auto kc, auto ic, u32x2& dst) {
    constexpr int k = decltype(kc)::value, i = decltype(ic)::value;
    constexpr int ad = g2_waddr(k);
    trread<(ad & 65535) + (i & 1) * 2048>(dst, adW[i >> 1] + (u32)(ad & ~65535));
  };
  bar();
  static_for<0, G2_NR>([&](auto kc) {
    constexpr int k = decltype(kc)::value;
    u32x2 t[8];
    static_for<0, 8>([&](auto ic) { rdW(kc, ic, t[decltype(ic)::value]); });
    asm volatile("s_waitcnt lgkmcnt(0)" : "+v"(t[0]), "+v"(t[1]), "+v"(t[2]), "+v"(t[3]), "+v"(t[4]), "+v"(t[5]), "+v"(t[6]), "+v"(t[7]) :: "memory");
#pragma unroll
    for (int i = 0; i < 4; ++i) Wr[k][i] = cat(t[2 * i], t[2 * i + 1]);
  });
  bar();
  auto rd = [&](auto kc, auto ic) {
    constexpr int k = decltype(kc)::value, i = decltype(ic)::value;
    if constexpr (i < 4) ldsread128<(g2_haddr(k) & 65535) + i * 1024>(Hf[k & 1][i], adH + (u32)(g2_haddr(k) & ~65535));
    else if constexpr (k >= G2_NR) rdW(kc, IC<i - 4>{}, Wf[k & 1][(i - 4) >> 1][(i - 4) & 1]);
  };
  auto step = [&](auto kc) {
    constexpr int k = decltype(kc)::value;
    u32x4 Wq[4];
    if constexpr (k >= G2_NR) {
      asm volatile("s_waitcnt lgkmcnt(0)" : "+v"(Wf[k & 1][0][0]), "+v"(Wf[k & 1][0][1]), "+v"(Wf[k & 1][1][0]), "+v"(Wf[k & 1][1][1]), "+v"(Wf[k & 1][2][0]), "+v"(Wf[k & 1][2][1]),
                   "+v"(Wf[k & 1][3][0]), "+v"(Wf[k & 1][3][1]), "+v"(Hf[k & 1][0]), "+v"(Hf[k & 1][1]), "+v"(Hf[k & 1][2]), "+v"(Hf[k & 1][3]) :: "memory");
#pragma unroll
      for (int i = 0; i < 4; ++i) Wq[i] = cat(Wf[k & 1][i][0], Wf[k & 1][i][1]);
    } else {
      asm volatile("s_waitcnt lgkmcnt(0)" : "+v"(Hf[k & 1][0]), "+v"(Hf[k & 1][1]), "+v"(Hf[k & 1][2]), "+v"(Hf[k & 1][3]) :: "memory");
#pragma unroll
      for (int i = 0; i < 4; ++i) Wq[i] = Wr[k][i];
    }
    if constexpr (k + 1 < 14) bar();
    static_for<0, 16>([&](auto jc) {
      constexpr int j = decltype(jc)::value, dl = j >> 2, ta = j & 3;
      mfma_bf16<k == 0, j == 0>(acc[dl][ta], Wq[dl], Hf[k & 1][ta]);
      __builtin_amdgcn_sched_barrier(0);
      if constexpr (k + 1 < 14 && j < 12) rd(IC<k + 1>{}, IC<j>{});
      __builtin_amdgcn_sched_barrier(0);
    });
  };
  bar();
  static_for<0, 4>([&](auto ic) { rd(IC<0>{}, ic); });
  {
    const rsrc_t rs = ws_rsrc(ws);
#pragma unroll
    for (int i = 0; i < 2; ++i)
      res[i] = __builtin_amdgcn_raw_buffer_load_b128(rs, (u32)(OFF_HN + ((size_t)(t64 * 64 + 32 * i + (tid >> 3)) * D + db * 64 + (tid & 7) * 8) * 2), 0, 16);
  }
  static_for<0, 14>(step);
  asm volatile("s_nop 7\n\ts_nop 7" ::: "memory");
  TR(ws, 12);
  bar();
  const int tid2 = fresh_tid(), lane2 = tid2 & 63;
#pragma unroll
  for (int dl = 0; dl < 4; ++dl)
#pragma unroll
    for (int ta = 0; ta < 4; ++ta)
      *(f32x4*)(smem + (w * 64 + 16 * ta + (lane2 & 15)) * G2_RS + (16 * dl + 4 * (lane2 >> 4)) * 4) = acc[dl][ta];
  lds_bar();
#pragma unroll
  for (int i = 0; i < 2; ++i) {
    const int t = (tid2 >> 3) + 32 * i, chn = tid2 & 7;
    const u32x4 rr = res[i];
    f32x4 s0 = {0.f, 0.f, 0.f, 0.f}, s1 = s0;
#pragma unroll
    for (int ww = 0; ww < 4; ++ww) {
      s0 += *(const f32x4*)(smem + (ww * 64 + t) * G2_RS + chn * 32);
      s1 += *(const f32x4*)(smem + (ww * 64 + t) * G2_RS + chn * 32 + 16);
    }
    u32 p0 = pk_bf16(s0[0], s0[1]), p1 = pk_bf16(s0[2], s0[3]), p2 = pk_bf16(s1[0], s1[1]), p3 = pk_bf16(s1[2], s1[3]);
    u32x4 o;
    o[0] = pk_bf16(bflo(rr[0]) + bflo(p0), bfhi(rr[0]) + bfhi(p0));
    o[1] = pk_bf16(bflo(rr[1]) + bflo(p1), bfhi(rr[1]) + bfhi(p1));
    o[2] = pk_bf16(bflo(rr[2]) + bflo(p2), bfhi(rr[2]) + bfhi(p2));
    o[3] = pk_bf16(bflo(rr[3]) + bflo(p3), bfhi(rr[3]) + bfhi(p3));
    *(u32x4*)(a.out + (size_t)(t64 * 64 + t) * D + db * 64 + chn * 8) = o;
  }
}

__global__ __launch_bounds__(512, 2) void s4_block_kernel(Args a) {
  extern __shared__ __attribute__((aligned(16))) char smem[];
  const int b = blockIdx.x;
  unsigned long long pc0 = 0; u32 clen = 0;
  asm volatile("s_getpc_b64 %0\n.Lpc_anchor:\n\ts_mov_b32 %1, .Lcode_end_mark-.Lpc_anchor" : "=s"(pc0), "=s"(clen));
  TR(a.ws, 0);
  if (b < NAUX) { aux_block(a, b, smem, (const char*)pc0, clen); TR(a.ws, 15); TRDUMP(a.ws); return; }
  const int m = b - NAUX;
  const u64 tag = phase_a(a, m, smem);
  phase_b(a, m, smem, tag);
  __syncthreads();
  phase_c(a, m, smem, tag);
  asm volatile(".Lcode_end_mark:");
  TR(a.ws, 15);
  TRDUMP(a.ws);
}

extern "C" void kernel_launch(void* const* d_in, const int* in_sizes, int n_in,
                              void* d_out, int out_size, void* d_ws, size_t ws_size,
                              hipStream_t stream) {
  Args a;
  a.x = (const u16*)d_in[0]; a.Are = (const u16*)d_in[1]; a.Aim = (const u16*)d_in[2];
  a.Bre = (const u16*)d_in[3]; a.Bim = (const u16*)d_in[4]; a.Cre = (const u16*)d_in[5];
  a.Cim = (const u16*)d_in[6]; a.logdt = (const u16*)d_in[7]; a.Dskip = (const u16*)d_in[8];
  a.w1 = (const u16*)d_in[10]; a.w2 = (const u16*)d_in[11]; a.ws = (char*)d_ws; a.out = (u16*)d_out; a.pad = nullptr;
  (void)hipFuncSetAttribute((const void*)s4_block_kernel, hipFuncAttributeMaxDynamicSharedMemorySize, SMEM_BYTES);
  s4_block_kernel<<<NAUX + NMAIN, 512, SMEM_BYTES, stream>>>(a);
}
```

```cpp
#define PF_CODE 1
#define PF_W 1
#include <hip/hip_runtime.h>
#include <hip/hip_bf16.h>
#include <stdint.h>
#include <type_traits>

static constexpr int L = 2048, D = 448, NM = 256, F = 1792;
static constexpr int LP = 2176;
static constexpr size_t OFF_ST = 0;
static constexpr size_t OFF_XT = 16384;
static constexpr size_t OFF_HT = OFF_XT + (size_t)D * LP * 2;
static constexpr size_t OFF_W1S = OFF_HT + (size_t)D * LP * 2;
static constexpr size_t OFF_HN = OFF_W1S + 8192;
static constexpr size_t OFF_H1F = OFF_HN + (size_t)L * D * 2;
static constexpr size_t OFF_SYNC = OFF_H1F + (size_t)L * F * 2;
static constexpr size_t SY_EP = 0, SY_LN = 1024  , SY_W1S = 4096, SY_CV = 8192  , SY_H1 = 32768  ;
static constexpr size_t WS_USED = OFF_SYNC + 65536;

typedef unsigned short u16;
typedef unsigned int u32;
typedef unsigned long long u64;
typedef _Float16 f16;
typedef f16 f16x2 __attribute__((ext_vector_type(2)));
typedef f16 f16x8 __attribute__((ext_vector_type(8)));
typedef __bf16 bf16x2 __attribute__((ext_vector_type(2)));
typedef __bf16 bf16x8 __attribute__((ext_vector_type(8)));
typedef float f32x2 __attribute__((ext_vector_type(2)));
typedef float f32x4 __attribute__((ext_vector_type(4)));
typedef u32 u32x2 __attribute__((ext_vector_type(2)));
typedef u32 u32x4 __attribute__((ext_vector_type(4)));
typedef short s16x4 __attribute__((ext_vector_type(4)));
#define LDS3 __attribute__((address_space(3)))
typedef __attribute__((address_space(1))) u64 gu64;
typedef __amdgpu_buffer_rsrc_t rsrc_t;

#define DI __device__ __forceinline__
DI float bf2f(u16 v) { return __uint_as_float(((u32)v) << 16); }
DI float bflo(u32 v) { return __uint_as_float(v << 16); }
DI float bfhi(u32 v) { return __uint_as_float(v & 0xffff0000u); }
DI u32 pk_bf16(float a, float b) { f32x2 v = {a, b}; return __builtin_bit_cast(u32, __builtin_convertvector(v, bf16x2)); }
DI u32 pk_f16(float a, float b) { f32x2 v = {a, b}; return __builtin_bit_cast(u32, __builtin_convertvector(v, f16x2)); }
DI float rbf(float x) { return bflo(pk_bf16(x, 0.f)); }
DI f16x2 as_f16x2(u32 v) { return __builtin_bit_cast(f16x2, v); }
DI bf16x2 as_bf16x2(u32 v) { return __builtin_bit_cast(bf16x2, v); }
template <int CTRL, int RM> DI float dppf(float old, float v) {
  return __int_as_float(__builtin_amdgcn_update_dpp(__float_as_int(old), __float_as_int(v), CTRL, RM, 0xf, false));
}
DI float wave_sum(float v) {
  v += dppf<0x111, 0xf>(0.f, v); v += dppf<0x112, 0xf>(0.f, v); v += dppf<0x114, 0xf>(0.f, v); v += dppf<0x118, 0xf>(0.f, v);
  v += dppf<0x142, 0xa>(0.f, v); v += dppf<0x143, 0xc>(0.f, v);
  return __int_as_float(__builtin_amdgcn_readlane(__float_as_int(v), 63));
}
DI float wave_max(float v) {
  v = fmaxf(v, dppf<0x111, 0xf>(v, v)); v = fmaxf(v, dppf<0x112, 0xf>(v, v)); v = fmaxf(v, dppf<0x114, 0xf>(v, v)); v = fmaxf(v, dppf<0x118, 0xf>(v, v));
  v = fmaxf(v, dppf<0x142, 0xa>(v, v)); v = fmaxf(v, dppf<0x143, 0xc>(v, v));
  return __int_as_float(__builtin_amdgcn_readlane(__float_as_int(v), 63));
}
DI u64 ld_agent(const void* p) { return __hip_atomic_load((const gu64*)p, __ATOMIC_RELAXED, __HIP_MEMORY_SCOPE_AGENT); }
DI void st_agent(void* p, u64 v) { __hip_atomic_store((gu64*)p, v, __ATOMIC_RELAXED, __HIP_MEMORY_SCOPE_AGENT); }
static constexpr u64 TAG_INC = 0x9E3779B97F4A7C15ull;
DI rsrc_t ws_rsrc(char* ws) { return __builtin_amdgcn_make_buffer_rsrc(ws, 0, (int)WS_USED, 0x00020000); }
template <int B, int E, class Fn> DI void static_for(Fn&& f) {
  if constexpr (B < E) { f(std::integral_constant<int, B>{}); static_for<B + 1, E>(f); }
}
template <int N> using IC = std::integral_constant<int, N>;
DI s16x4 tr_read(const void* p) { return __builtin_amdgcn_ds_read_tr16_b64_v4i16((LDS3 s16x4*)p); }
template <int N> DI void wait_vm() { asm volatile("s_waitcnt vmcnt(%0)" :: "n"(N) : "memory"); }
DI void bar() { __builtin_amdgcn_s_barrier(); }
DI int fresh_tid() { int t = threadIdx.x; asm volatile("" : "+v"(t)); return t; }
DI void lds_bar() { asm volatile("s_waitcnt lgkmcnt(0)" ::: "memory"); __builtin_amdgcn_s_barrier(); }
template <bool SC1> DI void glds16(u32 lds_dst, u32 voff, const void* sbase) {
  u32 keep;
  if constexpr (SC1)
    asm volatile("s_mov_b32 %0, m0\n\ts_mov_b32 m0, %1\n\ts_nop 0\n\tglobal_load_lds_dwordx4 %2, %3 sc1\n\ts_mov_b32 m0, %0"
                 : "=&s"(keep) : "s"(lds_dst), "v"(voff), "s"(sbase) : "memory");
  else
    asm volatile("s_mov_b32 %0, m0\n\ts_mov_b32 m0, %1\n\ts_nop 0\n\tglobal_load_lds_dwordx4 %2, %3\n\ts_mov_b32 m0, %0"
                 : "=&s"(keep) : "s"(lds_dst), "v"(voff), "s"(sbase) : "memory");
}
template <int OFF> DI void trread(u32x2& dst, u32 addr) {
  asm volatile("ds_read_b64_tr_b16 %0, %1 offset:%2" : "=v"(dst) : "v"(addr), "n"(OFF) : "memory");
}
template <int OFF> DI void ldsread128(u32x4& dst, u32 addr) {
  asm volatile("ds_read_b128 %0, %1 offset:%2" : "=v"(dst) : "v"(addr), "n"(OFF) : "memory");
}
template <bool FIRST, bool NOP> DI void mfma_bf16(f32x4& acc, const u32x4& a, const u32x4& b) {
  if constexpr (NOP) {
    if constexpr (FIRST) asm volatile("s_nop 1\n\tv_mfma_f32_16x16x32_bf16 %0, %1, %2, 0" : "=&v"(acc) : "v"(a), "v"(b));
    else asm volatile("s_nop 1\n\tv_mfma_f32_16x16x32_bf16 %0, %1, %2, %0" : "+v"(acc) : "v"(a), "v"(b));
  } else {
    if constexpr (FIRST) asm volatile("v_mfma_f32_16x16x32_bf16 %0, %1, %2, 0" : "=&v"(acc) : "v"(a), "v"(b));
    else asm volatile("v_mfma_f32_16x16x32_bf16 %0, %1, %2, %0" : "+v"(acc) : "v"(a), "v"(b));
  }
}
DI u32x4 cat(const u32x2& a, const u32x2& b) { return u32x4{a[0], a[1], b[0], b[1]}; }
DI u32 swz(int row) { return (u32)(((row & 3) << 2) | ((row >> 2) & 3)); }
DI f32x2 cmul(f32x2 y, f32x2 e) {
  f32x2 t, r;
  asm("v_pk_mul_f32 %0, %1, %2 op_sel_hi:[1,0]" : "=v"(t) : "v"(y), "v"(e));
  asm("v_pk_fma_f32 %0, %1, %2, %3 op_sel:[1,1,0] op_sel_hi:[0,1,1] neg_lo:[0,1,0]" : "=v"(r) : "v"(y), "v"(e), "v"(t));
  return r;
}
DI f32x2 cmul_conj(f32x2 p, f32x2 q) {
  f32x2 t, r;
  asm("v_pk_mul_f32 %0, %1, %2 op_sel_hi:[1,0] neg_hi:[1,0]" : "=v"(t) : "v"(p), "v"(q));
  asm("v_pk_fma_f32 %0, %1, %2, %3 op_sel:[1,1,0] op_sel_hi:[0,1,1] neg_lo:[1,0,0] neg_hi:[1,0,0]" : "=v"(r) : "v"(p), "v"(q), "v"(t));
  return r;
}

struct Args {
  const u16 *x, *Are, *Aim, *Bre, *Bim, *Cre, *Cim, *logdt, *Dskip, *w1, *w2;
  char* ws;
  u16* out;
  const void* pad;
};

constexpr int rne_bf16_int(int l) {
  if (l < 256) return l;
  int sh = 0; while ((l >> sh) >= 256) ++sh;
  const int q = l >> sh, rem = l & ((1 << sh) - 1), half = 1 << (sh - 1);
  const int up = (rem > half) || (rem == half && (q & 1));
  return (q + up) << sh;
}
constexpr int kslot(int m) {
  const int l = 2047 - m;
  if (l < 0) return 1089;
  const int t = rne_bf16_int(l);
  if (t >= 2048) return 1088;
  const int seg = (t >= 256) + (t >= 512) + (t >= 1024);
  const int row = seg == 0 ? (t & 7) : seg == 1 ? 8 + ((t >> 1) & 3) : seg == 2 ? 12 + ((t >> 2) & 1) : 14 + ((t >> 9) & 1);
  const int b = (t >> 3) & (seg >= 2 ? 63 : 31);
  return b * 17 + row;
}
struct SlotTab { u16 v[2064]; };
constexpr SlotTab make_slot_tab() { SlotTab t{}; for (int m = 0; m < 2064; ++m) t.v[m] = (u16)(kslot(m) * 4); return t; }
__device__ __attribute__((aligned(16))) const SlotTab g_slot_tab = make_slot_tab();

static constexpr int NAUX = 32;
static constexpr int NMAIN = 224;
static constexpr int SMEM_BYTES = 160 * 1024;
static constexpr unsigned SPIN_CAP = 1u << 20;
static constexpr int LDS_SIG = SMEM_BYTES - 320;

template <int NPL> DI void wait_flags(const char* base, int n, u64 tag) {
  const int lane = threadIdx.x & 63;
  unsigned spins = 0;
  for (;;) {
    bool ok = true;
#pragma unroll
    for (int k = 0; k < NPL; ++k) { const int i = k * 64 + lane; if (i < n) ok &= (ld_agent(base + i * 8) == tag); }
    if (__all(ok)) break;
    __builtin_amdgcn_s_sleep(1);
    if (++spins > SPIN_CAP) break;
  }
}

template <int NPL> DI void loaders_wait(char* smem, int idx, int w, const char* base, int n, u64 tag) {
  volatile u32* sig = (volatile u32*)(smem + LDS_SIG) + idx;
  if (w == 0) {
    wait_flags<NPL>(base, n, tag);
    *sig = 1u;
  } else {
    unsigned spins = 0;
    while (*sig == 0u) { __builtin_amdgcn_s_sleep(1); if (++spins > (SPIN_CAP << 4)) break; }
  }
  asm volatile("" ::: "memory");
}

DI void l2_touch(const char* base, int kbytes, int part, int nparts, char* smem) {
  const int lane = threadIdx.x & 63;
  const int w = __builtin_amdgcn_readfirstlane(threadIdx.x >> 6);
  const u32 dst = (u32)(size_t)(LDS3 char*)smem + 65536 + w * 1024;
  const int per = (kbytes + nparts - 1) / nparts;
  const int k0 = per * part, k1 = (k0 + per < kbytes) ? k0 + per : kbytes;
  int cnt = 0;
  for (int k = k0 + w; k < k1; k += 8) {
    glds16<false>(dst, lane * 16, base + (size_t)k * 1024);
    if ((++cnt & 7) == 0) wait_vm<32>();
  }
  wait_vm<0>();
}
static constexpr int XT_RS = 144;
static constexpr int LDS_X8 = 124 * 1024;
struct Pro { u32x4 row, tile; };
DI Pro pro_load(const Args& a, int b) {
  const int lane = threadIdx.x & 63, wv = threadIdx.x >> 6;
  Pro p; p.row = u32x4{0, 0, 0, 0}; p.tile = p.row;
  if (lane < 56) {
    p.tile = *(const u32x4*)(a.x + (size_t)(64 * (b >> 3) + 8 * wv + lane / 7) * D + 56 * (b & 7) + 8 * (lane % 7));
    p.row = *(const u32x4*)(a.x + (size_t)(8 * b + wv) * D + lane * 8);
  }
  return p;
}
DI u64 pro_store(const Args& a, int b, const Pro& p, u64 ep, char* smem) {
  const int lane = threadIdx.x & 63;
  const int wv = __builtin_amdgcn_readfirstlane(threadIdx.x >> 6);
  if (lane < 56) *(u32x4*)(smem + LDS_X8 + (8 * wv + lane / 7) * XT_RS + (lane % 7) * 16) = p.tile;
  if (threadIdx.x == 0) { *(volatile u32*)(smem + LDS_SIG) = 0u; *(volatile u32*)(smem + LDS_SIG + 4) = 0u; *(volatile u64*)(smem + LDS_SIG + 8) = ep + TAG_INC; *(volatile u32*)(smem + LDS_SIG + 48) = 0u; }
  __syncthreads();
  const u64 tag = *(volatile u64*)(smem + LDS_SIG + 8);
  if (wv == 7) {
    const int g = lane >> 4, i16 = lane & 15, q = i16 >> 2, pp = lane & 3;
    const char* src = smem + LDS_X8 + (16 * g + 8 * (pp >> 1) + q) * XT_RS + (4 * (pp & 1)) * 2;
    s16x4 lo[7], hi[7];
#pragma unroll
    for (int k = 0; k < 7; ++k) { lo[k] = tr_read(src + k * 16); hi[k] = tr_read(src + k * 16 + 4 * XT_RS); }
#pragma unroll
    for (int k = 0; k < 7; ++k)
      __builtin_amdgcn_raw_buffer_store_b128(cat(__builtin_bit_cast(u32x2, lo[k]), __builtin_bit_cast(u32x2, hi[k])), ws_rsrc(a.ws),
          (u32)(OFF_XT + (size_t)(56 * (b & 7) + 8 * k + (i16 & 7)) * LP * 2 + (64 * (b >> 3) + 16 * g + 8 * (i16 >> 3)) * 2), 0, 16);
  }
  return tag;
}
DI void pro_stats(const Args& a, int b, const Pro& p, u64 tag) {
  const int lane = threadIdx.x & 63, wv = threadIdx.x >> 6;
  float s = 0.f, q2 = 0.f;
#pragma unroll
  for (int k = 0; k < 4; ++k) {
    const float lo = bflo(p.row[k]), hi = bfhi(p.row[k]);
    s += lo; s += hi; q2 = __builtin_fmaf(lo, lo, q2); q2 = __builtin_fmaf(hi, hi, q2);
  }
  const float mean = wave_sum(s) * (1.0f / D);
  const float var = fmaxf(wave_sum(q2) * (1.0f / D) - mean * mean, 0.f);
  const float sd = rbf(__builtin_amdgcn_sqrtf(rbf(rbf(var) + 1e-5f)));
  if (lane == 0) st_agent(a.ws + OFF_ST + (8 * b + wv) * 8, ((u64)(u32)tag << 32) | pk_bf16(mean, sd));
}
DI void pro_flag(const Args& a, int b, u64 tag) {
  if ((threadIdx.x >> 6) == 7) {
    asm volatile("s_waitcnt vmcnt(0)" ::: "memory");
    if ((threadIdx.x & 63) == 0) st_agent(a.ws + OFF_SYNC + SY_LN + (b & 7) * 256 + (b >> 3) * 8, tag);
  }
}
DI void aux_block(const Args& a, int blk, char* smem, const char* code0, u32 code_len) {
  const int tid = threadIdx.x, lane = tid & 63;
  const int w = __builtin_amdgcn_readfirstlane(tid >> 6);
  const rsrc_t rs = ws_rsrc(a.ws);
  {
    const u32 dst = (u32)(size_t)(LDS3 char*)smem + 98304 + w * 1024;
    const int nk = ((int)code_len > 0 && code_len < (1u << 20)) ? (int)(code_len >> 10) : 0;
    for (int k = (blk >> 3) * 8 + w; k < nk; k += 32) glds16<false>(dst, lane * 16, code0 + (size_t)k * 1024);
  }
  const Pro pr = pro_load(a, blk);
  const u64 ep = ld_agent(w == 0 ? (const char*)(a.ws + OFF_SYNC + SY_EP) : (const char*)(a.x + (size_t)(8 * blk + w) * D));
  const u64 tag = pro_store(a, blk, pr, ep, smem);
  pro_flag(a, blk, tag);
  pro_stats(a, blk, pr, tag);
  {
    float* red = (float*)smem;
    const int cp = tid % 28, rg = tid / 28;
    float s0 = 0.f, s1 = 0.f;
    if (rg < 16) {
      u32 x[28];
#pragma unroll
      for (int i = 0; i < 28; ++i) x[i] = *(const u32*)(a.w1 + (size_t)(rg + 16 * i) * F + 56 * blk + 2 * cp);
#pragma unroll
      for (int i = 0; i < 28; ++i) { s0 += bflo(x[i]); s1 += bfhi(x[i]); }
      red[rg * 56 + 2 * cp] = s0; red[rg * 56 + 2 * cp + 1] = s1;
    }
    __syncthreads();
    if (tid < 56) {
      float s = 0.f;
#pragma unroll
      for (int i = 0; i < 16; ++i) s += red[i * 56 + tid];
      __builtin_amdgcn_raw_buffer_store_b32(__float_as_uint(s), rs, (u32)(OFF_W1S + (56 * blk + tid) * 4), 0, 16);
    }
    asm volatile("s_waitcnt vmcnt(0)" ::: "memory");
    __syncthreads();
    if (tid == 0) st_agent(a.ws + OFF_SYNC + SY_W1S + blk * 8, tag);
  }
  l2_touch((const char*)a.w1, D * F * 2 / 1024, blk >> 3, 4, smem);
  l2_touch((const char*)a.w2, D * F * 2 / 1024, blk >> 3, 4, smem);
  if (blk == 0 && w == 0) {
    wait_flags<1>(a.ws + OFF_SYNC + SY_W1S, NAUX, tag);
    wait_flags<4>(a.ws + OFF_SYNC + SY_CV, NMAIN, tag);
    if (lane == 0) st_agent(a.ws + OFF_SYNC + SY_EP, tag);
  }
}

static constexpr int CPS = 4160;
static constexpr int CH_BYTES = 61440;
static constexpr int CH_TAB = 0;
static constexpr int CH_SU = 8 * CPS;
static constexpr int CH_F = CH_SU + 4608 + 256;
static constexpr int LDS_SX = 136 * 1024;
static constexpr int CH_RED = 59392;

DI u64 phase_a(const Args& a, int m, char* smem_all) {
  const int tid = threadIdx.x & 255, lane = threadIdx.x & 63;
  const int wv = __builtin_amdgcn_readfirstlane(threadIdx.x >> 6);
  const int w = wv & 3, hw = wv >> 2;
  const int d = 2 * m + hw;
  char* smem = smem_all + hw * CH_BYTES;
  const float LOG2E = 1.4426950408889634f, INV2PI = 0.15915494309189535f;
  const u32 lds0 = (u32)(size_t)(LDS3 char*)smem;
  const Pro pr = pro_load(a, m + NAUX);
  const u64 ep = ld_agent(wv == 0 ? (const char*)(a.ws + OFF_SYNC + SY_EP) : (const char*)(a.Are + d * NM + 4 * w));
  const int n = tid;
  const u16 r_are = a.Are[d * NM + n], r_aim = a.Aim[d * NM + n], r_bre = a.Bre[d * NM + n], r_bim = a.Bim[d * NM + n];
  const u16 r_cre = a.Cre[d * NM + n], r_cim = a.Cim[d * NM + n], r_ldt = a.logdt[d], r_dsk = a.Dskip[d];
  u32 slot2[8];
  {
    const u32x4 s0 = *(const u32x4*)(g_slot_tab.v + 8 * tid), s1 = *(const u32x4*)(g_slot_tab.v + 8 * tid + 8);
#pragma unroll
    for (int k = 0; k < 4; ++k) { slot2[k] = s0[k]; slot2[4 + k] = s1[k]; }
  }
  __builtin_amdgcn_sched_barrier(0);
  const u64 tag = pro_store(a, m + NAUX, pr, ep, smem_all);
  float* red = (float*)(smem + CH_RED);
  const float dt = rbf(expf(bf2f(r_ldt)));
  const float bre = dt * bf2f(r_bre), bim = dt * bf2f(r_bim);
  const float cre = bf2f(r_cre), cim = bf2f(r_cim);
  const float zr = (dt * bf2f(r_are)) * LOG2E, zi = (dt * bf2f(r_aim)) * INV2PI;
  const float cr = cre * bre - cim * bim, ci = cre * bim + cim * bre;
  const float mx = fmaxf(fabsf(cr), fabsf(ci));
  const float lc = mx > 1e-36f ? __builtin_amdgcn_logf(mx) + 0.5f : -1000.f;
  const float sY = fmaxf(0.f, zr * 504.f);
  const float gm = fmaxf(lc + fmaxf(0.f, zr * 1536.f) + sY, lc + zr * 2048.f);
  const float G = ceilf(wave_max(gm));
  auto cexp = [&](float t, float lg) -> f32x2 {
    const float mm = __builtin_amdgcn_exp2f(__builtin_fmaf(zr, t, lg)), f = __builtin_amdgcn_fractf(zi * t);
    return f32x2{mm * __builtin_amdgcn_cosf(f), mm * __builtin_amdgcn_sinf(f)};
  };
  {
    f32x2* tb = (f32x2*)(smem + CH_TAB + n * 224);
    const f32x2 e1 = cexp(1.f, 0.f), e2 = cmul(e1, e1), e3 = cmul(e2, e1), e4 = cexp(4.f, 0.f), e6 = cmul(e3, e3);
    const f32x2 e8 = cexp(8.f, 0.f), e16 = cmul(e8, e8), e24 = cmul(e16, e8);
    const f32x2 e32 = cexp(32.f, 0.f), e64 = cmul(e32, e32), e96 = cmul(e64, e32);
    const f32x2 e128 = cexp(128.f, 0.f), e256 = cexp(256.f, 0.f), e384 = cmul(e256, e128);
    const f32x2 e512 = cexp(512.f, 0.f), e1024 = cexp(1024.f, 0.f);
    const float sy2 = __builtin_amdgcn_exp2f(-sY);
    const float cs = __builtin_amdgcn_exp2f(sY - G + 14.f);
    const f32x2 c0 = {cr * cs, ci * cs};
    *(f32x4*)(tb + 0) = f32x4{1.f, 0.f, e8[0], e8[1]};      *(f32x4*)(tb + 2) = f32x4{e16[0], e16[1], e24[0], e24[1]};
    *(f32x4*)(tb + 4) = f32x4{sy2, 0.f, e32[0] * sy2, e32[1] * sy2}; *(f32x4*)(tb + 6) = f32x4{e64[0] * sy2, e64[1] * sy2, e96[0] * sy2, e96[1] * sy2};
    const f32x2 p1 = cmul(c0, e4), p2 = cmul(c0, e256), p3 = cmul(c0, e512);
    *(f32x4*)(tb + 8) = f32x4{c0[0], c0[1], p1[0], p1[1]};  *(f32x4*)(tb + 10) = f32x4{p2[0], p2[1], p3[0], p3[1]};
    *(f32x4*)(tb + 12) = f32x4{1.f, 0.f, e1[0], e1[1]};     *(f32x4*)(tb + 14) = f32x4{e2[0], e2[1], e3[0], e3[1]};
    *(f32x4*)(tb + 16) = f32x4{1.f, 0.f, e2[0], e2[1]};     *(f32x4*)(tb + 18) = f32x4{e4[0], e4[1], e6[0], e6[1]};
    *(f32x4*)(tb + 20) = f32x4{1.f, 0.f, e4[0], e4[1]};     *(f32x4*)(tb + 22) = f32x4{e512[0], e512[1], e1024[0], e1024[1]};
    *(f32x4*)(tb + 24) = f32x4{e128[0], e128[1], e256[0], e256[1]}; *(f32x4*)(tb + 26) = f32x4{e384[0], e384[1], 0.f, 0.f};
    const f32x2 e2048 = cexp(2048.f, 14.f - G);
    float t2048 = cr * e2048[0] - ci * e2048[1];
    t2048 = wave_sum(t2048);
    if (lane == 0) { red[w] = G; red[4 + w] = t2048; }
  }
  __builtin_amdgcn_fence(__ATOMIC_RELEASE, "wavefront");
  __builtin_amdgcn_wave_barrier();
  pro_flag(a, m + NAUX, tag);
  pro_stats(a, m + NAUX, pr, tag);
  const int c = lane & 15, g = lane >> 4;
  const rsrc_t rs = ws_rsrc(a.ws);
  u32x4 gr[4];
  const char* flg = a.ws + OFF_SYNC + SY_LN + ((2 * m) / 56) * 256 + (lane & 31) * 8;
  u64 fl = 0;
  bool have = (wv != 0);
  auto load_x = [&]() {
#pragma unroll
    for (int i = 0; i < 4; ++i) {
      glds16<true>(lds0 + LDS_SX + i * 1024, lane * 16 + i * 1024, a.ws + OFF_XT + (size_t)(2 * m) * LP * 2);
      glds16<true>(lds0 + LDS_SX + 4096 + i * 1024, lane * 16 + i * 1024, a.ws + OFF_XT + (size_t)(2 * m + 1) * LP * 2);
    }
  };
  auto poll = [&]() { if (wv == 0 && !have) fl = ld_agent(flg); };
  auto check = [&]() {
    if (wv == 0 && !have) {
      if (__all(fl == tag)) { have = true; load_x(); }
    }
  };
  auto load_st = [&]() {
#pragma unroll
    for (int k = 0; k < 4; ++k) gr[k] = __builtin_amdgcn_raw_buffer_load_b128(rs, (u32)(OFF_ST + tid * 64 + k * 16), 0, 16);
  };
  auto load_st_fast = [&]() {
#pragma unroll
    for (int k = 0; k < 4; ++k) gr[k] = __builtin_amdgcn_raw_buffer_load_b128(rs, (u32)(OFF_ST + tid * 64 + k * 16), 0, 0);
  };
  const int r = c;
  f32x4 acc[4];
#pragma unroll
  for (int k = 0; k < 4; ++k) acc[k] = f32x4{0.f, 0.f, 0.f, 0.f};
  {
    const char* tb = smem + CH_TAB + (64 * w + 4 * g) * 224;
    const char* pA = tb + (c & 3) * 8;
    const char* pB = tb + 32 + (c >> 2) * 8;
    const char* pP = tb + 64 + (r >> 2) * 8;
    const char* pQ = tb + 96 + ((r < 8 ? 0 : r < 12 ? 4 : 8) + (r & 3)) * 8;
    const char* pE = tb + 192;
    f32x2 tA[2][4], tB[2][4], tP[2][4], tQ[2][4], tE3[2][4];
    f32x4 tE[2][4];
    auto ldtab = [&](auto sc_) {
      constexpr int s = decltype(sc_)::value;
#pragma unroll
      for (int i = 0; i < 4; ++i) {
        const int o = (16 * s + i) * 224;
        tA[s & 1][i] = *(const f32x2*)(pA + o); tB[s & 1][i] = *(const f32x2*)(pB + o);
        tP[s & 1][i] = *(const f32x2*)(pP + o); tQ[s & 1][i] = *(const f32x2*)(pQ + o);
        tE[s & 1][i] = *(const f32x4*)(pE + o); tE3[s & 1][i] = *(const f32x2*)(pE + o + 16);
      }
    };
    auto kstep = [&](auto sc_) {
      constexpr int s = decltype(sc_)::value;
      if constexpr (s < 3) ldtab(IC<s + 1>{});
      __builtin_amdgcn_sched_barrier(0);
      u32x4 xf, y0, y1, y2, y3;
#pragma unroll
      for (int i = 0; i < 4; ++i) {
        const f32x2 yv = cmul(tA[s & 1][i], tB[s & 1][i]);
        const f32x2 xv = cmul_conj(tP[s & 1][i], tQ[s & 1][i]);
        y0[i] = pk_f16(yv[0], yv[1]);
        xf[i] = pk_f16(xv[0], xv[1]);
        const f32x4 E12 = tE[s & 1][i];
        const f32x2 v1 = cmul(yv, f32x2{E12[0], E12[1]}), v2 = cmul(yv, f32x2{E12[2], E12[3]}), v3 = cmul(yv, tE3[s & 1][i]);
        y1[i] = pk_f16(v1[0], v1[1]); y2[i] = pk_f16(v2[0], v2[1]); y3[i] = pk_f16(v3[0], v3[1]);
      }
      f16x8 xa = __builtin_bit_cast(f16x8, xf);
      __builtin_amdgcn_sched_barrier(0);
      acc[0] = __builtin_amdgcn_mfma_f32_16x16x32_f16(xa, __builtin_bit_cast(f16x8, y0), acc[0], 0, 0, 0);
      acc[1] = __builtin_amdgcn_mfma_f32_16x16x32_f16(xa, __builtin_bit_cast(f16x8, y1), acc[1], 0, 0, 0);
      acc[2] = __builtin_amdgcn_mfma_f32_16x16x32_f16(xa, __builtin_bit_cast(f16x8, y2), acc[2], 0, 0, 0);
      acc[3] = __builtin_amdgcn_mfma_f32_16x16x32_f16(xa, __builtin_bit_cast(f16x8, y3), acc[3], 0, 0, 0);
      __builtin_amdgcn_sched_barrier(0);
      if constexpr (s == 0) poll();
      if constexpr (s == 2) { check(); poll(); }
      __builtin_amdgcn_sched_barrier(0);
    };
    ldtab(IC<0>{});
    static_for<0, 4>(kstep);
  }
  check();
  poll();
  float* part = (float*)(smem + CH_TAB);
#pragma unroll
  for (int k = 0; k < 4; ++k) *(f32x4*)(part + w * 3584 + (16 * k + r) * 16 + 4 * g) = acc[k];
  __syncthreads();
  load_st_fast();
  float* Fv = (float*)(smem + CH_F);
  const float Gm = fmaxf(fmaxf(red[0], red[1]), fmaxf(red[2], red[3]));
  float sc[4];
#pragma unroll
  for (int k = 0; k < 4; ++k) sc[k] = __builtin_amdgcn_exp2f(red[k] - Gm - 8.f);
  {
    const f32x4* pp = (const f32x4*)part;
    f32x4 sv = pp[tid] * sc[0] + pp[896 + tid] * sc[1] + pp[1792 + tid] * sc[2] + pp[2688 + tid] * sc[3];
    float* fo = Fv + (tid >> 2) * 17 + 4 * (tid & 3);
    fo[0] = sv[0]; fo[1] = sv[1]; fo[2] = sv[2]; fo[3] = sv[3];
    if (tid == 0) { Fv[1088] = red[4] * sc[0] + red[5] * sc[1] + red[6] * sc[2] + red[7] * sc[3]; Fv[1089] = 0.f; }
  }
  const float kinv = __builtin_amdgcn_exp2f(Gm - 6.f);
  check();
  __syncthreads();
  {
    u32 e[8];
#pragma unroll
    for (int k = 0; k < 8; ++k)
      e[k] = pk_f16(*(const float*)((const char*)Fv + (slot2[k] & 0xffffu)), *(const float*)((const char*)Fv + (slot2[k] >> 16)));
    auto put = [&](int ch, const u32 (&ee)[8]) {
      u32 o[7];
#pragma unroll
      for (int i = 0; i < 7; ++i) o[i] = __builtin_amdgcn_alignbyte(ee[i + 1], ee[i], 2);
#pragma unroll
      for (int s = 0; s < 4; ++s) {
        *(u32x4*)(smem + (2 * s) * CPS + ch * 16) = u32x4{ee[s], ee[s + 1], ee[s + 2], ee[s + 3]};
        *(u32x4*)(smem + (2 * s + 1) * CPS + ch * 16) = u32x4{o[s], o[s + 1], o[s + 2], o[s + 3]};
      }
    };
    put(tid, e);
    if (tid == 255) { const u32 z[8] = {0, 0, 0, 0, 0, 0, 0, 0}; put(256, z); }
  }
  u16* su = (u16*)(smem + CH_SU);
  const u16* sx = (const u16*)(smem_all + LDS_SX + hw * 4096);
  if (wv == 0) {
    if (!have) {
      unsigned spins = 0;
      while (!__all(fl == tag)) { __builtin_amdgcn_s_sleep(1); fl = ld_agent(flg); if (++spins > SPIN_CAP) break; }
      load_x();
    }
    asm volatile("s_waitcnt vmcnt(0)" ::: "memory");
  }
  {
    const u32 tg = (u32)tag;
    unsigned spins = 0;
    for (;;) {
      bool ok = true;
#pragma unroll
      for (int k = 0; k < 4; ++k) ok &= (gr[k][1] == tg) && (gr[k][3] == tg);
      if (__all(ok)) break;
      __builtin_amdgcn_s_sleep(1);
      __builtin_amdgcn_fence(__ATOMIC_ACQUIRE, "agent");
      load_st();
      if (++spins > SPIN_CAP) break;
    }
  }
  __syncthreads();
  {
    const u32x4 xr = *(const u32x4*)(sx + tid * 8);
    u32x4 o;
#pragma unroll
    for (int k = 0; k < 4; ++k) {
      const u32 s0 = gr[k][0], s1 = gr[k][2];
      const u32 dpk = pk_bf16(bflo(xr[k]) - bflo(s0), bfhi(xr[k]) - bflo(s1));
      const u32 upk = pk_bf16(bflo(dpk) * __builtin_amdgcn_rcpf(bfhi(s0)), bfhi(dpk) * __builtin_amdgcn_rcpf(bfhi(s1)));
      o[k] = pk_f16(bflo(upk), bfhi(upk));
    }
    *(u32x4*)(su + 256 + tid * 8) = o;
    if (tid < 32) *(u32x4*)(su + tid * 8) = u32x4{0, 0, 0, 0};
  }
  __syncthreads();
  f32x4 cv[8];
#pragma unroll
  for (int q = 0; q < 8; ++q) cv[q] = f32x4{0.f, 0.f, 0.f, 0.f};
  const int sig = (7 - c) & 7;
  const char* abase = smem + sig * CPS + 16 * ((c < 8 ? 253 : 252) + g);
#pragma unroll
  for (int rr2 = 0; rr2 < 2; ++rr2) {
    const int rs2 = 2 * w + rr2;
    f16x8 B[8];
#pragma unroll
    for (int q = 0; q < 8; ++q) B[q] = *(const f16x8*)(su + 240 + 256 * q - 32 * rs2 + 16 * c + 8 * g);
    f16x8 A[8];
#pragma unroll
    for (int k = 0; k < 8; ++k) A[k] = *(const f16x8*)(abase - 64 * (rs2 + 8 * k));
#pragma unroll
    for (int k = 0; k < 8; ++k)
#pragma unroll
      for (int q = k; q < 8; ++q) cv[q] = __builtin_amdgcn_mfma_f32_16x16x32_f16(A[k], B[q - k], cv[q], 0, 0, 0);
  }
  __syncthreads();
  f32x4* rb = (f32x4*)smem;
#pragma unroll
  for (int q = 0; q < 8; ++q)
    if ((q >> 1) != w) rb[(w * 8 + q) * 64 + lane] = cv[q];
  __syncthreads();
  const float dsk = bf2f(r_dsk);
  u32x2 hh[2];
#pragma unroll
  for (int i = 0; i < 2; ++i) {
    f32x4 y = cv[0];
#pragma unroll
    for (int q = 0; q < 8; ++q) if (q == 2 * w + i) y = cv[q];
    const int q = 2 * w + i;
#pragma unroll
    for (int s = 1; s < 4; ++s) y += rb[(((w + s) & 3) * 8 + q) * 64 + lane];
    const int t = 256 * q + 16 * c + 4 * g;
    u32x2 uu = *(const u32x2*)(su + 256 + t);
    const u32x2 xx = *(const u32x2*)(sx + t);
    f16x2 ua = as_f16x2(uu[0]), ub = as_f16x2(uu[1]);
    float uf[4] = {(float)ua[0], (float)ua[1], (float)ub[0], (float)ub[1]};
    u32 y01 = pk_bf16(y[0] * kinv, y[1] * kinv), y23 = pk_bf16(y[2] * kinv, y[3] * kinv);
    u32 s01 = pk_bf16(uf[0] * dsk, uf[1] * dsk), s23 = pk_bf16(uf[2] * dsk, uf[3] * dsk);
    u32 r01 = pk_bf16(bflo(y01) + bflo(s01), bfhi(y01) + bfhi(s01));
    u32 r23 = pk_bf16(bflo(y23) + bflo(s23), bfhi(y23) + bfhi(s23));
    hh[i][0] = pk_bf16(bflo(xx[0]) + bflo(r01), bfhi(xx[0]) + bfhi(r01));
    hh[i][1] = pk_bf16(bflo(xx[1]) + bflo(r23), bfhi(xx[1]) + bfhi(r23));
  }
  {
    const auto r0 = __builtin_amdgcn_permlane16_swap(hh[0][0], hh[1][0], false, false);
    const auto r1 = __builtin_amdgcn_permlane16_swap(hh[0][1], hh[1][1], false, false);
    const int t = 256 * (2 * w + (g & 1)) + 16 * c + 8 * (g >> 1);
    __builtin_amdgcn_raw_buffer_store_b128(u32x4{r0[0], r1[0], r0[1], r1[1]}, rs, (u32)(OFF_HT + (size_t)d * LP * 2 + t * 2), 0, 16);
  }
  asm volatile("s_waitcnt vmcnt(0) lgkmcnt(0)" ::: "memory");
  __syncthreads();
  if (threadIdx.x < 8) st_agent(a.ws + OFF_SYNC + SY_CV + threadIdx.x * 2048 + m * 8, tag);
  return tag;
}

static constexpr int G1_R = 8;
static constexpr int G1_STG = 16384;
static constexpr int G1_ST = G1_R * G1_STG;

constexpr int g1_cnt(int n) {
  if (n < 0) return 0;
  const int ks = n >> 2, c = n & 3;
  return (n + 4 < 56 ? 2 : 0) + ((c < 2 && ks + 1 < 14) ? 4 : 0);
}
constexpr int g1_wait(int n) {
  const int c = n & 3;
  const int breq = (g1_cnt(n - 4) - 2) + g1_cnt(n - 3) + g1_cnt(n - 2) + g1_cnt(n - 1);
  const int areq = g1_cnt(n - 2) + g1_cnt(n - 1);
  return (c == 0 && areq < breq) ? areq : breq;
}

DI void c_prefetch_w(const Args& a, int m, char* smem);
DI void phase_b(const Args& a, int m, char* smem, u64 tag) {
  const int tid = fresh_tid(), lane = tid & 63;
  const int wv = __builtin_amdgcn_readfirstlane(tid >> 6);
  const int w = wv & 3;
  const int xcd = m & 7, bi = m >> 3;
  const int tb = 2 * xcd + bi / 14, fb = bi % 14;
  const int c16 = lane & 15, g = lane >> 4, q = (lane & 15) >> 2, p = lane & 3;
  const u32 lds0 = (u32)(size_t)(LDS3 char*)smem;
  f32x2* st = (f32x2*)(smem + G1_ST);
  char* ws = a.ws;

  if (wv >= 4) {
    u32 vh[2], vw[2];
#pragma unroll
    for (int k = 0; k < 2; ++k) {
      const int row = 8 * w + 4 * k + g;
      vh[k] = (u32)(row * LP * 2) + (u32)((c16 ^ swz(row)) * 16);
      vw[k] = (u32)(row * F * 2) + (u32)((c16 ^ swz(row)) * 16);
    }
    const char* hb = ws + OFF_HT + tb * 256;
    const char* wb = (const char*)a.w1 + fb * 256;
    auto ldw = [&](auto kc) {
      constexpr int ks = decltype(kc)::value;
      constexpr int sl = (ks % G1_R) * G1_STG + 8192;
      glds16<false>(lds0 + sl + (8 * w) * 256, vw[0], wb + (size_t)ks * 32 * F * 2);
      glds16<false>(lds0 + sl + (8 * w + 4) * 256, vw[1], wb + (size_t)ks * 32 * F * 2);
    };
    auto ldh = [&](auto kc) {
      constexpr int ks = decltype(kc)::value;
      constexpr int sl = (ks % G1_R) * G1_STG;
      glds16<true>(lds0 + sl + (8 * w) * 256, vh[0], hb + (size_t)ks * 32 * LP * 2);
      glds16<true>(lds0 + sl + (8 * w + 4) * 256, vh[1], hb + (size_t)ks * 32 * LP * 2);
    };
    static_for<0, G1_R>(ldw);
    if (w == 0) wait_flags<1>(ws + OFF_SYNC + SY_W1S, NAUX, tag);
    loaders_wait<4>(smem, 0, w, ws + OFF_SYNC + SY_CV + xcd * 2048, NMAIN, tag);
    if (w == 0) glds16<true>(lds0 + G1_ST + 1024, lane * 16, ws + OFF_W1S + fb * 512);
    static_for<0, G1_R>(ldh);
    auto stepl = [&](auto kc) {
      constexpr int ks = decltype(kc)::value;
      constexpr int refills = ks > 3 ? (ks - 3 < 14 - G1_R ? ks - 3 : 14 - G1_R) : 0;
      constexpr int need_after = ks < G1_R ? (2 * (G1_R - 1 - ks) + 4 * refills) : (4 * (G1_R + refills - 1 - ks));
      wait_vm<need_after>();
      bar();
      if constexpr (ks >= 3 && ks - 3 + G1_R < 14) { ldh(IC<ks - 3 + G1_R>{}); ldw(IC<ks - 3 + G1_R>{}); }
    };
    static_for<0, 14>(stepl);
    bar();
    bar();
    __builtin_amdgcn_s_sleep(12);
    c_prefetch_w(a, m, smem);
    return;
  }
  const int fi = w >> 1, ti = w & 1;
  f32x4 acc[4][4];
  float s1[2] = {0.f, 0.f}, s2[2] = {0.f, 0.f};
  const bf16x2 ones = __builtin_bit_cast(bf16x2, 0x3f803f80u);
  u32 adB[2][4], adA[2][4];
#pragma unroll
  for (int t = 0; t < 2; ++t) {
    const int row = 8 * g + 4 * t + q;
    const u32 a0 = lds0 + (u32)(row * 256) + (((u32)(p >> 1) ^ swz(row)) << 4) + 8 * (p & 1);
#pragma unroll
    for (int c = 0; c < 4; ++c) {
      adB[t][c] = a0 ^ (u32)(32 * (4 * ti + ((2 * fi + c) & 3)));
      adA[t][c] = (a0 ^ (u32)(32 * (4 * fi + c))) + 8192;
    }
  }
  u32x2 B[8][2], A[2][4][2];
  u32x4 Af[4];
  u32 hoff[2];
#pragma unroll
  for (int c = 0; c < 2; ++c)
    hoff[c] = (u32)(OFF_HN + ((size_t)(tb * 128 + 16 * (4 * ti + ((2 * fi + c) & 3)) + c16) * D + 32 * fb + 8 * g) * 2);
  auto rdB = [&](auto nc, auto tc) {
    constexpr int n = decltype(nc)::value, t = decltype(tc)::value, sl = (n >> 2) % G1_R;
    trread<(sl & 3) * G1_STG>(B[n & 7][t], adB[t][n & 3]);
  };
  auto rdA = [&](auto kc, auto ic, auto tc) {
    constexpr int ks = decltype(kc)::value, i = decltype(ic)::value, t = decltype(tc)::value, sl = ks % G1_R;
    trread<(sl & 3) * G1_STG>(A[ks & 1][i][t], adA[t][i]);
  };
  auto group = [&](auto nc) {
    constexpr int n = decltype(nc)::value;
    constexpr int ks = n >> 2, c = n & 3, sl = n & 7;
    constexpr bool nb = n + 4 < 56, na = (c < 2) && (ks + 1 < 14);
    constexpr int wt = n < 4 ? (n == 0 ? 6 : n == 1 ? 10 : n == 2 ? 14 : 14) : g1_wait(n);
    if constexpr (c == 0) {
      asm volatile("s_waitcnt lgkmcnt(%10)" : "+v"(B[sl][0]), "+v"(B[sl][1]), "+v"(A[ks & 1][0][0]), "+v"(A[ks & 1][0][1]), "+v"(A[ks & 1][1][0]), "+v"(A[ks & 1][1][1]),
                   "+v"(A[ks & 1][2][0]), "+v"(A[ks & 1][2][1]), "+v"(A[ks & 1][3][0]), "+v"(A[ks & 1][3][1]) : "n"(wt) : "memory");
#pragma unroll
      for (int i = 0; i < 4; ++i) Af[i] = cat(A[ks & 1][i][0], A[ks & 1][i][1]);
    } else {
      asm volatile("s_waitcnt lgkmcnt(%2)" : "+v"(B[sl][0]), "+v"(B[sl][1]) : "n"(wt) : "memory");
    }
    const u32x4 bfr = cat(B[sl][0], B[sl][1]);
    mfma_bf16<ks == 0, true>(acc[0][c], Af[0], bfr);
    __builtin_amdgcn_sched_barrier(0);
    if constexpr (c == 0 && ks + 1 < 14) bar();
    if constexpr (c == 0 && ((ks + 1) & 3) == 0) {
      const u32 dl = (((ks + 1) >> 2) & 1) ? 65536u : (u32)-65536;
#pragma unroll
      for (int t = 0; t < 2; ++t)
#pragma unroll
        for (int cc = 0; cc < 4; ++cc) { adB[t][cc] += dl; adA[t][cc] += dl; }
    }
    if constexpr (nb) rdB(IC<n + 4>{}, IC<0>{});
    __builtin_amdgcn_sched_barrier(0);
    mfma_bf16<ks == 0, false>(acc[1][c], Af[1], bfr);
    __builtin_amdgcn_sched_barrier(0);
    if constexpr (nb) rdB(IC<n + 4>{}, IC<1>{});
    if constexpr (na) rdA(IC<ks + 1>{}, IC<2 * c>{}, IC<0>{});
    __builtin_amdgcn_sched_barrier(0);
    mfma_bf16<ks == 0, false>(acc[2][c], Af[2], bfr);
    __builtin_amdgcn_sched_barrier(0);
    if constexpr (na) { rdA(IC<ks + 1>{}, IC<2 * c>{}, IC<1>{}); rdA(IC<ks + 1>{}, IC<2 * c + 1>{}, IC<0>{}); }
    if constexpr (c < 2) {
#pragma unroll
      for (int k = 0; k < 2; ++k) {
        bf16x2 x2 = as_bf16x2(bfr[k]);
        s1[c] = __builtin_amdgcn_fdot2_f32_bf16(x2, ones, s1[c], false);
        s2[c] = __builtin_amdgcn_fdot2_f32_bf16(x2, x2, s2[c], false);
      }
    }
    __builtin_amdgcn_sched_barrier(0);
    mfma_bf16<ks == 0, false>(acc[3][c], Af[3], bfr);
    __builtin_amdgcn_sched_barrier(0);
    if constexpr (na) rdA(IC<ks + 1>{}, IC<2 * c + 1>{}, IC<1>{});
    if constexpr (c < 2) {
#pragma unroll
      for (int k = 2; k < 4; ++k) {
        bf16x2 x2 = as_bf16x2(bfr[k]);
        s1[c] = __builtin_amdgcn_fdot2_f32_bf16(x2, ones, s1[c], false);
        s2[c] = __builtin_amdgcn_fdot2_f32_bf16(x2, x2, s2[c], false);
      }
      if (ks == fb) __builtin_amdgcn_raw_buffer_store_b128(bfr, ws_rsrc(ws), hoff[c], 0, 16);
    }
    __builtin_amdgcn_sched_barrier(0);
  };
  bar();
  static_for<0, 4>([&](auto ic) { rdA(IC<0>{}, ic, IC<0>{}); rdA(IC<0>{}, ic, IC<1>{}); });
  rdB(IC<0>{}, IC<0>{}); rdB(IC<0>{}, IC<1>{}); rdB(IC<1>{}, IC<0>{}); rdB(IC<1>{}, IC<1>{});
  asm volatile("s_waitcnt lgkmcnt(4)" ::: "memory");
  rdB(IC<2>{}, IC<0>{}); rdB(IC<2>{}, IC<1>{}); rdB(IC<3>{}, IC<0>{}); rdB(IC<3>{}, IC<1>{});
  static_for<0, 56>(group);
  asm volatile("s_nop 7\n\ts_nop 7" ::: "memory");
  bar();
  const int lane2 = fresh_tid() & 63, c16b = lane2 & 15;
#pragma unroll
  for (int i = 0; i < 2; ++i) {
    float sa = s1[i], sb = s2[i];
    sa += __shfl_xor(sa, 16); sa += __shfl_xor(sa, 32);
    sb += __shfl_xor(sb, 16); sb += __shfl_xor(sb, 32);
    const float mean = sa * (1.0f / D);
    const float var = fmaxf(sb * (1.0f / D) - mean * mean, 0.f);
    const float mu = rbf(mean);
    const float sd = rbf(__builtin_amdgcn_sqrtf(rbf(rbf(var) + 1e-5f)));
    const int tt = 4 * ti + ((2 * fi + i) & 3);
    if (lane2 < 16) st[tt * 16 + lane2] = f32x2{mu, __builtin_amdgcn_rcpf(sd)};
  }
  lds_bar();
  f32x4 wsv[4];
#pragma unroll
  for (int i = 0; i < 4; ++i) wsv[i] = *(const f32x4*)(smem + G1_ST + 1024 + (64 * fi + 4 * (lane2 >> 4) + 16 * i) * 4);
#pragma unroll
  for (int c = 0; c < 4; ++c) {
    const int tt = 4 * ti + ((2 * fi + c) & 3);
    const f32x2 ms = st[tt * 16 + c16b];
#pragma unroll
    for (int pp = 0; pp < 2; ++pp) {
      float v0[4], v1[4];
#pragma unroll
      for (int k = 0; k < 4; ++k) {
        v0[k] = fmaxf((acc[2 * pp][c][k] - ms[0] * wsv[2 * pp][k]) * ms[1], 0.f);
        v1[k] = fmaxf((acc[2 * pp + 1][c][k] - ms[0] * wsv[2 * pp + 1][k]) * ms[1], 0.f);
      }
      u32x4 o = {pk_bf16(v0[0], v0[1]), pk_bf16(v0[2], v0[3]), pk_bf16(v1[0], v1[1]), pk_bf16(v1[2], v1[3])};
      __builtin_amdgcn_raw_buffer_store_b128(o, ws_rsrc(ws), (u32)(OFF_H1F + (((size_t)(tb * 8 + tt) * 56 + fb * 4 + 2 * fi + pp) * 64 + lane2) * 16), 0, 16);
    }
  }
  asm volatile("s_waitcnt vmcnt(0)" ::: "memory");
  if (lane2 == 0) {
    const u32 old = __hip_atomic_fetch_add((LDS3 u32*)(smem + LDS_SIG + 48), 1u, __ATOMIC_RELAXED, __HIP_MEMORY_SCOPE_WORKGROUP);
    if (old == 3u) st_agent(ws + OFF_SYNC + SY_H1 + tb * 128 + fb * 8, tag);
  }
}

static constexpr int G2_NR = 5;
static constexpr int G2_RS = 272;
constexpr int g2_waddr(int k) { return k < 5 ? k * 16384 : k < 8 ? 81920 + (k - 5) * 16384 : k == 8 ? 133120 : k < 11 ? (k - 9) * 16384 : 81920 + (k - 11) * 16384; }
constexpr int g2_haddr(int k) { return 32768 + (k % 3) * 16384; }
constexpr int g2_after(int k) {
  int seq[40] = {}; int n = 0;
  seq[n++] = 109; seq[n++] = 110; seq[n++] = 0; seq[n++] = 1; seq[n++] = 2;
  for (int j = 0; j < k; ++j) {
    if (j >= 1 && j + 2 < 14) seq[n++] = j + 2;
    if (j >= 6 && j <= 8) seq[n++] = 100 + j + 5;
  }
  int last = -1;
  for (int i = 0; i < n; ++i) if (seq[i] == k || seq[i] == 100 + k) last = i;
  return n - 1 - last;
}
struct CW { u32 vw[4]; const char* wb; u32 lds0; int w; };
DI CW c_wsetup(const Args& a, int m, char* smem) {
  const int lane = fresh_tid() & 63;
  const int w = __builtin_amdgcn_readfirstlane(threadIdx.x >> 6) & 3;
  const int fb = (m >> 3) % 14, db = fb % 7;
  CW c; c.w = w; c.lds0 = (u32)(size_t)(LDS3 char*)smem;
  const int rr = lane >> 3;
#pragma unroll
  for (int i = 0; i < 4; ++i) {
    const int row = 8 * i + rr;
    c.vw[i] = (u32)(row * D * 2) + ((((u32)(lane & 7)) ^ (u32)(((row >> 1) & 3) << 1)) << 4);
  }
  c.wb = (const char*)a.w2 + ((size_t)(448 * w) * D + db * 64) * 2;
  return c;
}
template <int K> DI void c_ldw(const CW& c) {
#pragma unroll
  for (int i = 0; i < 4; ++i) glds16<false>(c.lds0 + g2_waddr(K) + c.w * 4096 + i * 1024, c.vw[i], c.wb + (size_t)K * 32 * D * 2);
}
DI void c_prefetch_w(const Args& a, int m, char* smem) {
  const CW c = c_wsetup(a, m, smem);
  static_for<0, 9>([&](auto kc) { c_ldw<decltype(kc)::value>(c); });
}

DI void phase_c(const Args& a, int m, char* smem, u64 tag) {
  const int tid = fresh_tid(), lane = tid & 63;
  const int wv = __builtin_amdgcn_readfirstlane(tid >> 6);
  const int w = wv & 3;
  const int xcd = m & 7, bi = m >> 3;
  const int tb = 2 * xcd + bi / 14, fb = bi % 14;
  const int t64 = 2 * tb + fb / 7, db = fb % 7;
  const u32 lds0 = (u32)(size_t)(LDS3 char*)smem;
  char* ws = a.ws;
  const int c16 = lane & 15, g = lane >> 4, q = (lane & 15) >> 2, p = lane & 3;
  if (wv >= 4) {
    const CW c = c_wsetup(a, m, smem);
    u32 vo[4];
#pragma unroll
    for (int i = 0; i < 4; ++i) vo[i] = lane * 16 + i * 56 * 1024;
    const char* hb = ws + OFF_H1F + ((size_t)(t64 * 4) * 56 + 14 * w) * 1024;
    auto ldh = [&](auto kc) {
      constexpr int k = decltype(kc)::value;
#pragma unroll
      for (int i = 0; i < 4; ++i) glds16<true>(lds0 + g2_haddr(k) + w * 4096 + i * 1024, vo[i], hb + k * 1024);
    };
    wait_vm<0>();
    bar();
    bar();
    c_ldw<9>(c); c_ldw<10>(c);
    loaders_wait<1>(smem, 1, w, ws + OFF_SYNC + SY_H1 + tb * 128, 14, tag);
    ldh(IC<0>{}); ldh(IC<1>{}); ldh(IC<2>{});
    auto stepl = [&](auto kc) {
      constexpr int k = decltype(kc)::value;
      wait_vm<4 * g2_after(k)>();
      bar();
      if constexpr (k >= 1 && k + 2 < 14) ldh(IC<k + 2>{});
      if constexpr (k >= 6 && k <= 8) c_ldw<k + 5>(c);
    };
    static_for<0, 14>(stepl);
    bar();
    bar();
    return;
  }
  u32x4 res[2];
  f32x4 acc[4][4];
  u32 adW[4];
  {
    const int row = 4 * g + q;
    const u32 f = (u32)((row >> 1) & 3);
    const u32 a0 = lds0 + w * 4096 + (u32)(row * 128) + (u32)((p >> 1) * 16 + 8 * (p & 1));
#pragma unroll
    for (int dl = 0; dl < 4; ++dl) adW[dl] = a0 + ((((u32)dl) ^ f) << 5);
  }
  const u32 adH = lds0 + w * 4096 + lane * 16;
  u32x2 Wf[2][4][2];
  u32x4 Hf[2][4], Wr[G2_NR][4];
  auto rdW = [&](auto kc, auto ic, u32x2& dst) {
    constexpr int k = decltype(kc)::value, i = decltype(ic)::value;
    constexpr int ad = g2_waddr(k);
    trread<(ad & 65535) + (i & 1) * 2048>(dst, adW[i >> 1] + (u32)(ad & ~65535));
  };
  bar();
  static_for<0, G2_NR>([&](auto kc) {
    constexpr int k = decltype(kc)::value;
    u32x2 t[8];
    static_for<0, 8>([&](auto ic) { rdW(kc, ic, t[decltype(ic)::value]); });
    asm volatile("s_waitcnt lgkmcnt(0)" : "+v"(t[0]), "+v"(t[1]), "+v"(t[2]), "+v"(t[3]), "+v"(t[4]), "+v"(t[5]), "+v"(t[6]), "+v"(t[7]) :: "memory");
#pragma unroll
    for (int i = 0; i < 4; ++i) Wr[k][i] = cat(t[2 * i], t[2 * i + 1]);
  });
  bar();
  auto rd = [&](auto kc, auto ic) {
    constexpr int k = decltype(kc)::value, i = decltype(ic)::value;
    if constexpr (i < 4) ldsread128<(g2_haddr(k) & 65535) + i * 1024>(Hf[k & 1][i], adH + (u32)(g2_haddr(k) & ~65535));
    else if constexpr (k >= G2_NR) rdW(kc, IC<i - 4>{}, Wf[k & 1][(i - 4) >> 1][(i - 4) & 1]);
  };
  auto step = [&](auto kc) {
    constexpr int k = decltype(kc)::value;
    u32x4 Wq[4];
    if constexpr (k >= G2_NR) {
      asm volatile("s_waitcnt lgkmcnt(0)" : "+v"(Wf[k & 1][0][0]), "+v"(Wf[k & 1][0][1]), "+v"(Wf[k & 1][1][0]), "+v"(Wf[k & 1][1][1]), "+v"(Wf[k & 1][2][0]), "+v"(Wf[k & 1][2][1]),
                   "+v"(Wf[k & 1][3][0]), "+v"(Wf[k & 1][3][1]), "+v"(Hf[k & 1][0]), "+v"(Hf[k & 1][1]), "+v"(Hf[k & 1][2]), "+v"(Hf[k & 1][3]) :: "memory");
#pragma unroll
      for (int i = 0; i < 4; ++i) Wq[i] = cat(Wf[k & 1][i][0], Wf[k & 1][i][1]);
    } else {
      asm volatile("s_waitcnt lgkmcnt(0)" : "+v"(Hf[k & 1][0]), "+v"(Hf[k & 1][1]), "+v"(Hf[k & 1][2]), "+v"(Hf[k & 1][3]) :: "memory");
#pragma unroll
      for (int i = 0; i < 4; ++i) Wq[i] = Wr[k][i];
    }
    if constexpr (k + 1 < 14) bar();
    static_for<0, 16>([&](auto jc) {
      constexpr int j = decltype(jc)::value, dl = j >> 2, ta = j & 3;
      mfma_bf16<k == 0, j == 0>(acc[dl][ta], Wq[dl], Hf[k & 1][ta]);
      __builtin_amdgcn_sched_barrier(0);
      if constexpr (k + 1 < 14 && j < 12) rd(IC<k + 1>{}, IC<j>{});
      __builtin_amdgcn_sched_barrier(0);
    });
  };
  bar();
  static_for<0, 4>([&](auto ic) { rd(IC<0>{}, ic); });
  {
    const rsrc_t rs = ws_rsrc(ws);
#pragma unroll
    for (int i = 0; i < 2; ++i)
      res[i] = __builtin_amdgcn_raw_buffer_load_b128(rs, (u32)(OFF_HN + ((size_t)(t64 * 64 + 32 * i + (tid >> 3)) * D + db * 64 + (tid & 7) * 8) * 2), 0, 16);
  }
  static_for<0, 14>(step);
  asm volatile("s_nop 7\n\ts_nop 7" ::: "memory");
  bar();
  const int tid2 = fresh_tid(), lane2 = tid2 & 63;
#pragma unroll
  for (int dl = 0; dl < 4; ++dl)
#pragma unroll
    for (int ta = 0; ta < 4; ++ta)
      *(f32x4*)(smem + (w * 64 + 16 * ta + (lane2 & 15)) * G2_RS + (16 * dl + 4 * (lane2 >> 4)) * 4) = acc[dl][ta];
  lds_bar();
#pragma unroll
  for (int i = 0; i < 2; ++i) {
    const int t = (tid2 >> 3) + 32 * i, chn = tid2 & 7;
    const u32x4 rr = res[i];
    f32x4 s0 = {0.f, 0.f, 0.f, 0.f}, s1 = s0;
#pragma unroll
    for (int ww = 0; ww < 4; ++ww) {
      s0 += *(const f32x4*)(smem + (ww * 64 + t) * G2_RS + chn * 32);
      s1 += *(const f32x4*)(smem + (ww * 64 + t) * G2_RS + chn * 32 + 16);
    }
    u32 p0 = pk_bf16(s0[0], s0[1]), p1 = pk_bf16(s0[2], s0[3]), p2 = pk_bf16(s1[0], s1[1]), p3 = pk_bf16(s1[2], s1[3]);
    u32x4 o;
    o[0] = pk_bf16(bflo(rr[0]) + bflo(p0), bfhi(rr[0]) + bfhi(p0));
    o[1] = pk_bf16(bflo(rr[1]) + bflo(p1), bfhi(rr[1]) + bfhi(p1));
    o[2] = pk_bf16(bflo(rr[2]) + bflo(p2), bfhi(rr[2]) + bfhi(p2));
    o[3] = pk_bf16(bflo(rr[3]) + bflo(p3), bfhi(rr[3]) + bfhi(p3));
    *(u32x4*)(a.out + (size_t)(t64 * 64 + t) * D + db * 64 + chn * 8) = o;
  }
}

__global__ __launch_bounds__(512, 2) void s4_block_kernel(Args a) {
  extern __shared__ __attribute__((aligned(16))) char smem[];
  const int b = blockIdx.x;
  unsigned long long pc0 = 0; u32 clen = 0;
  asm volatile("s_getpc_b64 %0\n.Lpc_anchor:\n\ts_mov_b32 %1, .Lcode_end_mark-.Lpc_anchor" : "=s"(pc0), "=s"(clen));
  if (b < NAUX) { aux_block(a, b, smem, (const char*)pc0, clen); return; }
  const int m = b - NAUX;
  const u64 tag = phase_a(a, m, smem);
  phase_b(a, m, smem, tag);
  __syncthreads();
  phase_c(a, m, smem, tag);
  asm volatile(".Lcode_end_mark:");
}

extern "C" void kernel_launch(void* const* d_in, const int* in_sizes, int n_in,
                              void* d_out, int out_size, void* d_ws, size_t ws_size,
                              hipStream_t stream) {
  Args a;
  a.x = (const u16*)d_in[0]; a.Are = (const u16*)d_in[1]; a.Aim = (const u16*)d_in[2];
  a.Bre = (const u16*)d_in[3]; a.Bim = (const u16*)d_in[4]; a.Cre = (const u16*)d_in[5];
  a.Cim = (const u16*)d_in[6]; a.logdt = (const u16*)d_in[7]; a.Dskip = (const u16*)d_in[8];
  a.w1 = (const u16*)d_in[10]; a.w2 = (const u16*)d_in[11]; a.ws = (char*)d_ws; a.out = (u16*)d_out; a.pad = nullptr;
  (void)hipFuncSetAttribute((const void*)s4_block_kernel, hipFuncAttributeMaxDynamicSharedMemorySize, SMEM_BYTES);
  s4_block_kernel<<<NAUX + NMAIN, 512, SMEM_BYTES, stream>>>(a);
}
```
